```python
import torch

def _softmax(x, axis=-1):
    return torch.softmax(x, dim=axis)
def _gelu(x):
    return torch.nn.functional.gelu(x, approximate="tanh")
def _silu(x):
    return torch.nn.functional.silu(x)
def _sigmoid(x):
    return torch.sigmoid(x)
def _layernorm(x, axis=-1, eps=1e-5):
    m = x.mean(dim=axis, keepdim=True)
    v = x.var(dim=axis, keepdim=True, unbiased=False)
    return (x - m) / torch.sqrt(v + eps)
def _scatter_add(tgt, idx, src, axis=0):
    return tgt.clone().index_add_(axis, idx, src)
def _segment_sum(src, seg, n):
    out = torch.zeros((n,) + src.shape[1:], dtype=src.dtype)
    return out.index_add_(0, seg, src)
def _pad(x, pw):
    flat = tuple(v for pair in reversed(pw) for v in pair)
    return torch.nn.functional.pad(x, flat)

kernel_name = 'recurrent_scan__linear_kernel_hedgehog__dense_activation_silu__prompt'

def setup_inputs():

    _w = (lambda a: (lambda t: t.to(torch.bfloat16) if t.is_floating_point() else t)(torch.as_tensor(a)))
    def _A_setup():
        B, S, D, H = 1, 2048, 576, 32
        Dh = D // H
        return {
            "x": _w((lambda *s: torch.randn(*s, dtype=torch.bfloat16) * 0.05)(B, S, D)),
            "wqkv": _w((lambda *s: torch.randn(*s, dtype=torch.bfloat16) * 0.4)(D, 3 * D)),
            "w_phi": _w((lambda *s: torch.randn(*s, dtype=torch.bfloat16) * 0.4)(H, Dh, Dh) * (Dh ** -0.5)),
            "wo": _w((lambda *s: torch.randn(*s, dtype=torch.bfloat16) * 0.2)(D, D)),
        }
    _A_in = _A_setup()

    def _B_setup():
        B, S, D, F = 1, 2048, 576, 4608
        return {
            "x": _w((lambda *s: torch.randn(*s, dtype=torch.bfloat16) * 0.05)(B, S, D)),
            "w1": _w((lambda *s: torch.randn(*s, dtype=torch.bfloat16) * 0.4)(D, F) * (D ** -0.5)),
            "w2": _w((lambda *s: torch.randn(*s, dtype=torch.bfloat16) * 0.1)(F, D) * (F ** -0.5)),
        }
    _B_in = _B_setup()

    return {
        "A_x": _A_in["x"],
        "A_wqkv": _A_in["wqkv"],
        "A_w_phi": _A_in["w_phi"],
        "A_wo": _A_in["wo"],
        "B_x": _B_in["x"],
        "B_w1": _B_in["w1"],
        "B_w2": _B_in["w2"],
    }

def reference(A_x, A_wqkv, A_w_phi, A_wo, B_x, B_w1, B_w2):
    def _A_ref(x, wqkv, w_phi, wo):
        B, S, D = x.shape
        H = 32; Dh = D // H
        qkv = (lambda x, *d: x.permute(*d))((x @ wqkv).reshape(B, S, 3, H, Dh), 2, 0, 3, 1, 4)
        q, k, v = qkv[0], qkv[1], qkv[2]
        pq = torch.einsum("bhsd,hdf->bhsf", q, w_phi)
        pk = torch.einsum("bhsd,hdf->bhsf", k, w_phi)
        phi_q = torch.concatenate([_softmax(pq, axis=-1), _softmax(-pq, axis=-1)], axis=-1)
        phi_k = torch.concatenate([_softmax(pk, axis=-1), _softmax(-pk, axis=-1)], axis=-1)
        kv = torch.einsum("bhsf,bhsd->bhfd", phi_k, v)
        z  = torch.einsum("bhsf->bhf", phi_k)
        num = torch.einsum("bhqf,bhfd->bhqd", phi_q, kv)
        den = torch.einsum("bhqf,bhf->bhq", phi_q, z)[..., None] + 1e-6
        o = num / den
        return (lambda x, *d: x.permute(*d))(o, 0, 2, 1, 3).reshape(B, S, D) @ wo

    def _B_ref(x, w1, w2):
        h = torch.einsum("bsd,df->bsf", x, w1)
        h = _silu(h)
        out = torch.einsum("bsf,fd->bsd", h, w2)
        return out

    x = A_x
    xn = _layernorm(x, axis=-1)
    s = _A_ref(xn, A_wqkv, A_w_phi, A_wo)
    g = _silu(_B_ref(xn, B_w1, B_w2))
    return x + s * g
```

```cpp
#include <hip/hip_runtime.h>
#include <hip/hip_bf16.h>
typedef unsigned short u16;
__device__ __forceinline__ float bf2f(u16 v){ return __uint_as_float(((unsigned)v)<<16); }
__device__ __forceinline__ u16 f2bf(float f){ unsigned u=__float_as_uint(f); u += 0x7fffu + ((u>>16)&1u); return (u16)(u>>16); }
__device__ __forceinline__ float rbf(float f){ return bf2f(f2bf(f)); }
constexpr int S=2048, D=576, H=32, DH=18, F=4608;

__global__ void k_ln(const u16* x, u16* xn){
  int row=blockIdx.x; int t=threadIdx.x;
  float v[9]; float s=0;
  for(int i=0;i<9;i++){ v[i]=bf2f(x[row*D+t+64*i]); s+=v[i]; }
  for(int o=32;o>0;o>>=1) s+=__shfl_xor(s,o);
  float mean=s/D; float q=0;
  for(int i=0;i<9;i++){ float d=v[i]-mean; q+=d*d; }
  for(int o=32;o>0;o>>=1) q+=__shfl_xor(q,o);
  float var=q/D;
  float m=rbf(mean); float vb=rbf(var);
  float den=rbf(sqrtf(rbf(vb+1e-5f)));
  for(int i=0;i<9;i++){ xn[row*D+t+64*i]=f2bf(rbf(v[i]-m)/den); }
}
__global__ void k_gemm(const u16* A,const u16* B,u16* C,int M,int N,int K,int mode){
  __shared__ float As[16][65]; __shared__ float Bs[16][65];
  int tx=threadIdx.x&15, ty=threadIdx.x>>4;
  int bm=blockIdx.y*64, bn=blockIdx.x*64;
  float acc[4][4]={};
  for(int k0=0;k0<K;k0+=16){
    for(int i=threadIdx.x;i<1024;i+=256){ int r=i>>4,c=i&15; As[c][r]=bf2f(A[(size_t)(bm+r)*K+k0+c]); }
    for(int i=threadIdx.x;i<1024;i+=256){ int r=i>>6,c=i&63; Bs[r][c]=bf2f(B[(size_t)(k0+r)*N+bn+c]); }
    __syncthreads();
    for(int k=0;k<16;k++){
      float a[4],b[4];
      for(int i=0;i<4;i++){a[i]=As[k][ty*4+i]; b[i]=Bs[k][tx*4+i];}
      for(int i=0;i<4;i++)for(int j=0;j<4;j++)acc[i][j]+=a[i]*b[j];
    }
    __syncthreads();
  }
  for(int i=0;i<4;i++)for(int j=0;j<4;j++){
    float v=rbf(acc[i][j]);
    if(mode==1){ v = v/(1.f+__expf(-v)); }
    C[(size_t)(bm+ty*4+i)*N+bn+tx*4+j]=f2bf(v);
  }
}
__global__ __launch_bounds__(256) void k_phi(const u16* qkv,const u16* wphi,u16* phiq,u16* phik){
  int idx=blockIdx.x*blockDim.x+threadIdx.x;
  if(idx>=S*H*2) return;
  int which=idx&1; int h=(idx>>1)%H; int s=idx/(2*H);
  float q[18]; for(int d=0;d<18;d++) q[d]=bf2f(qkv[(size_t)s*1728+which*576+h*18+d]);
  float p[18]; float mx=-1e30f,mn=1e30f;
  for(int f=0;f<18;f++){ float a=0; for(int d=0;d<18;d++) a+=q[d]*bf2f(wphi[(h*18+d)*18+f]); p[f]=rbf(a); mx=fmaxf(mx,p[f]); mn=fminf(mn,p[f]); }
  float s1=0,s2=0; float e1[18],e2[18];
  for(int f=0;f<18;f++){ e1[f]=__expf(p[f]-mx); s1+=e1[f]; e2[f]=__expf(mn-p[f]); s2+=e2[f]; }
  u16* o=(which?phik:phiq)+((size_t)s*H+h)*36;
  for(int f=0;f<18;f++){ o[f]=f2bf(e1[f]/s1); o[18+f]=f2bf(e2[f]/s2); }
}
__global__ void k_kv(const u16* phik,const u16* qkv,float* kv){
  int h=blockIdx.x; int t=threadIdx.x; if(t>=36*19) return;
  int f=t/19,d=t%19; float a=0;
  for(int s=0;s<S;s++){ float p=bf2f(phik[((size_t)s*H+h)*36+f]); float v=(d<18)?bf2f(qkv[(size_t)s*1728+1152+h*18+d]):1.f; a+=p*v; }
  kv[h*684+t]=rbf(a);
}
__global__ void k_o(const u16* phiq,const float* kv,u16* o){
  int idx=blockIdx.x*blockDim.x+threadIdx.x; if(idx>=S*H) return;
  int h=idx%H,s=idx/H;
  float p[36]; for(int f=0;f<36;f++)p[f]=bf2f(phiq[((size_t)s*H+h)*36+f]);
  float den=0; for(int f=0;f<36;f++) den+=p[f]*kv[h*684+f*19+18];
  den=rbf(rbf(den)+1e-6f);
  for(int d=0;d<18;d++){ float a=0; for(int f=0;f<36;f++) a+=p[f]*kv[h*684+f*19+d]; o[(size_t)s*D+h*18+d]=f2bf(rbf(a)/den); }
}
__global__ void k_fin(const u16* x,const u16* s,const u16* g,u16* out){
  int i=blockIdx.x*blockDim.x+threadIdx.x; if(i>=S*D)return;
  out[i]=f2bf(bf2f(x[i])+rbf(bf2f(s[i])*bf2f(g[i])));
}
extern "C" void kernel_launch(void* const* d_in, const int* in_sizes, int n_in,
                              void* d_out, int out_size, void* d_ws, size_t ws_size,
                              hipStream_t stream) {
  const u16* x=(const u16*)d_in[0]; const u16* wqkv=(const u16*)d_in[1]; const u16* wphi=(const u16*)d_in[2];
  const u16* wo=(const u16*)d_in[3]; const u16* w1=(const u16*)d_in[5]; const u16* w2=(const u16*)d_in[6];
  char* ws=(char*)d_ws; size_t off=0;
  auto al=[&](size_t b){ void* p=ws+off; off+=(b+255)&~(size_t)255; return p; };
  u16* xn=(u16*)al(S*D*2); u16* qkv=(u16*)al(S*1728*2); u16* h=(u16*)al((size_t)S*F*2);
  u16* phiq=(u16*)al(S*H*36*2); u16* phik=(u16*)al(S*H*36*2); float* kv=(float*)al(H*684*4);
  u16* o=(u16*)al(S*D*2); u16* s=(u16*)al(S*D*2); u16* g=(u16*)al(S*D*2);
  k_ln<<<S,64,0,stream>>>(x,xn);
  k_gemm<<<dim3(1728/64,S/64),256,0,stream>>>(xn,wqkv,qkv,S,1728,D,0);
  k_gemm<<<dim3(F/64,S/64),256,0,stream>>>(xn,w1,h,S,F,D,1);
  k_gemm<<<dim3(D/64,S/64),256,0,stream>>>(h,w2,g,S,D,F,1);
  k_phi<<<(S*H*2+255)/256,256,0,stream>>>(qkv,wphi,phiq,phik);
  k_kv<<<H,704,0,stream>>>(phik,qkv,kv);
  k_o<<<(S*H+255)/256,256,0,stream>>>(phiq,kv,o);
  k_gemm<<<dim3(D/64,S/64),256,0,stream>>>(o,wo,s,S,D,D,0);
  k_fin<<<(S*D+255)/256,256,0,stream>>>(x,s,g,(u16*)d_out);
}
```

```cpp
#include <hip/hip_runtime.h>
#include <hip/hip_bf16.h>

constexpr unsigned G_OFF = 0u;
constexpr unsigned FH_OFF = 4096u;
constexpr unsigned FKV_OFF = 1052672u;
constexpr unsigned FE_OFF = 1314816u;
constexpr unsigned FP_OFF = 1576960u;
constexpr unsigned KV_OFF = 1708032u;
constexpr unsigned KR_OFF = 3805184u;
constexpr unsigned O_OFF = 3936256u;
constexpr unsigned PM_OFF = 6295552u;
constexpr unsigned PX_OFF = 12587008u;
constexpr unsigned H_OFF = 16781312u;
constexpr unsigned WS_END = 35655680u;
constexpr unsigned REG = 40960u;
constexpr unsigned PHI_L = 32768u;
constexpr unsigned VIM_L = 37888u;
constexpr unsigned ZER_L = 40832u;
constexpr unsigned NG = 4u;
constexpr unsigned NPF = 2u;
constexpr unsigned NWF = 1u;


__global__ __launch_bounds__(256, 1) void fused_kernel(const void* x, const void* wqkv, const void* wphi, const void* wo,
                                                       const void* w1, const void* w2, void* out, void* ws) {
  __shared__ __attribute__((aligned(1024))) char smem[163840];
  asm volatile(
    "s_load_dwordx2 s[40:41], s[0:1], 0\n"
    "s_load_dwordx2 s[44:45], s[0:1], 8\n"
    "s_load_dwordx2 s[48:49], s[0:1], 32\n"
    "s_load_dwordx2 s[64:65], s[0:1], 16\n"
    "s_load_dwordx2 s[60:61], s[0:1], 56\n"
    "s_load_dwordx2 s[52:53], s[0:1], 40\n"
    "s_load_dwordx2 s[56:57], s[0:1], 24\n"
    "s_load_dwordx2 s[4:5], s[0:1], 48\n"
    "v_lshrrev_b32 v103, 2, v0\n"
    "s_bfe_u32 s19, s2, 196611\n"
    "s_lshl_b32 s19, s19, 13\n"
    "v_lshl_add_u32 v110, v103, 7, s19\n"
    "s_getpc_b64 s[26:27]\n"
    ".Lfused_pf:\n"
    "v_min_u32 v110, .Lfused_body_end-.Lfused_pf-4, v110\n"
    "global_load_dword a253, v110, s[26:27]\n"
    "v_readfirstlane_b32 s34, v0\n"
    "s_lshr_b32 s34, s34, 6\n"
    "s_lshr_b32 s19, s2, 5\n"
    "s_bfe_u32 s35, s2, 65538\n"
    "s_lshl3_add_u32 s35, s35, s19\n"
    "s_lshl2_add_u32 s37, s2, s34\n"
    "v_and_b32 v1, 63, v0\n"
    "s_mul_i32 s68, s34, 40960\n"
    "s_lshl2_add_u32 s29, s35, s34\n"
    "s_lshr_b32 s11, s2, 5\n"
    "s_sub_u32 s19, s11, 9\n"
    "s_cmp_ge_u32 s11, 9\n"
    "s_cselect_b32 s11, s19, s11\n"
    "s_lshl_b32 s19, s11, 7\n"
    "s_mul_i32 s88, s29, 36864\n"
    "s_add_u32 s88, s88, s19\n"
    "v_lshrrev_b32 v89, 4, v1\n"
    "v_and_b32 v102, 7, v1\n"
    "v_xor_b32 v103, v102, v89\n"
    "v_lshrrev_b32 v101, 3, v1\n"
    "v_mul_u32_u24 v102, 1152, v101\n"
    "v_lshl_add_u32 v104, v103, 4, v102\n"
    "s_lshr_b32 s13, s2, 3\n"
    "s_lshl2_add_u32 s13, s13, s34\n"
    "s_and_b32 s19, s2, 7\n"
    "s_lshl_b32 s19, s19, 7\n"
    "s_add_u32 s19, s19, s13\n"
    "s_mul_i32 s19, s19, 2304\n"
    "v_lshl_add_u32 v103, v1, 7, s19\n"
    "v_cmp_gt_u32 vcc, 18, v1\n"
    "s_nop 1\n"
    "v_cndmask_b32 v108, 2.0, v103, vcc\n"
    "s_mov_b32 s42, 2359296\n"
    "s_mov_b32 s43, 131072\n"
    "s_mov_b32 s46, 1990656\n"
    "s_mov_b32 s47, 131072\n"
    "v_xor_b32 v103, 64, v104\n"
    "v_add_u32 v105, 8192, v103\n"
    "v_add_u32 v106, 16384, v104\n"
    "v_add_u32 v107, 16384, v105\n"
    "v_mul_u32_u24 v103, 43, v1\n"
    "v_lshrrev_b32 v109, 7, v103\n"
    "v_mul_u32_u24 v102, 768, v109\n"
    "s_and_b32 s24, s2, 3\n"
    "s_lshl_b32 s24, s24, 8\n"
    "s_bfe_u32 s19, s2, 65538\n"
    "s_mul_i32 s19, s19, 96\n"
    "s_add_u32 s19, s19, s13\n"
    "s_mul_i32 s19, s19, 10368\n"
    "s_add_u32 s19, s19, s24\n"
    "v_lshl_add_u32 v101, v1, 7, s19\n"
    "v_add_u32 v103, v102, v101\n"
    "v_cmp_gt_u32 vcc, 27, v1\n"
    "s_nop 1\n"
    "v_cndmask_b32 v109, 2.0, v103, vcc\n"
    "s_bfe_u32 s19, s2, 131075\n"
    "s_and_b32 s36, s2, 3\n"
    "s_lshl2_add_u32 s36, s36, s19\n"
    "s_lshr_b32 s39, s36, 2\n"
    "s_and_b32 s3, s36, 3\n"
    "v_lshrrev_b32 v84, 5, v1\n"
    "v_and_b32 v85, 31, v1\n"
    "v_bfe_u32 v86, v1, 4, 1\n"
    "v_bfe_u32 v87, v1, 2, 2\n"
    "v_and_b32 v88, 3, v1\n"
    "v_lshl_add_u32 v90, v86, 2, v88\n"
    "s_add_u32 s19, s68, 2048\n"
    "v_lshl_add_u32 v83, v85, 7, s19\n"
    "v_bfe_u32 v103, v85, 1, 3\n"
    "v_xor_b32 v82, v103, v84\n"
    "v_lshl_add_u32 v48, v82, 4, v83\n"
    "v_xor_b32 v103, 2, v82\n"
    "v_lshl_add_u32 v49, v103, 4, v83\n"
    "v_xor_b32 v103, 4, v82\n"
    "v_lshl_add_u32 v50, v103, 4, v83\n"
    "v_xor_b32 v103, 6, v82\n"
    "v_lshl_add_u32 v51, v103, 4, v83\n"
    "s_mov_b32 s50, 5308416\n"
    "s_mov_b32 s51, 131072\n"
    "s_mov_b32 s66, 20736\n"
    "s_mov_b32 s67, 131072\n"
    "s_mov_b32 s62, 36704256\n"
    "s_mov_b32 s63, 131072\n"
    "v_mov_b32 v32, 1065369472\n"
    "s_mov_b32 s98, 221184\n"
    "s_mov_b32 s81, 589824\n"
    "s_mov_b32 s54, 5308416\n"
    "s_mov_b32 s55, 131072\n"
    "s_mov_b32 s58, 663552\n"
    "s_mov_b32 s59, 131072\n"
    "s_mov_b32 s25, 4294901760\n"
    "s_mov_b32 s26, 0\n"
    "s_mov_b32 s27, -1\n"
    "v_mov_b32 v15, 0\n"
    "s_and_b32 s15, s34, 1\n"
    "s_lshr_b32 s16, s34, 1\n"
    "s_and_b32 s95, s36, 1\n"
    "s_mov_b32 s94, 1152\n"
    "s_mov_b32 s8, 80\n"
    "s_lshl1_add_u32 s19, s15, s16\n"
    "s_lshl4_add_u32 s28, s11, s19\n"
    "v_lshl_add_u32 v81, v89, 2, v88\n"
    "v_mov_b32 v102, 16\n"
    "v_add_u32 v100, 2, v81\n"
    "v_add_u32 v99, 6, v81\n"
    "v_cmp_gt_u32 vcc, 11, v81\n"
    "s_nop 1\n"
    "v_cndmask_b32 v101, v99, v100, vcc\n"
    "v_cmp_eq_u32 vcc, 5, v81\n"
    "s_nop 1\n"
    "v_cndmask_b32 v103, v101, v102, vcc\n"
    "v_cmp_gt_u32 vcc, 5, v81\n"
    "s_nop 1\n"
    "v_cndmask_b32 v80, v103, v81, vcc\n"
    "v_and_b32 v79, 7, v80\n"
    "s_xor_b32 s19, s36, 1\n"
    "s_mul_i32 s19, s19, 72\n"
    "v_mov_b32 v65, s19\n"
    "s_mul_i32 s19, s36, 72\n"
    "v_mov_b32 v64, s19\n"
    "v_lshrrev_b32 v100, 3, v80\n"
    "v_mad_u32_u24 v101, v87, 3, v100\n"
    "v_lshlrev_b32 v99, 4, v79\n"
    "v_cmp_gt_u32 vcc, 4, v79\n"
    "s_nop 1\n"
    "v_cndmask_b32 v100, 56, v99, vcc\n"
    "v_mad_u32_u24 v102, v101, s94, v100\n"
    "v_cmp_gt_u32 vcc, 5, v81\n"
    "s_nop 1\n"
    "v_cndmask_b32 v101, v64, v65, vcc\n"
    "v_add_u32 v103, v102, v101\n"
    "v_cmp_ne_u32 vcc, 15, v81\n"
    "s_nop 1\n"
    "v_cndmask_b32 v18, 2.0, v103, vcc\n"
    "v_lshl_add_u32 v102, v89, 4, v87\n"
    "v_mul_u32_u24 v103, 9216, v102\n"
    "v_lshl_add_u32 v20, v88, 4, v103\n"
    "s_mul_i32 s69, s28, 13824\n"
    "s_lshl_b32 s19, s95, 6\n"
    "s_mul_i32 s24, s36, 576\n"
    "s_mul_i32 s70, s28, 36864\n"
    "s_add_u32 s70, s70, s24\n"
    "s_add_u32 s70, s70, s19\n"
    "s_mul_i32 s19, s95, 576\n"
    "s_add_u32 s72, s70, 512\n"
    "s_sub_u32 s72, s72, s19\n"
    "s_add_u32 s71, s70, 256\n"
    "v_mul_u32_u24 v102, 40960, v84\n"
    "v_lshl_add_u32 v103, v87, 6, v102\n"
    "v_lshl_add_u32 v16, v90, 3, v103\n"
    "v_add_u32 v17, 81920, v16\n"
    "v_mul_u32_u24 v103, 9216, v87\n"
    "v_lshl_add_u32 v19, v81, 4, v103\n"
    "s_waitcnt lgkmcnt(0)\n"
    "s_add_u32 m0, s68, 2048\n"
    "buffer_load_dword a254, v108, s[40:43], 0 offen\n"
    "buffer_load_dwordx4 v104, s[40:43], s88 offen lds\n"
    "buffer_load_dwordx4 v105, s[40:43], s88 offen offset:1024 lds\n"
    "buffer_load_dwordx4 v106, s[40:43], s88 offen offset:2048 lds\n"
    "buffer_load_dwordx4 v107, s[40:43], s88 offen offset:3072 lds\n"
    "s_add_u32 m0, s68, 6144\n"
    "s_cmp_eq_u32 s11, 8\n"
    "s_cselect_b32 s87, 4294966144, 0\n"
    "s_add_u32 s88, s88, 128\n"
    "s_add_u32 s88, s88, s87\n"
    "buffer_load_dwordx4 v104, s[40:43], s88 offen lds\n"
    "v_mul_u32_u24 v103, 1152, v87\n"
    "v_lshl_add_u32 v65, v88, 4, v103\n"
    "v_add_u32 v102, s39, v89\n"
    "v_and_b32 v103, 3, v102\n"
    "v_lshl_add_u32 v21, v103, 6, v65\n"
    "v_and_b32 v103, 15, v1\n"
    "v_lshlrev_b32 v102, 12, v89\n"
    "v_lshl_add_u32 v22, v103, 4, v102\n"
    "v_mul_u32_u24 v103, 18432, v89\n"
    "s_lshl_b32 s24, s3, 8\n"
    "s_lshl_b32 s19, s95, 6\n"
    "s_add_u32 s19, s19, 1024\n"
    "s_sub_u32 s19, s19, s24\n"
    "v_add_u32 v102, s19, v65\n"
    "buffer_load_dwordx4 v105, s[40:43], s88 offen offset:1024 lds\n"
    "v_add_u32 v23, v103, v102\n"
    "v_lshl_add_u32 v75, v84, 2, v87\n"
    "v_lshrrev_b32 v99, 1, v87\n"
    "v_and_b32 v98, 1, v87\n"
    "v_mad_u32_u24 v100, v99, 36, v98\n"
    "v_add_u32 v101, 16, v100\n"
    "v_mul_u32_u24 v102, 1152, v101\n"
    "v_lshl_add_u32 v103, v88, 4, v102\n"
    "v_add_u32 v102, s16, v89\n"
    "v_cmp_eq_u32 vcc, 0, v102\n"
    "s_nop 1\n"
    "v_cndmask_b32 v24, 2.0, v103, vcc\n"
    "v_lshlrev_b32 v25, 4, v1\n"
    "v_lshlrev_b32 v103, 2, v1\n"
    "v_cmp_gt_u32 vcc, 32, v1\n"
    "s_nop 1\n"
    "v_cndmask_b32 v26, 2.0, v103, vcc\n"
    "buffer_load_dwordx4 v106, s[40:43], s88 offen offset:2048 lds\n"
    "v_lshlrev_b32 v27, 7, v88\n"
    "v_and_b32 v103, 15, v1\n"
    "v_lshlrev_b32 v28, 7, v103\n"
    "v_mul_u32_u24 v103, 40, v75\n"
    "v_add_u32 v71, 38912, v103\n"
    "v_lshrrev_b32 v103, 1, v90\n"
    "v_lshl_add_u32 v74, v103, 3, v71\n"
    "v_and_b32 v73, 1, v88\n"
    "v_mov_b32 v72, 40352\n"
    "v_mov_b32 v63, v72\n"
    "v_mov_b32 v57, v72\n"
    "v_mov_b32 v103, 1\n"
    "v_cmp_le_u32 vcc, 2, v75\n"
    "s_nop 1\n"
    "v_cndmask_b32 v70, 0, v103, vcc\n"
    "buffer_load_dwordx4 v107, s[40:43], s88 offen offset:3072 lds\n"
    "v_cmp_eq_u32 vcc, 1, v84\n"
    "s_nop 1\n"
    "v_cndmask_b32 v68, 9, v84, vcc\n"
    "v_cmp_eq_u32 vcc, 0, v73\n"
    "s_nop 1\n"
    "v_cndmask_b32 v52, v72, v74, vcc\n"
    "v_add_u32 v103, 32, v71\n"
    "v_cmp_eq_u32 vcc, 0, v90\n"
    "s_nop 1\n"
    "v_cndmask_b32 v58, v72, v103, vcc\n"
    "v_add_u32 v103, 320, v74\n"
    "v_cmp_eq_u32 vcc, 0, v73\n"
    "s_nop 1\n"
    "v_cndmask_b32 v53, v72, v103, vcc\n"
    "v_add_u32 v103, 352, v71\n"
    "v_cmp_eq_u32 vcc, 0, v90\n"
    "s_nop 1\n"
    "v_cndmask_b32 v59, v72, v103, vcc\n"
    "v_add_u32 v103, 640, v74\n"
    "s_add_u32 m0, s68, 10240\n"
    "s_cmp_eq_u32 s11, 7\n"
    "s_cselect_b32 s87, 4294966144, 0\n"
    "s_add_u32 s88, s88, 128\n"
    "s_add_u32 s88, s88, s87\n"
    "buffer_load_dwordx4 v104, s[40:43], s88 offen lds\n"
    "v_cmp_eq_u32 vcc, v70, v73\n"
    "s_nop 1\n"
    "v_cndmask_b32 v54, v72, v103, vcc\n"
    "v_add_u32 v103, 672, v71\n"
    "v_cmp_eq_u32 vcc, v70, v90\n"
    "s_nop 1\n"
    "v_cndmask_b32 v60, v72, v103, vcc\n"
    "v_add_u32 v103, 960, v74\n"
    "v_cmp_eq_u32 vcc, 1, v73\n"
    "s_nop 1\n"
    "v_cndmask_b32 v55, v72, v103, vcc\n"
    "v_add_u32 v103, 992, v71\n"
    "v_cmp_eq_u32 vcc, 1, v90\n"
    "s_nop 1\n"
    "v_cndmask_b32 v61, v72, v103, vcc\n"
    "v_add_u32 v103, 1120, v74\n"
    "v_cmp_eq_u32 vcc, v68, v73\n"
    "s_nop 1\n"
    "v_cndmask_b32 v56, v72, v103, vcc\n"
    "buffer_load_dwordx4 v105, s[40:43], s88 offen offset:1024 lds\n"
    "v_add_u32 v103, 1152, v71\n"
    "v_cmp_eq_u32 vcc, v68, v90\n"
    "s_nop 1\n"
    "v_cndmask_b32 v62, v72, v103, vcc\n"
    "v_mul_u32_u24 v70, 80, v85\n"
    "v_lshl_add_u32 v71, v84, 3, v70\n"
    "v_lshl_add_u32 v103, v84, 3, v87\n"
    "v_mul_u32_u24 v74, 80, v103\n"
    "s_add_u32 s28, s68, 37888\n"
    "s_mul_i32 s19, s15, 40\n"
    "s_mul_i32 s29, s16, 40960\n"
    "s_add_u32 s29, s29, s19\n"
    "s_add_u32 s29, s29, 38912\n"
    "s_add_u32 s19, s68, 32768\n"
    "v_lshl_add_u32 v102, v85, 1, v84\n"
    "buffer_load_dwordx4 v106, s[40:43], s88 offen offset:2048 lds\n"
    "v_mul_u32_u24 v103, 80, v102\n"
    "v_add_u32 v69, s19, v103\n"
    "v_accvgpr_write_b32 a144, v69\n"
    "v_add_u32 v69, s28, v71\n"
    "v_accvgpr_write_b32 a145, v69\n"
    "s_add_u32 s19, s28, 72\n"
    "v_add_u32 v103, s19, v70\n"
    "v_lshlrev_b32 v102, 3, v84\n"
    "v_sub_u32 v69, v103, v102\n"
    "v_accvgpr_write_b32 a146, v69\n"
    "s_mul_i32 s19, s34, 40\n"
    "s_add_u32 s19, s19, 32768\n"
    "v_lshl_add_u32 v103, v90, 3, s19\n"
    "v_lshl_add_u32 v69, v74, 1, v103\n"
    "buffer_load_dwordx4 v107, s[40:43], s88 offen offset:3072 lds\n"
    "v_accvgpr_write_b32 a147, v69\n"
    "s_lshl_b32 s19, s16, 5\n"
    "s_add_u32 s19, s19, 37888\n"
    "v_lshl_add_u32 v102, v90, 3, s19\n"
    "v_mov_b32 v101, 37960\n"
    "v_cmp_gt_u32 vcc, 5, v90\n"
    "s_nop 1\n"
    "v_cndmask_b32 v103, v101, v102, vcc\n"
    "v_add_u32 v69, v74, v103\n"
    "v_accvgpr_write_b32 a148, v69\n"
    "v_mad_u32_u24 v102, v84, 24, v85\n"
    "v_lshlrev_b32 v103, 4, v102\n"
    "v_cmp_gt_u32 vcc, 24, v85\n"
    "s_nop 1\n"
    "v_cndmask_b32 v69, 2.0, v103, vcc\n"
    "v_accvgpr_write_b32 a149, v69\n"
    "s_add_u32 m0, s68, 14336\n"
    "s_cmp_eq_u32 s11, 6\n"
    "s_cselect_b32 s87, 4294966144, 0\n"
    "s_add_u32 s88, s88, 128\n"
    "s_add_u32 s88, s88, s87\n"
    "s_waitcnt vmcnt(8)\n"
    "ds_read_b128 v[112:115], v48\n"
    "ds_read_b128 v[116:119], v49\n"
    "ds_read_b128 v[120:123], v50\n"
    "ds_read_b128 v[124:127], v51\n"
    "buffer_load_dwordx4 v104, s[40:43], s88 offen lds\n"
    "v_lshlrev_b32 v103, 3, v1\n"
    "v_cmp_gt_u32 vcc, 32, v1\n"
    "s_nop 1\n"
    "v_cndmask_b32 v69, 2.0, v103, vcc\n"
    "v_accvgpr_write_b32 a150, v69\n"
    "v_add_u32 v103, s29, v71\n"
    "v_lshlrev_b32 v101, 3, v1\n"
    "buffer_load_dwordx4 v105, s[40:43], s88 offen offset:1024 lds\n"
    "v_add_u32 v102, 120832, v101\n"
    "v_cmp_gt_u32 vcc, 24, v85\n"
    "s_nop 1\n"
    "v_cndmask_b32 v69, v102, v103, vcc\n"
    "v_accvgpr_write_b32 a151, v69\n"
    "s_add_u32 s19, s29, 32\n"
    "v_add_u32 v103, s19, v70\n"
    "buffer_load_dwordx4 v106, s[40:43], s88 offen offset:2048 lds\n"
    "v_lshlrev_b32 v101, 3, v1\n"
    "v_add_u32 v102, 120832, v101\n"
    "v_cmp_gt_u32 vcc, 24, v1\n"
    "s_nop 1\n"
    "v_cndmask_b32 v69, v102, v103, vcc\n"
    "v_accvgpr_write_b32 a152, v69\n"
    "v_and_b32 v68, 1, v87\n"
    "buffer_load_dwordx4 v107, s[40:43], s88 offen offset:3072 lds\n"
    "v_lshrrev_b32 v103, 3, v85\n"
    "v_lshl_add_u32 v67, v103, 2, v88\n"
    "s_add_u32 s19, s68, 40832\n"
    "v_mov_b32 v66, s19\n"
    "v_mul_u32_u24 v103, 41120, v84\n"
    "v_mul_u32_u24 v101, 80, v67\n"
    "s_add_u32 m0, s68, 18432\n"
    "s_cmp_eq_u32 s11, 5\n"
    "s_cselect_b32 s87, 4294966144, 0\n"
    "s_add_u32 s88, s88, 128\n"
    "s_add_u32 s88, s88, s87\n"
    "s_waitcnt vmcnt(8) lgkmcnt(0)\n"
    "ds_read_b128 v[128:131], v48 offset:4096\n"
    "ds_read_b128 v[132:135], v49 offset:4096\n"
    "ds_read_b128 v[136:139], v50 offset:4096\n"
    "ds_read_b128 v[140:143], v51 offset:4096\n"
    "buffer_load_dwordx4 v104, s[40:43], s88 offen lds\n"
    "v_add_u32 v102, 38912, v101\n"
    "v_dot2_f32_bf16 v36, v112, v32, 0\n"
    "v_add_u32 v74, v103, v102\n"
    "v_dot2_f32_bf16 v40, v112, v112, 0\n"
    "v_cmp_eq_u32 vcc, v84, v68\n"
    "v_dot2_f32_bf16 v37, v113, v32, 0\n"
    "v_dot2_f32_bf16 v41, v113, v113, 0\n"
    "v_cndmask_b32 v69, v66, v74, vcc\n"
    "v_accvgpr_write_b32 a153, v69\n"
    "v_dot2_f32_bf16 v38, v114, v32, 0\n"
    "v_add_u32 v102, 1280, v74\n"
    "v_dot2_f32_bf16 v42, v114, v114, 0\n"
    "v_dot2_f32_bf16 v39, v115, v32, 0\n"
    "v_dot2_f32_bf16 v43, v115, v115, 0\n"
    "buffer_load_dwordx4 v105, s[40:43], s88 offen offset:1024 lds\n"
    "v_mul_u32_u24 v100, 40960, v84\n"
    "v_dot2_f32_bf16 v36, v116, v32, v36\n"
    "v_add_u32 v101, 40512, v100\n"
    "v_dot2_f32_bf16 v40, v116, v116, v40\n"
    "v_cmp_gt_u32 vcc, 2, v67\n"
    "v_dot2_f32_bf16 v37, v117, v32, v37\n"
    "v_dot2_f32_bf16 v41, v117, v117, v41\n"
    "v_cndmask_b32 v103, v101, v102, vcc\n"
    "v_xor_b32 v101, v68, v84\n"
    "v_dot2_f32_bf16 v38, v118, v32, v38\n"
    "v_lshl_add_u32 v102, v101, 2, v67\n"
    "v_dot2_f32_bf16 v42, v118, v118, v42\n"
    "v_dot2_f32_bf16 v39, v119, v32, v39\n"
    "v_dot2_f32_bf16 v43, v119, v119, v43\n"
    "buffer_load_dwordx4 v106, s[40:43], s88 offen offset:2048 lds\n"
    "v_cmp_gt_u32 vcc, 3, v102\n"
    "v_dot2_f32_bf16 v36, v120, v32, v36\n"
    "v_dot2_f32_bf16 v40, v120, v120, v40\n"
    "v_cndmask_b32 v69, v66, v103, vcc\n"
    "v_accvgpr_write_b32 a154, v69\n"
    "v_dot2_f32_bf16 v37, v121, v32, v37\n"
    "v_and_b32 v102, 7, v1\n"
    "v_dot2_f32_bf16 v41, v121, v121, v41\n"
    "v_lshrrev_b32 v100, 3, v1\n"
    "v_dot2_f32_bf16 v38, v122, v32, v38\n"
    "v_lshlrev_b32 v101, 12, v100\n"
    "v_dot2_f32_bf16 v42, v122, v122, v42\n"
    "v_dot2_f32_bf16 v39, v123, v32, v39\n"
    "v_dot2_f32_bf16 v43, v123, v123, v43\n"
    "buffer_load_dwordx4 v107, s[40:43], s88 offen offset:3072 lds\n"
    "v_lshl_add_u32 v103, v102, 7, v101\n"
    "v_dot2_f32_bf16 v36, v124, v32, v36\n"
    "v_cmp_gt_u32 vcc, 32, v1\n"
    "v_dot2_f32_bf16 v40, v124, v124, v40\n"
    "v_dot2_f32_bf16 v37, v125, v32, v37\n"
    "v_cndmask_b32 v69, 2.0, v103, vcc\n"
    "v_accvgpr_write_b32 a155, v69\n"
    "v_dot2_f32_bf16 v41, v125, v125, v41\n"
    "v_mul_u32_u24 v103, 1152, v85\n"
    "v_dot2_f32_bf16 v38, v126, v32, v38\n"
    "v_lshl_add_u32 v69, v84, 4, v103\n"
    "v_dot2_f32_bf16 v42, v126, v126, v42\n"
    "v_dot2_f32_bf16 v39, v127, v32, v39\n"
    "v_dot2_f32_bf16 v43, v127, v127, v43\n"
    "s_add_u32 m0, s68, 22528\n"
    "s_cmp_eq_u32 s11, 4\n"
    "s_cselect_b32 s87, 4294966144, 0\n"
    "s_add_u32 s88, s88, 128\n"
    "s_add_u32 s88, s88, s87\n"
    "s_waitcnt vmcnt(8) lgkmcnt(0)\n"
    "ds_read_b128 v[144:147], v48 offset:8192\n"
    "ds_read_b128 v[148:151], v49 offset:8192\n"
    "ds_read_b128 v[152:155], v50 offset:8192\n"
    "ds_read_b128 v[156:159], v51 offset:8192\n"
    "buffer_load_dwordx4 v104, s[40:43], s88 offen lds\n"
    "v_accvgpr_write_b32 a156, v69\n"
    "v_dot2_f32_bf16 v36, v128, v32, v36\n"
    "v_lshrrev_b32 v102, 1, v88\n"
    "v_dot2_f32_bf16 v40, v128, v128, v40\n"
    "v_lshrrev_b32 v100, 2, v1\n"
    "v_dot2_f32_bf16 v37, v129, v32, v37\n"
    "v_mul_u32_u24 v101, 1152, v100\n"
    "v_dot2_f32_bf16 v41, v129, v129, v41\n"
    "v_lshl_add_u32 v103, v102, 4, v101\n"
    "v_dot2_f32_bf16 v38, v130, v32, v38\n"
    "s_mul_i32 s24, s15, 18432\n"
    "v_dot2_f32_bf16 v42, v130, v130, v42\n"
    "v_dot2_f32_bf16 v39, v131, v32, v39\n"
    "v_dot2_f32_bf16 v43, v131, v131, v43\n"
    "buffer_load_dwordx4 v105, s[40:43], s88 offen offset:1024 lds\n"
    "s_lshl3_add_u32 s19, s16, s24\n"
    "v_dot2_f32_bf16 v36, v132, v32, v36\n"
    "v_lshl_add_u32 v102, v73, 2, s19\n"
    "v_dot2_f32_bf16 v40, v132, v132, v40\n"
    "v_add_u32 v69, v103, v102\n"
    "v_dot2_f32_bf16 v37, v133, v32, v37\n"
    "v_accvgpr_write_b32 a157, v69\n"
    "v_dot2_f32_bf16 v41, v133, v133, v41\n"
    "v_mul_u32_u24 v102, 36, v84\n"
    "v_dot2_f32_bf16 v38, v134, v32, v38\n"
    "v_lshl_add_u32 v103, v86, 3, v102\n"
    "v_dot2_f32_bf16 v42, v134, v134, v42\n"
    "v_dot2_f32_bf16 v39, v135, v32, v39\n"
    "v_dot2_f32_bf16 v43, v135, v135, v43\n"
    "buffer_load_dwordx4 v106, s[40:43], s88 offen offset:2048 lds\n"
    "v_mad_u32_u24 v69, v103, s94, v65\n"
    "v_dot2_f32_bf16 v36, v136, v32, v36\n"
    "v_accvgpr_write_b32 a158, v69\n"
    "v_dot2_f32_bf16 v40, v136, v136, v40\n"
    "v_lshl_add_u32 v98, v89, 1, s16\n"
    "v_dot2_f32_bf16 v37, v137, v32, v37\n"
    "s_lshr_b32 s19, s36, 1\n"
    "v_dot2_f32_bf16 v41, v137, v137, v41\n"
    "v_lshl_add_u32 v99, v98, 0, s19\n"
    "v_dot2_f32_bf16 v38, v138, v32, v38\n"
    "v_and_b32 v100, 7, v99\n"
    "v_dot2_f32_bf16 v42, v138, v138, v42\n"
    "v_dot2_f32_bf16 v39, v139, v32, v39\n"
    "v_dot2_f32_bf16 v43, v139, v139, v43\n"
    "buffer_load_dwordx4 v107, s[40:43], s88 offen offset:3072 lds\n"
    "v_lshrrev_b32 v99, 1, v87\n"
    "v_dot2_f32_bf16 v36, v140, v32, v36\n"
    "v_lshl_add_u32 v101, v100, 1, v99\n"
    "v_dot2_f32_bf16 v40, v140, v140, v40\n"
    "v_mad_u32_u24 v102, v101, 36, v68\n"
    "v_dot2_f32_bf16 v37, v141, v32, v37\n"
    "v_mul_u32_u24 v103, 1152, v102\n"
    "v_dot2_f32_bf16 v41, v141, v141, v41\n"
    "v_lshl_add_u32 v69, v88, 4, v103\n"
    "v_dot2_f32_bf16 v38, v142, v32, v38\n"
    "v_accvgpr_write_b32 a159, v69\n"
    "v_dot2_f32_bf16 v42, v142, v142, v42\n"
    "v_dot2_f32_bf16 v39, v143, v32, v39\n"
    "v_dot2_f32_bf16 v43, v143, v143, v43\n"
    "s_add_u32 m0, s68, 26624\n"
    "s_cmp_eq_u32 s11, 3\n"
    "s_cselect_b32 s87, 4294966144, 0\n"
    "s_add_u32 s88, s88, 128\n"
    "s_add_u32 s88, s88, s87\n"
    "s_waitcnt vmcnt(8) lgkmcnt(0)\n"
    "ds_read_b128 v[160:163], v48 offset:12288\n"
    "ds_read_b128 v[164:167], v49 offset:12288\n"
    "ds_read_b128 v[168:171], v50 offset:12288\n"
    "ds_read_b128 v[172:175], v51 offset:12288\n"
    "buffer_load_dwordx4 v104, s[40:43], s88 offen lds\n"
    "v_lshrrev_b32 v103, 2, v1\n"
    "v_dot2_f32_bf16 v36, v144, v32, v36\n"
    "s_cmp_ge_u32 s35, 14\n"
    "s_cselect_b32 s19, 1073741824, 0\n"
    "v_dot2_f32_bf16 v40, v144, v144, v40\n"
    "v_lshl_add_u32 v102, v88, 5, s19\n"
    "v_dot2_f32_bf16 v37, v145, v32, v37\n"
    "v_lshl_add_u32 v69, v103, 11, v102\n"
    "v_dot2_f32_bf16 v41, v145, v145, v41\n"
    "v_accvgpr_write_b32 a160, v69\n"
    "v_dot2_f32_bf16 v38, v146, v32, v38\n"
    "v_lshlrev_b32 v102, 5, v88\n"
    "v_dot2_f32_bf16 v42, v146, v146, v42\n"
    "v_dot2_f32_bf16 v39, v147, v32, v39\n"
    "v_dot2_f32_bf16 v43, v147, v147, v43\n"
    "buffer_load_dwordx4 v105, s[40:43], s88 offen offset:1024 lds\n"
    "v_lshl_add_u32 v103, v89, 3, v102\n"
    "v_dot2_f32_bf16 v36, v148, v32, v36\n"
    "v_cmp_eq_u32 vcc, 0, v87\n"
    "v_dot2_f32_bf16 v40, v148, v148, v40\n"
    "v_dot2_f32_bf16 v37, v149, v32, v37\n"
    "v_cndmask_b32 v69, 2.0, v103, vcc\n"
    "v_accvgpr_write_b32 a161, v69\n"
    "v_dot2_f32_bf16 v41, v149, v149, v41\n"
    "v_lshrrev_b32 v102, 2, v1\n"
    "v_dot2_f32_bf16 v38, v150, v32, v38\n"
    "v_mul_u32_u24 v103, 1152, v102\n"
    "v_dot2_f32_bf16 v42, v150, v150, v42\n"
    "v_dot2_f32_bf16 v39, v151, v32, v39\n"
    "v_dot2_f32_bf16 v43, v151, v151, v43\n"
    "buffer_load_dwordx4 v106, s[40:43], s88 offen offset:2048 lds\n"
    "v_lshl_add_u32 v69, v88, 4, v103\n"
    "v_dot2_f32_bf16 v36, v152, v32, v36\n"
    "v_accvgpr_write_b32 a162, v69\n"
    "v_dot2_f32_bf16 v40, v152, v152, v40\n"
    "v_lshl_add_u32 v103, v84, 3, s68\n"
    "v_dot2_f32_bf16 v37, v153, v32, v37\n"
    "v_lshl_add_u32 v69, v85, 6, v103\n"
    "v_dot2_f32_bf16 v41, v153, v153, v41\n"
    "v_accvgpr_write_b32 a163, v69\n"
    "v_dot2_f32_bf16 v38, v154, v32, v38\n"
    "s_and_b32 s24, s13, 31\n"
    "v_dot2_f32_bf16 v42, v154, v154, v42\n"
    "v_dot2_f32_bf16 v39, v155, v32, v39\n"
    "v_dot2_f32_bf16 v43, v155, v155, v43\n"
    "buffer_load_dwordx4 v107, s[40:43], s88 offen offset:3072 lds\n"
    "s_mul_i32 s24, s24, 3\n"
    "v_dot2_f32_bf16 v36, v156, v32, v36\n"
    "s_lshr_b32 s23, s13, 5\n"
    "v_dot2_f32_bf16 v40, v156, v156, v40\n"
    "s_mul_i32 s23, s23, 288\n"
    "v_dot2_f32_bf16 v37, v157, v32, v37\n"
    "s_mul_i32 s19, s39, 1152\n"
    "v_dot2_f32_bf16 v41, v157, v157, v41\n"
    "s_add_u32 s19, s19, s23\n"
    "v_dot2_f32_bf16 v38, v158, v32, v38\n"
    "s_add_u32 s19, s19, s24\n"
    "v_dot2_f32_bf16 v42, v158, v158, v42\n"
    "v_dot2_f32_bf16 v39, v159, v32, v39\n"
    "v_dot2_f32_bf16 v43, v159, v159, v43\n"
    "s_add_u32 m0, s68, 30720\n"
    "s_cmp_eq_u32 s11, 2\n"
    "s_cselect_b32 s87, 4294966144, 0\n"
    "s_add_u32 s88, s88, 128\n"
    "s_add_u32 s88, s88, s87\n"
    "s_waitcnt vmcnt(8) lgkmcnt(0)\n"
    "ds_read_b128 v[176:179], v48 offset:16384\n"
    "ds_read_b128 v[180:183], v49 offset:16384\n"
    "ds_read_b128 v[184:187], v50 offset:16384\n"
    "ds_read_b128 v[188:191], v51 offset:16384\n"
    "buffer_load_dwordx4 v104, s[40:43], s88 offen lds\n"
    "s_mul_i32 s13, s19, 1152\n"
    "v_dot2_f32_bf16 v36, v160, v32, v36\n"
    "s_lshr_b32 s19, s37, 2\n"
    "v_dot2_f32_bf16 v40, v160, v160, v40\n"
    "s_lshl_b32 s82, s34, 8\n"
    "v_dot2_f32_bf16 v37, v161, v32, v37\n"
    "s_add_u32 s82, s82, s19\n"
    "v_dot2_f32_bf16 v41, v161, v161, v41\n"
    "s_mul_i32 s82, s82, 3456\n"
    "v_dot2_f32_bf16 v38, v162, v32, v38\n"
    "s_mul_i32 s28, s36, 1296\n"
    "v_dot2_f32_bf16 v42, v162, v162, v42\n"
    "v_dot2_f32_bf16 v39, v163, v32, v39\n"
    "v_dot2_f32_bf16 v43, v163, v163, v43\n"
    "buffer_load_dwordx4 v105, s[40:43], s88 offen offset:1024 lds\n"
    "s_xor_b32 s29, s36, 1\n"
    "v_dot2_f32_bf16 v36, v164, v32, v36\n"
    "s_mul_i32 s29, s29, 1296\n"
    "v_dot2_f32_bf16 v40, v164, v164, v40\n"
    "s_lshl_b32 s19, s34, 6\n"
    "v_dot2_f32_bf16 v37, v165, v32, v37\n"
    "v_add_u32 v79, s19, v0\n"
    "v_dot2_f32_bf16 v41, v165, v165, v41\n"
    "v_mul_u32_u24 v103, 6554, v79\n"
    "v_dot2_f32_bf16 v38, v166, v32, v38\n"
    "v_lshrrev_b32 v78, 16, v103\n"
    "v_dot2_f32_bf16 v42, v166, v166, v42\n"
    "v_dot2_f32_bf16 v39, v167, v32, v39\n"
    "v_dot2_f32_bf16 v43, v167, v167, v43\n"
    "buffer_load_dwordx4 v106, s[40:43], s88 offen offset:2048 lds\n"
    "v_mul_u32_u24 v103, 10, v78\n"
    "v_dot2_f32_bf16 v36, v168, v32, v36\n"
    "v_sub_u32 v65, v79, v103\n"
    "v_dot2_f32_bf16 v40, v168, v168, v40\n"
    "v_mul_u32_u24 v101, 36, v78\n"
    "v_dot2_f32_bf16 v37, v169, v32, v37\n"
    "v_lshl_add_u32 v102, v65, 2, v101\n"
    "v_dot2_f32_bf16 v41, v169, v169, v41\n"
    "v_cmp_gt_u32 vcc, 9, v65\n"
    "v_dot2_f32_bf16 v38, v170, v32, v38\n"
    "v_dot2_f32_bf16 v42, v170, v170, v42\n"
    "v_cndmask_b32 v103, 2.0, v102, vcc\n"
    "v_dot2_f32_bf16 v39, v171, v32, v39\n"
    "v_dot2_f32_bf16 v43, v171, v171, v43\n"
    "buffer_load_dwordx4 v107, s[40:43], s88 offen offset:3072 lds\n"
    "v_cmp_gt_u32 vcc, 36, v78\n"
    "v_dot2_f32_bf16 v36, v172, v32, v36\n"
    "v_dot2_f32_bf16 v40, v172, v172, v40\n"
    "v_cndmask_b32 v76, 2.0, v103, vcc\n"
    "s_lshl_b32 s19, s34, 6\n"
    "v_dot2_f32_bf16 v37, v173, v32, v37\n"
    "s_add_u32 s19, s19, 64\n"
    "v_dot2_f32_bf16 v41, v173, v173, v41\n"
    "v_add_u32 v79, s19, v0\n"
    "v_dot2_f32_bf16 v38, v174, v32, v38\n"
    "v_mul_u32_u24 v103, 6554, v79\n"
    "v_dot2_f32_bf16 v42, v174, v174, v42\n"
    "v_dot2_f32_bf16 v39, v175, v32, v39\n"
    "v_dot2_f32_bf16 v43, v175, v175, v43\n"
    "s_add_u32 m0, s68, 34816\n"
    "s_cmp_eq_u32 s11, 1\n"
    "s_cselect_b32 s87, 4294966144, 0\n"
    "s_add_u32 s88, s88, 128\n"
    "s_add_u32 s88, s88, s87\n"
    "s_waitcnt vmcnt(8) lgkmcnt(0)\n"
    "ds_read_b128 v[192:195], v48 offset:20480\n"
    "ds_read_b128 v[196:199], v49 offset:20480\n"
    "ds_read_b128 v[200:203], v50 offset:20480\n"
    "ds_read_b128 v[204:207], v51 offset:20480\n"
    "buffer_load_dwordx4 v104, s[40:43], s88 offen lds\n"
    "v_lshrrev_b32 v78, 16, v103\n"
    "v_dot2_f32_bf16 v36, v176, v32, v36\n"
    "v_mul_u32_u24 v103, 10, v78\n"
    "v_dot2_f32_bf16 v40, v176, v176, v40\n"
    "v_sub_u32 v65, v79, v103\n"
    "v_dot2_f32_bf16 v37, v177, v32, v37\n"
    "v_mul_u32_u24 v101, 36, v78\n"
    "v_dot2_f32_bf16 v41, v177, v177, v41\n"
    "v_lshl_add_u32 v102, v65, 2, v101\n"
    "v_dot2_f32_bf16 v38, v178, v32, v38\n"
    "v_cmp_gt_u32 vcc, 9, v65\n"
    "v_dot2_f32_bf16 v42, v178, v178, v42\n"
    "v_dot2_f32_bf16 v39, v179, v32, v39\n"
    "v_cndmask_b32 v103, 2.0, v102, vcc\n"
    "v_dot2_f32_bf16 v43, v179, v179, v43\n"
    "buffer_load_dwordx4 v105, s[40:43], s88 offen offset:1024 lds\n"
    "v_cmp_gt_u32 vcc, 36, v78\n"
    "v_dot2_f32_bf16 v36, v180, v32, v36\n"
    "v_dot2_f32_bf16 v40, v180, v180, v40\n"
    "v_cndmask_b32 v77, 2.0, v103, vcc\n"
    "v_lshlrev_b32 v103, 7, v1\n"
    "v_dot2_f32_bf16 v37, v181, v32, v37\n"
    "v_cmp_gt_u32 vcc, 27, v1\n"
    "v_dot2_f32_bf16 v41, v181, v181, v41\n"
    "v_dot2_f32_bf16 v38, v182, v32, v38\n"
    "v_cndmask_b32 v0, 2.0, v103, vcc\n"
    "v_dot2_f32_bf16 v42, v182, v182, v42\n"
    "v_dot2_f32_bf16 v39, v183, v32, v39\n"
    "v_dot2_f32_bf16 v43, v183, v183, v43\n"
    "buffer_load_dwordx4 v106, s[40:43], s88 offen offset:2048 lds\n"
    "v_dot2_f32_bf16 v36, v184, v32, v36\n"
    "v_dot2_f32_bf16 v40, v184, v184, v40\n"
    "v_dot2_f32_bf16 v37, v185, v32, v37\n"
    "v_dot2_f32_bf16 v41, v185, v185, v41\n"
    "v_dot2_f32_bf16 v38, v186, v32, v38\n"
    "v_dot2_f32_bf16 v42, v186, v186, v42\n"
    "v_dot2_f32_bf16 v39, v187, v32, v39\n"
    "v_dot2_f32_bf16 v43, v187, v187, v43\n"
    "buffer_load_dwordx4 v107, s[40:43], s88 offen offset:3072 lds\n"
    "v_dot2_f32_bf16 v36, v188, v32, v36\n"
    "v_dot2_f32_bf16 v40, v188, v188, v40\n"
    "v_dot2_f32_bf16 v37, v189, v32, v37\n"
    "v_dot2_f32_bf16 v41, v189, v189, v41\n"
    "v_dot2_f32_bf16 v38, v190, v32, v38\n"
    "v_dot2_f32_bf16 v42, v190, v190, v42\n"
    "v_dot2_f32_bf16 v39, v191, v32, v39\n"
    "v_dot2_f32_bf16 v43, v191, v191, v43\n"
    "s_lshl_b32 s19, s34, 9\n"
    "s_add_u32 m0, s19, 38912\n"
    "s_nop 0\n"
    "buffer_load_dword v76, s[64:67], s28 offen lds\n"
    "s_add_u32 m0, s19, 39168\n"
    "s_nop 0\n"
    "buffer_load_dword v77, s[64:67], s28 offen lds\n"
    "s_add_u32 m0, s19, 79872\n"
    "s_nop 0\n"
    "buffer_load_dword v76, s[64:67], s29 offen lds\n"
    "s_add_u32 m0, s19, 80128\n"
    "s_nop 0\n"
    "buffer_load_dword v77, s[64:67], s29 offen lds\n"
    "s_mov_b32 s64, s4\n"
    "s_mov_b32 s65, s5\n"
    "s_mov_b32 s66, 2359296\n"
    "buffer_load_dword v29, v15, s[60:63], 0 offen sc1\n"
    "s_waitcnt vmcnt(13) lgkmcnt(0)\n"
    "ds_read_b128 v[208:211], v48 offset:24576\n"
    "ds_read_b128 v[212:215], v49 offset:24576\n"
    "ds_read_b128 v[216:219], v50 offset:24576\n"
    "ds_read_b128 v[220:223], v51 offset:24576\n"
    "s_add_u32 m0, s68, 0\n"
    "s_nop 0\n"
    "buffer_load_dwordx4 v18, s[44:47], s69 offen lds\n"
    "v_dot2_f32_bf16 v36, v192, v32, v36\n"
    "v_dot2_f32_bf16 v40, v192, v192, v40\n"
    "v_dot2_f32_bf16 v37, v193, v32, v37\n"
    "v_dot2_f32_bf16 v41, v193, v193, v41\n"
    "v_dot2_f32_bf16 v38, v194, v32, v38\n"
    "v_dot2_f32_bf16 v42, v194, v194, v42\n"
    "v_dot2_f32_bf16 v39, v195, v32, v39\n"
    "v_dot2_f32_bf16 v43, v195, v195, v43\n"
    "v_dot2_f32_bf16 v36, v196, v32, v36\n"
    "v_dot2_f32_bf16 v40, v196, v196, v40\n"
    "v_dot2_f32_bf16 v37, v197, v32, v37\n"
    "v_dot2_f32_bf16 v41, v197, v197, v41\n"
    "v_dot2_f32_bf16 v38, v198, v32, v38\n"
    "v_dot2_f32_bf16 v42, v198, v198, v42\n"
    "v_dot2_f32_bf16 v39, v199, v32, v39\n"
    "v_dot2_f32_bf16 v43, v199, v199, v43\n"
    "s_add_u32 m0, s68, 1024\n"
    "s_nop 0\n"
    "buffer_load_dwordx4 v20, s[48:51], s72 offen lds\n"
    "v_dot2_f32_bf16 v36, v200, v32, v36\n"
    "v_dot2_f32_bf16 v40, v200, v200, v40\n"
    "v_dot2_f32_bf16 v37, v201, v32, v37\n"
    "v_dot2_f32_bf16 v41, v201, v201, v41\n"
    "v_dot2_f32_bf16 v38, v202, v32, v38\n"
    "v_dot2_f32_bf16 v42, v202, v202, v42\n"
    "v_dot2_f32_bf16 v39, v203, v32, v39\n"
    "v_dot2_f32_bf16 v43, v203, v203, v43\n"
    "v_dot2_f32_bf16 v36, v204, v32, v36\n"
    "v_dot2_f32_bf16 v40, v204, v204, v40\n"
    "v_dot2_f32_bf16 v37, v205, v32, v37\n"
    "v_dot2_f32_bf16 v41, v205, v205, v41\n"
    "v_dot2_f32_bf16 v38, v206, v32, v38\n"
    "v_dot2_f32_bf16 v42, v206, v206, v42\n"
    "v_dot2_f32_bf16 v39, v207, v32, v39\n"
    "v_dot2_f32_bf16 v43, v207, v207, v43\n"
    "s_waitcnt vmcnt(11) lgkmcnt(0)\n"
    "ds_read_b128 v[224:227], v48 offset:28672\n"
    "ds_read_b128 v[228:231], v49 offset:28672\n"
    "ds_read_b128 v[232:235], v50 offset:28672\n"
    "ds_read_b128 v[236:239], v51 offset:28672\n"
    "s_add_u32 s20, s69, 54272\n"
    "buffer_load_dwordx4 v18, s[44:47], s20 offen offset:1024 lds\n"
    "v_dot2_f32_bf16 v36, v208, v32, v36\n"
    "v_dot2_f32_bf16 v40, v208, v208, v40\n"
    "v_dot2_f32_bf16 v37, v209, v32, v37\n"
    "v_dot2_f32_bf16 v41, v209, v209, v41\n"
    "v_dot2_f32_bf16 v38, v210, v32, v38\n"
    "v_dot2_f32_bf16 v42, v210, v210, v42\n"
    "v_dot2_f32_bf16 v39, v211, v32, v39\n"
    "v_dot2_f32_bf16 v43, v211, v211, v43\n"
    "v_dot2_f32_bf16 v36, v212, v32, v36\n"
    "v_dot2_f32_bf16 v40, v212, v212, v40\n"
    "v_dot2_f32_bf16 v37, v213, v32, v37\n"
    "v_dot2_f32_bf16 v41, v213, v213, v41\n"
    "v_dot2_f32_bf16 v38, v214, v32, v38\n"
    "v_dot2_f32_bf16 v42, v214, v214, v42\n"
    "v_dot2_f32_bf16 v39, v215, v32, v39\n"
    "v_dot2_f32_bf16 v43, v215, v215, v43\n"
    "s_add_u32 s20, s69, 108544\n"
    "buffer_load_dwordx4 v18, s[44:47], s20 offen offset:2048 lds\n"
    "v_dot2_f32_bf16 v36, v216, v32, v36\n"
    "v_dot2_f32_bf16 v40, v216, v216, v40\n"
    "v_dot2_f32_bf16 v37, v217, v32, v37\n"
    "v_dot2_f32_bf16 v41, v217, v217, v41\n"
    "v_dot2_f32_bf16 v38, v218, v32, v38\n"
    "v_dot2_f32_bf16 v42, v218, v218, v42\n"
    "v_dot2_f32_bf16 v39, v219, v32, v39\n"
    "v_dot2_f32_bf16 v43, v219, v219, v43\n"
    "v_dot2_f32_bf16 v36, v220, v32, v36\n"
    "v_dot2_f32_bf16 v40, v220, v220, v40\n"
    "v_dot2_f32_bf16 v37, v221, v32, v37\n"
    "v_dot2_f32_bf16 v41, v221, v221, v41\n"
    "v_dot2_f32_bf16 v38, v222, v32, v38\n"
    "v_dot2_f32_bf16 v42, v222, v222, v42\n"
    "v_dot2_f32_bf16 v39, v223, v32, v39\n"
    "v_dot2_f32_bf16 v43, v223, v223, v43\n"
    "s_waitcnt vmcnt(9) lgkmcnt(0)\n"
    "ds_read_b128 v[240:243], v48 offset:32768\n"
    "ds_read_b128 v[244:247], v49 offset:32768\n"
    "ds_read_b128 v[248:251], v50 offset:32768\n"
    "ds_read_b128 v[252:255], v51 offset:32768\n"
    "s_add_u32 s20, s69, 162816\n"
    "buffer_load_dwordx4 v18, s[44:47], s20 offen offset:3072 lds\n"
    "v_dot2_f32_bf16 v36, v224, v32, v36\n"
    "v_dot2_f32_bf16 v40, v224, v224, v40\n"
    "v_dot2_f32_bf16 v37, v225, v32, v37\n"
    "v_dot2_f32_bf16 v41, v225, v225, v41\n"
    "v_dot2_f32_bf16 v38, v226, v32, v38\n"
    "v_dot2_f32_bf16 v42, v226, v226, v42\n"
    "v_dot2_f32_bf16 v39, v227, v32, v39\n"
    "v_dot2_f32_bf16 v43, v227, v227, v43\n"
    "v_dot2_f32_bf16 v36, v228, v32, v36\n"
    "v_dot2_f32_bf16 v40, v228, v228, v40\n"
    "v_dot2_f32_bf16 v37, v229, v32, v37\n"
    "v_dot2_f32_bf16 v41, v229, v229, v41\n"
    "v_dot2_f32_bf16 v38, v230, v32, v38\n"
    "v_dot2_f32_bf16 v42, v230, v230, v42\n"
    "v_dot2_f32_bf16 v39, v231, v32, v39\n"
    "v_dot2_f32_bf16 v43, v231, v231, v43\n"
    "s_cmp_eq_u32 s11, 8\n"
    "s_cselect_b32 s20, 4293197824, s98\n"
    "s_add_u32 s69, s69, s20\n"
    "s_add_u32 m0, s68, 5120\n"
    "s_nop 0\n"
    "buffer_load_dwordx4 v18, s[44:47], s69 offen lds\n"
    "v_dot2_f32_bf16 v36, v232, v32, v36\n"
    "v_dot2_f32_bf16 v40, v232, v232, v40\n"
    "v_dot2_f32_bf16 v37, v233, v32, v37\n"
    "v_dot2_f32_bf16 v41, v233, v233, v41\n"
    "v_dot2_f32_bf16 v38, v234, v32, v38\n"
    "v_dot2_f32_bf16 v42, v234, v234, v42\n"
    "v_dot2_f32_bf16 v39, v235, v32, v39\n"
    "v_dot2_f32_bf16 v43, v235, v235, v43\n"
    "v_dot2_f32_bf16 v36, v236, v32, v36\n"
    "v_dot2_f32_bf16 v40, v236, v236, v40\n"
    "v_dot2_f32_bf16 v37, v237, v32, v37\n"
    "v_dot2_f32_bf16 v41, v237, v237, v41\n"
    "v_dot2_f32_bf16 v38, v238, v32, v38\n"
    "v_dot2_f32_bf16 v42, v238, v238, v42\n"
    "v_dot2_f32_bf16 v39, v239, v32, v39\n"
    "v_dot2_f32_bf16 v43, v239, v239, v43\n"
    "s_waitcnt lgkmcnt(0)\n"
    "v_dot2_f32_bf16 v36, v240, v32, v36\n"
    "v_dot2_f32_bf16 v40, v240, v240, v40\n"
    "v_dot2_f32_bf16 v37, v241, v32, v37\n"
    "v_dot2_f32_bf16 v41, v241, v241, v41\n"
    "v_dot2_f32_bf16 v38, v242, v32, v38\n"
    "v_dot2_f32_bf16 v42, v242, v242, v42\n"
    "v_dot2_f32_bf16 v39, v243, v32, v39\n"
    "v_dot2_f32_bf16 v43, v243, v243, v43\n"
    "v_dot2_f32_bf16 v36, v244, v32, v36\n"
    "v_dot2_f32_bf16 v40, v244, v244, v40\n"
    "v_dot2_f32_bf16 v37, v245, v32, v37\n"
    "v_dot2_f32_bf16 v41, v245, v245, v41\n"
    "v_dot2_f32_bf16 v38, v246, v32, v38\n"
    "v_dot2_f32_bf16 v42, v246, v246, v42\n"
    "v_dot2_f32_bf16 v39, v247, v32, v39\n"
    "v_dot2_f32_bf16 v43, v247, v247, v43\n"
    "v_dot2_f32_bf16 v36, v248, v32, v36\n"
    "v_dot2_f32_bf16 v40, v248, v248, v40\n"
    "v_dot2_f32_bf16 v37, v249, v32, v37\n"
    "v_dot2_f32_bf16 v41, v249, v249, v41\n"
    "v_dot2_f32_bf16 v38, v250, v32, v38\n"
    "v_dot2_f32_bf16 v42, v250, v250, v42\n"
    "v_dot2_f32_bf16 v39, v251, v32, v39\n"
    "v_dot2_f32_bf16 v43, v251, v251, v43\n"
    "v_dot2_f32_bf16 v36, v252, v32, v36\n"
    "v_dot2_f32_bf16 v40, v252, v252, v40\n"
    "v_dot2_f32_bf16 v37, v253, v32, v37\n"
    "v_dot2_f32_bf16 v41, v253, v253, v41\n"
    "v_dot2_f32_bf16 v38, v254, v32, v38\n"
    "v_dot2_f32_bf16 v42, v254, v254, v42\n"
    "v_dot2_f32_bf16 v39, v255, v32, v39\n"
    "v_dot2_f32_bf16 v43, v255, v255, v43\n"
    "s_nop 1\n"
    "v_pk_add_f32 v[36:37], v[36:37], v[38:39]\n"
    "v_pk_add_f32 v[40:41], v[40:41], v[42:43]\n"
    "s_cmp_eq_u32 s11, 8\n"
    "s_cselect_b32 s20, 4290248704, s81\n"
    "s_add_u32 s72, s72, s20\n"
    "s_add_u32 m0, s68, 6144\n"
    "s_nop 0\n"
    "buffer_load_dwordx4 v20, s[48:51], s72 offen lds\n"
    "v_add_f32 v36, v36, v37\n"
    "v_add_f32 v38, v40, v41\n"
    "v_mov_b32 v37, v36\n"
    "v_mov_b32 v39, v38\n"
    "s_nop 0\n"
    "v_permlane32_swap_b32 v36, v37\n"
    "v_permlane32_swap_b32 v38, v39\n"
    "v_add_f32 v36, v36, v37\n"
    "v_add_f32 v38, v38, v39\n"
    "v_mul_f32 v36, 987991609, v36\n"
    "v_mul_f32 v38, 987991609, v38\n"
    "v_fma_f32 v38, -v36, v36, v38\n"
    "v_max_f32 v38, 0, v38\n"
    "v_cvt_pk_bf16_f32 v38, 0, v38\n"
    "v_cvt_pk_bf16_f32 v36, 0, v36\n"
    "v_add_f32 v38, 925353388, v38\n"
    "v_cvt_pk_bf16_f32 v38, 0, v38\n"
    "v_sqrt_f32 v38, v38\n"
    "s_nop 0\n"
    "v_cvt_pk_bf16_f32 v38, 0, v38\n"
    "v_rcp_f32 v38, v38\n"
    "s_nop 0\n"
    "v_mul_f32 v30, 3216550459, v38\n"
    "v_mul_f32 v31, v36, v30\n"
    "v_sub_f32 v31, 0, v31\n"
    "v_mov_b32 v32, v30\n"
    "v_mov_b32 v33, v30\n"
    "v_mov_b32 v34, v31\n"
    "v_mov_b32 v35, v31\n"
    "s_add_u32 s20, s69, 54272\n"
    "buffer_load_dwordx4 v18, s[44:47], s20 offen offset:1024 lds\n"
    "v_lshlrev_b32 v64, 16, v112\n"
    "v_and_b32 v65, s25, v112\n"
    "v_lshlrev_b32 v66, 16, v113\n"
    "v_and_b32 v67, s25, v113\n"
    "v_lshlrev_b32 v68, 16, v114\n"
    "v_and_b32 v69, s25, v114\n"
    "v_lshlrev_b32 v70, 16, v115\n"
    "v_and_b32 v71, s25, v115\n"
    "v_pk_fma_f32 v[64:65], v[64:65], v[32:33], v[34:35]\n"
    "v_pk_fma_f32 v[66:67], v[66:67], v[32:33], v[34:35]\n"
    "v_pk_fma_f32 v[68:69], v[68:69], v[32:33], v[34:35]\n"
    "v_pk_fma_f32 v[70:71], v[70:71], v[32:33], v[34:35]\n"
    "v_lshlrev_b32 v72, 16, v116\n"
    "v_and_b32 v73, s25, v116\n"
    "v_lshlrev_b32 v74, 16, v117\n"
    "v_and_b32 v75, s25, v117\n"
    "v_lshlrev_b32 v76, 16, v118\n"
    "v_and_b32 v77, s25, v118\n"
    "v_lshlrev_b32 v78, 16, v119\n"
    "v_and_b32 v79, s25, v119\n"
    "v_pk_fma_f32 v[72:73], v[72:73], v[32:33], v[34:35]\n"
    "v_pk_fma_f32 v[74:75], v[74:75], v[32:33], v[34:35]\n"
    "v_pk_fma_f32 v[76:77], v[76:77], v[32:33], v[34:35]\n"
    "v_pk_fma_f32 v[78:79], v[78:79], v[32:33], v[34:35]\n"
    "v_cvt_pk_bf16_f32 v112, v64, v65\n"
    "v_cvt_pk_bf16_f32 v113, v66, v67\n"
    "v_cvt_pk_bf16_f32 v114, v68, v69\n"
    "v_cvt_pk_bf16_f32 v115, v70, v71\n"
    "v_lshlrev_b32 v64, 16, v120\n"
    "v_and_b32 v65, s25, v120\n"
    "v_lshlrev_b32 v66, 16, v121\n"
    "v_and_b32 v67, s25, v121\n"
    "v_lshlrev_b32 v68, 16, v122\n"
    "v_and_b32 v69, s25, v122\n"
    "v_lshlrev_b32 v70, 16, v123\n"
    "v_and_b32 v71, s25, v123\n"
    "v_pk_fma_f32 v[64:65], v[64:65], v[32:33], v[34:35]\n"
    "v_pk_fma_f32 v[66:67], v[66:67], v[32:33], v[34:35]\n"
    "v_pk_fma_f32 v[68:69], v[68:69], v[32:33], v[34:35]\n"
    "v_pk_fma_f32 v[70:71], v[70:71], v[32:33], v[34:35]\n"
    "v_cvt_pk_bf16_f32 v116, v72, v73\n"
    "v_cvt_pk_bf16_f32 v117, v74, v75\n"
    "v_cvt_pk_bf16_f32 v118, v76, v77\n"
    "v_cvt_pk_bf16_f32 v119, v78, v79\n"
    "s_add_u32 s20, s69, 108544\n"
    "buffer_load_dwordx4 v18, s[44:47], s20 offen offset:2048 lds\n"
    "v_lshlrev_b32 v72, 16, v124\n"
    "v_and_b32 v73, s25, v124\n"
    "v_lshlrev_b32 v74, 16, v125\n"
    "v_and_b32 v75, s25, v125\n"
    "v_lshlrev_b32 v76, 16, v126\n"
    "v_and_b32 v77, s25, v126\n"
    "v_lshlrev_b32 v78, 16, v127\n"
    "v_and_b32 v79, s25, v127\n"
    "v_pk_fma_f32 v[72:73], v[72:73], v[32:33], v[34:35]\n"
    "v_pk_fma_f32 v[74:75], v[74:75], v[32:33], v[34:35]\n"
    "v_pk_fma_f32 v[76:77], v[76:77], v[32:33], v[34:35]\n"
    "v_pk_fma_f32 v[78:79], v[78:79], v[32:33], v[34:35]\n"
    "v_cvt_pk_bf16_f32 v120, v64, v65\n"
    "v_cvt_pk_bf16_f32 v121, v66, v67\n"
    "v_cvt_pk_bf16_f32 v122, v68, v69\n"
    "v_cvt_pk_bf16_f32 v123, v70, v71\n"
    "v_lshlrev_b32 v64, 16, v128\n"
    "v_and_b32 v65, s25, v128\n"
    "v_lshlrev_b32 v66, 16, v129\n"
    "v_and_b32 v67, s25, v129\n"
    "v_lshlrev_b32 v68, 16, v130\n"
    "v_and_b32 v69, s25, v130\n"
    "v_lshlrev_b32 v70, 16, v131\n"
    "v_and_b32 v71, s25, v131\n"
    "v_pk_fma_f32 v[64:65], v[64:65], v[32:33], v[34:35]\n"
    "v_pk_fma_f32 v[66:67], v[66:67], v[32:33], v[34:35]\n"
    "v_pk_fma_f32 v[68:69], v[68:69], v[32:33], v[34:35]\n"
    "v_pk_fma_f32 v[70:71], v[70:71], v[32:33], v[34:35]\n"
    "v_cvt_pk_bf16_f32 v124, v72, v73\n"
    "v_cvt_pk_bf16_f32 v125, v74, v75\n"
    "v_cvt_pk_bf16_f32 v126, v76, v77\n"
    "v_cvt_pk_bf16_f32 v127, v78, v79\n"
    "v_lshlrev_b32 v72, 16, v132\n"
    "v_and_b32 v73, s25, v132\n"
    "v_lshlrev_b32 v74, 16, v133\n"
    "v_and_b32 v75, s25, v133\n"
    "v_lshlrev_b32 v76, 16, v134\n"
    "v_and_b32 v77, s25, v134\n"
    "v_lshlrev_b32 v78, 16, v135\n"
    "v_and_b32 v79, s25, v135\n"
    "v_pk_fma_f32 v[72:73], v[72:73], v[32:33], v[34:35]\n"
    "v_pk_fma_f32 v[74:75], v[74:75], v[32:33], v[34:35]\n"
    "v_pk_fma_f32 v[76:77], v[76:77], v[32:33], v[34:35]\n"
    "v_pk_fma_f32 v[78:79], v[78:79], v[32:33], v[34:35]\n"
    "v_cvt_pk_bf16_f32 v128, v64, v65\n"
    "v_cvt_pk_bf16_f32 v129, v66, v67\n"
    "v_cvt_pk_bf16_f32 v130, v68, v69\n"
    "v_cvt_pk_bf16_f32 v131, v70, v71\n"
    "s_add_u32 s20, s69, 162816\n"
    "buffer_load_dwordx4 v18, s[44:47], s20 offen offset:3072 lds\n"
    "v_lshlrev_b32 v64, 16, v136\n"
    "v_and_b32 v65, s25, v136\n"
    "v_lshlrev_b32 v66, 16, v137\n"
    "v_and_b32 v67, s25, v137\n"
    "v_lshlrev_b32 v68, 16, v138\n"
    "v_and_b32 v69, s25, v138\n"
    "v_lshlrev_b32 v70, 16, v139\n"
    "v_and_b32 v71, s25, v139\n"
    "v_pk_fma_f32 v[64:65], v[64:65], v[32:33], v[34:35]\n"
    "v_pk_fma_f32 v[66:67], v[66:67], v[32:33], v[34:35]\n"
    "v_pk_fma_f32 v[68:69], v[68:69], v[32:33], v[34:35]\n"
    "v_pk_fma_f32 v[70:71], v[70:71], v[32:33], v[34:35]\n"
    "v_cvt_pk_bf16_f32 v132, v72, v73\n"
    "v_cvt_pk_bf16_f32 v133, v74, v75\n"
    "v_cvt_pk_bf16_f32 v134, v76, v77\n"
    "v_cvt_pk_bf16_f32 v135, v78, v79\n"
    "v_lshlrev_b32 v72, 16, v140\n"
    "v_and_b32 v73, s25, v140\n"
    "v_lshlrev_b32 v74, 16, v141\n"
    "v_and_b32 v75, s25, v141\n"
    "v_lshlrev_b32 v76, 16, v142\n"
    "v_and_b32 v77, s25, v142\n"
    "v_lshlrev_b32 v78, 16, v143\n"
    "v_and_b32 v79, s25, v143\n"
    "v_pk_fma_f32 v[72:73], v[72:73], v[32:33], v[34:35]\n"
    "v_pk_fma_f32 v[74:75], v[74:75], v[32:33], v[34:35]\n"
    "v_pk_fma_f32 v[76:77], v[76:77], v[32:33], v[34:35]\n"
    "v_pk_fma_f32 v[78:79], v[78:79], v[32:33], v[34:35]\n"
    "v_cvt_pk_bf16_f32 v136, v64, v65\n"
    "v_cvt_pk_bf16_f32 v137, v66, v67\n"
    "v_cvt_pk_bf16_f32 v138, v68, v69\n"
    "v_cvt_pk_bf16_f32 v139, v70, v71\n"
    "v_lshlrev_b32 v64, 16, v144\n"
    "v_and_b32 v65, s25, v144\n"
    "v_lshlrev_b32 v66, 16, v145\n"
    "v_and_b32 v67, s25, v145\n"
    "v_lshlrev_b32 v68, 16, v146\n"
    "v_and_b32 v69, s25, v146\n"
    "v_lshlrev_b32 v70, 16, v147\n"
    "v_and_b32 v71, s25, v147\n"
    "v_pk_fma_f32 v[64:65], v[64:65], v[32:33], v[34:35]\n"
    "v_pk_fma_f32 v[66:67], v[66:67], v[32:33], v[34:35]\n"
    "v_pk_fma_f32 v[68:69], v[68:69], v[32:33], v[34:35]\n"
    "v_pk_fma_f32 v[70:71], v[70:71], v[32:33], v[34:35]\n"
    "v_cvt_pk_bf16_f32 v140, v72, v73\n"
    "v_cvt_pk_bf16_f32 v141, v74, v75\n"
    "v_cvt_pk_bf16_f32 v142, v76, v77\n"
    "v_cvt_pk_bf16_f32 v143, v78, v79\n"
    "s_cmp_eq_u32 s11, 7\n"
    "s_cselect_b32 s20, 4293197824, s98\n"
    "s_add_u32 s69, s69, s20\n"
    "s_add_u32 m0, s68, 10240\n"
    "s_nop 0\n"
    "buffer_load_dwordx4 v18, s[44:47], s69 offen lds\n"
    "v_lshlrev_b32 v72, 16, v148\n"
    "v_and_b32 v73, s25, v148\n"
    "v_lshlrev_b32 v74, 16, v149\n"
    "v_and_b32 v75, s25, v149\n"
    "v_lshlrev_b32 v76, 16, v150\n"
    "v_and_b32 v77, s25, v150\n"
    "v_lshlrev_b32 v78, 16, v151\n"
    "v_and_b32 v79, s25, v151\n"
    "v_pk_fma_f32 v[72:73], v[72:73], v[32:33], v[34:35]\n"
    "v_pk_fma_f32 v[74:75], v[74:75], v[32:33], v[34:35]\n"
    "v_pk_fma_f32 v[76:77], v[76:77], v[32:33], v[34:35]\n"
    "v_pk_fma_f32 v[78:79], v[78:79], v[32:33], v[34:35]\n"
    "v_cvt_pk_bf16_f32 v144, v64, v65\n"
    "v_cvt_pk_bf16_f32 v145, v66, v67\n"
    "v_cvt_pk_bf16_f32 v146, v68, v69\n"
    "v_cvt_pk_bf16_f32 v147, v70, v71\n"
    "v_lshlrev_b32 v64, 16, v152\n"
    "v_and_b32 v65, s25, v152\n"
    "v_lshlrev_b32 v66, 16, v153\n"
    "v_and_b32 v67, s25, v153\n"
    "v_lshlrev_b32 v68, 16, v154\n"
    "v_and_b32 v69, s25, v154\n"
    "v_lshlrev_b32 v70, 16, v155\n"
    "v_and_b32 v71, s25, v155\n"
    "v_pk_fma_f32 v[64:65], v[64:65], v[32:33], v[34:35]\n"
    "v_pk_fma_f32 v[66:67], v[66:67], v[32:33], v[34:35]\n"
    "v_pk_fma_f32 v[68:69], v[68:69], v[32:33], v[34:35]\n"
    "v_pk_fma_f32 v[70:71], v[70:71], v[32:33], v[34:35]\n"
    "v_cvt_pk_bf16_f32 v148, v72, v73\n"
    "v_cvt_pk_bf16_f32 v149, v74, v75\n"
    "v_cvt_pk_bf16_f32 v150, v76, v77\n"
    "v_cvt_pk_bf16_f32 v151, v78, v79\n"
    "v_lshlrev_b32 v72, 16, v156\n"
    "v_and_b32 v73, s25, v156\n"
    "v_lshlrev_b32 v74, 16, v157\n"
    "v_and_b32 v75, s25, v157\n"
    "v_lshlrev_b32 v76, 16, v158\n"
    "v_and_b32 v77, s25, v158\n"
    "v_lshlrev_b32 v78, 16, v159\n"
    "v_and_b32 v79, s25, v159\n"
    "v_pk_fma_f32 v[72:73], v[72:73], v[32:33], v[34:35]\n"
    "v_pk_fma_f32 v[74:75], v[74:75], v[32:33], v[34:35]\n"
    "v_pk_fma_f32 v[76:77], v[76:77], v[32:33], v[34:35]\n"
    "v_pk_fma_f32 v[78:79], v[78:79], v[32:33], v[34:35]\n"
    "v_cvt_pk_bf16_f32 v152, v64, v65\n"
    "v_cvt_pk_bf16_f32 v153, v66, v67\n"
    "v_cvt_pk_bf16_f32 v154, v68, v69\n"
    "v_cvt_pk_bf16_f32 v155, v70, v71\n"
    "s_cmp_eq_u32 s11, 7\n"
    "s_cselect_b32 s20, 4290248704, s81\n"
    "s_add_u32 s72, s72, s20\n"
    "s_add_u32 m0, s68, 11264\n"
    "s_nop 0\n"
    "buffer_load_dwordx4 v20, s[48:51], s72 offen lds\n"
    "v_lshlrev_b32 v64, 16, v160\n"
    "v_and_b32 v65, s25, v160\n"
    "v_lshlrev_b32 v66, 16, v161\n"
    "v_and_b32 v67, s25, v161\n"
    "v_lshlrev_b32 v68, 16, v162\n"
    "v_and_b32 v69, s25, v162\n"
    "v_lshlrev_b32 v70, 16, v163\n"
    "v_and_b32 v71, s25, v163\n"
    "v_pk_fma_f32 v[64:65], v[64:65], v[32:33], v[34:35]\n"
    "v_pk_fma_f32 v[66:67], v[66:67], v[32:33], v[34:35]\n"
    "v_pk_fma_f32 v[68:69], v[68:69], v[32:33], v[34:35]\n"
    "v_pk_fma_f32 v[70:71], v[70:71], v[32:33], v[34:35]\n"
    "v_cvt_pk_bf16_f32 v156, v72, v73\n"
    "v_cvt_pk_bf16_f32 v157, v74, v75\n"
    "v_cvt_pk_bf16_f32 v158, v76, v77\n"
    "v_cvt_pk_bf16_f32 v159, v78, v79\n"
    "s_waitcnt vmcnt(7)\n"
    "s_barrier\n"
    "ds_read_b64_tr_b16 a[80:81], v52\n"
    "ds_read_b64_tr_b16 a[82:83], v53\n"
    "ds_read_b64_tr_b16 a[84:85], v54\n"
    "ds_read_b64_tr_b16 a[86:87], v55\n"
    "ds_read_b64_tr_b16 a[88:89], v56\n"
    "ds_read_b64_tr_b16 a[90:91], v57\n"
    "ds_read_b64_tr_b16 a[92:93], v58\n"
    "ds_read_b64_tr_b16 a[94:95], v59\n"
    "ds_read_b64_tr_b16 a[96:97], v60\n"
    "ds_read_b64_tr_b16 a[98:99], v61\n"
    "ds_read_b64_tr_b16 a[100:101], v62\n"
    "ds_read_b64_tr_b16 a[102:103], v63\n"
    "ds_read_b64_tr_b16 a[220:221], v52 offset:40960\n"
    "ds_read_b64_tr_b16 a[222:223], v53 offset:40960\n"
    "ds_read_b64_tr_b16 a[224:225], v54 offset:40960\n"
    "ds_read_b64_tr_b16 a[226:227], v55 offset:40960\n"
    "ds_read_b64_tr_b16 a[228:229], v56 offset:40960\n"
    "ds_read_b64_tr_b16 a[230:231], v57 offset:40960\n"
    "ds_read_b64_tr_b16 a[232:233], v58 offset:40960\n"
    "ds_read_b64_tr_b16 a[234:235], v59 offset:40960\n"
    "ds_read_b64_tr_b16 a[236:237], v60 offset:40960\n"
    "ds_read_b64_tr_b16 a[238:239], v61 offset:40960\n"
    "ds_read_b64_tr_b16 a[240:241], v62 offset:40960\n"
    "ds_read_b64_tr_b16 a[242:243], v63 offset:40960\n"
    "ds_read_b64_tr_b16 a[104:105], v16\n"
    "ds_read_b64_tr_b16 a[106:107], v17\n"
    "ds_read_b64_tr_b16 a[108:109], v16 offset:256\n"
    "ds_read_b64_tr_b16 a[110:111], v17 offset:256\n"
    "ds_read_b64_tr_b16 a[112:113], v16 offset:512\n"
    "ds_read_b64_tr_b16 a[114:115], v17 offset:512\n"
    "ds_read_b64_tr_b16 a[116:117], v16 offset:768\n"
    "ds_read_b64_tr_b16 a[118:119], v17 offset:768\n"
    "ds_read_b64_tr_b16 a[120:121], v16 offset:1024\n"
    "ds_read_b64_tr_b16 a[122:123], v17 offset:1024\n"
    "ds_read_b64_tr_b16 a[124:125], v16 offset:2048\n"
    "ds_read_b64_tr_b16 a[126:127], v17 offset:2048\n"
    "ds_read_b64_tr_b16 a[128:129], v16 offset:2304\n"
    "ds_read_b64_tr_b16 a[130:131], v17 offset:2304\n"
    "v_lshlrev_b32 v72, 16, v164\n"
    "v_and_b32 v73, s25, v164\n"
    "v_lshlrev_b32 v74, 16, v165\n"
    "v_and_b32 v75, s25, v165\n"
    "v_lshlrev_b32 v76, 16, v166\n"
    "v_and_b32 v77, s25, v166\n"
    "v_lshlrev_b32 v78, 16, v167\n"
    "v_and_b32 v79, s25, v167\n"
    "v_pk_fma_f32 v[72:73], v[72:73], v[32:33], v[34:35]\n"
    "v_pk_fma_f32 v[74:75], v[74:75], v[32:33], v[34:35]\n"
    "v_pk_fma_f32 v[76:77], v[76:77], v[32:33], v[34:35]\n"
    "v_pk_fma_f32 v[78:79], v[78:79], v[32:33], v[34:35]\n"
    "v_cvt_pk_bf16_f32 v160, v64, v65\n"
    "v_cvt_pk_bf16_f32 v161, v66, v67\n"
    "v_cvt_pk_bf16_f32 v162, v68, v69\n"
    "v_cvt_pk_bf16_f32 v163, v70, v71\n"
    "v_lshlrev_b32 v64, 16, v168\n"
    "v_and_b32 v65, s25, v168\n"
    "v_lshlrev_b32 v66, 16, v169\n"
    "v_and_b32 v67, s25, v169\n"
    "v_lshlrev_b32 v68, 16, v170\n"
    "v_and_b32 v69, s25, v170\n"
    "v_lshlrev_b32 v70, 16, v171\n"
    "v_and_b32 v71, s25, v171\n"
    "v_pk_fma_f32 v[64:65], v[64:65], v[32:33], v[34:35]\n"
    "v_pk_fma_f32 v[66:67], v[66:67], v[32:33], v[34:35]\n"
    "v_pk_fma_f32 v[68:69], v[68:69], v[32:33], v[34:35]\n"
    "v_pk_fma_f32 v[70:71], v[70:71], v[32:33], v[34:35]\n"
    "v_cvt_pk_bf16_f32 v164, v72, v73\n"
    "v_cvt_pk_bf16_f32 v165, v74, v75\n"
    "v_cvt_pk_bf16_f32 v166, v76, v77\n"
    "v_cvt_pk_bf16_f32 v167, v78, v79\n"
    "s_add_u32 s20, s69, 54272\n"
    "buffer_load_dwordx4 v18, s[44:47], s20 offen offset:1024 lds\n"
    "v_lshlrev_b32 v72, 16, v172\n"
    "v_and_b32 v73, s25, v172\n"
    "v_lshlrev_b32 v74, 16, v173\n"
    "v_and_b32 v75, s25, v173\n"
    "v_lshlrev_b32 v76, 16, v174\n"
    "v_and_b32 v77, s25, v174\n"
    "v_lshlrev_b32 v78, 16, v175\n"
    "v_and_b32 v79, s25, v175\n"
    "v_pk_fma_f32 v[72:73], v[72:73], v[32:33], v[34:35]\n"
    "v_pk_fma_f32 v[74:75], v[74:75], v[32:33], v[34:35]\n"
    "v_pk_fma_f32 v[76:77], v[76:77], v[32:33], v[34:35]\n"
    "v_pk_fma_f32 v[78:79], v[78:79], v[32:33], v[34:35]\n"
    "v_cvt_pk_bf16_f32 v168, v64, v65\n"
    "v_cvt_pk_bf16_f32 v169, v66, v67\n"
    "v_cvt_pk_bf16_f32 v170, v68, v69\n"
    "v_cvt_pk_bf16_f32 v171, v70, v71\n"
    "v_lshlrev_b32 v64, 16, v176\n"
    "v_and_b32 v65, s25, v176\n"
    "v_lshlrev_b32 v66, 16, v177\n"
    "v_and_b32 v67, s25, v177\n"
    "v_lshlrev_b32 v68, 16, v178\n"
    "v_and_b32 v69, s25, v178\n"
    "v_lshlrev_b32 v70, 16, v179\n"
    "v_and_b32 v71, s25, v179\n"
    "v_pk_fma_f32 v[64:65], v[64:65], v[32:33], v[34:35]\n"
    "v_pk_fma_f32 v[66:67], v[66:67], v[32:33], v[34:35]\n"
    "v_pk_fma_f32 v[68:69], v[68:69], v[32:33], v[34:35]\n"
    "v_pk_fma_f32 v[70:71], v[70:71], v[32:33], v[34:35]\n"
    "v_cvt_pk_bf16_f32 v172, v72, v73\n"
    "v_cvt_pk_bf16_f32 v173, v74, v75\n"
    "v_cvt_pk_bf16_f32 v174, v76, v77\n"
    "v_cvt_pk_bf16_f32 v175, v78, v79\n"
    "v_lshlrev_b32 v72, 16, v180\n"
    "v_and_b32 v73, s25, v180\n"
    "v_lshlrev_b32 v74, 16, v181\n"
    "v_and_b32 v75, s25, v181\n"
    "v_lshlrev_b32 v76, 16, v182\n"
    "v_and_b32 v77, s25, v182\n"
    "v_lshlrev_b32 v78, 16, v183\n"
    "v_and_b32 v79, s25, v183\n"
    "v_pk_fma_f32 v[72:73], v[72:73], v[32:33], v[34:35]\n"
    "v_pk_fma_f32 v[74:75], v[74:75], v[32:33], v[34:35]\n"
    "v_pk_fma_f32 v[76:77], v[76:77], v[32:33], v[34:35]\n"
    "v_pk_fma_f32 v[78:79], v[78:79], v[32:33], v[34:35]\n"
    "v_cvt_pk_bf16_f32 v176, v64, v65\n"
    "v_cvt_pk_bf16_f32 v177, v66, v67\n"
    "v_cvt_pk_bf16_f32 v178, v68, v69\n"
    "v_cvt_pk_bf16_f32 v179, v70, v71\n"
    "s_add_u32 s20, s69, 108544\n"
    "buffer_load_dwordx4 v18, s[44:47], s20 offen offset:2048 lds\n"
    "s_add_u32 s20, s69, 162816\n"
    "buffer_load_dwordx4 v18, s[44:47], s20 offen offset:3072 lds\n"
    "s_cmp_eq_u32 s11, 6\n"
    "s_cselect_b32 s20, 4293197824, s98\n"
    "s_add_u32 s69, s69, s20\n"
    "s_add_u32 m0, s68, 15360\n"
    "s_nop 0\n"
    "buffer_load_dwordx4 v18, s[44:47], s69 offen lds\n"
    "v_cvt_pk_bf16_f32 v180, v72, v73\n"
    "v_cvt_pk_bf16_f32 v181, v74, v75\n"
    "v_cvt_pk_bf16_f32 v182, v76, v77\n"
    "v_cvt_pk_bf16_f32 v183, v78, v79\n"
    "s_waitcnt lgkmcnt(8)\n"
    "v_mfma_f32_32x32x16_bf16 v[48:63], a[104:107], v[112:115], 0\n"
    "s_lshl2_add_u32 s0, s35, s39\n"
    "ds_read_b64_tr_b16 a[132:133], v16 offset:2560\n"
    "ds_read_b64_tr_b16 a[134:135], v17 offset:2560\n"
    "v_lshlrev_b32 v2, 16, v184\n"
    "v_and_b32 v3, s25, v184\n"
    "v_lshlrev_b32 v4, 16, v185\n"
    "v_and_b32 v5, s25, v185\n"
    "v_mfma_f32_32x32x16_bf16 v[64:79], a[108:111], v[112:115], 0\n"
    "s_lshl2_add_u32 s7, s0, s34\n"
    "ds_read_b64_tr_b16 a[136:137], v16 offset:2816\n"
    "ds_read_b64_tr_b16 a[138:139], v17 offset:2816\n"
    "v_lshlrev_b32 v6, 16, v186\n"
    "v_and_b32 v7, s25, v186\n"
    "v_lshlrev_b32 v8, 16, v187\n"
    "v_and_b32 v9, s25, v187\n"
    "v_mfma_f32_32x32x16_bf16 v[80:95], a[112:115], v[112:115], 0\n"
    "s_waitcnt lgkmcnt(8)\n"
    "ds_read_b64_tr_b16 a[140:141], v16 offset:1280\n"
    "ds_read_b64_tr_b16 a[142:143], v17 offset:1280\n"
    "v_fma_f32 v2, v2, v30, v31\n"
    "v_fma_f32 v3, v3, v30, v31\n"
    "v_fma_f32 v4, v4, v30, v31\n"
    "v_fma_f32 v5, v5, v30, v31\n"
    "v_mfma_f32_32x32x16_bf16 v[96:111], a[116:119], v[112:115], 0\n"
    "s_lshl_b32 s7, s7, 12\n"
    "ds_read_b64_tr_b16 a[104:105], v16 offset:3072\n"
    "ds_read_b64_tr_b16 a[106:107], v17 offset:3072\n"
    "v_fma_f32 v6, v6, v30, v31\n"
    "v_fma_f32 v7, v7, v30, v31\n"
    "v_fma_f32 v8, v8, v30, v31\n"
    "v_fma_f32 v9, v9, v30, v31\n"
    "v_mfma_f32_32x32x16_bf16 v[32:47], a[120:123], v[112:115], 0\n"
    "s_waitcnt lgkmcnt(6)\n"
    "s_cmp_eq_u32 s11, 6\n"
    "s_cselect_b32 s20, 4290248704, s81\n"
    "s_add_u32 s72, s72, s20\n"
    "s_add_u32 m0, s68, 16384\n"
    "ds_read_b64_tr_b16 a[108:109], v16 offset:3328\n"
    "ds_read_b64_tr_b16 a[110:111], v17 offset:3328\n"
    "v_mfma_f32_32x32x16_bf16 v[48:63], a[124:127], v[116:119], v[48:63]\n"
    "s_add_u32 s7, s7, 4096\n"
    "ds_read_b64_tr_b16 a[112:113], v16 offset:3584\n"
    "ds_read_b64_tr_b16 a[114:115], v17 offset:3584\n"
    "buffer_load_dwordx4 v20, s[48:51], s72 offen lds\n"
    "v_cvt_pk_bf16_f32 v184, v2, v3\n"
    "v_cvt_pk_bf16_f32 v185, v4, v5\n"
    "v_mfma_f32_32x32x16_bf16 v[64:79], a[128:131], v[116:119], v[64:79]\n"
    "s_lshl_b32 s6, s3, 7\n"
    "ds_read_b64_tr_b16 a[116:117], v16 offset:3840\n"
    "ds_read_b64_tr_b16 a[118:119], v17 offset:3840\n"
    "v_cvt_pk_bf16_f32 v186, v6, v7\n"
    "v_cvt_pk_bf16_f32 v187, v8, v9\n"
    "v_lshlrev_b32 v2, 16, v188\n"
    "v_and_b32 v3, s25, v188\n"
    "v_mfma_f32_32x32x16_bf16 v[80:95], a[132:135], v[116:119], v[80:95]\n"
    "s_waitcnt lgkmcnt(8)\n"
    "ds_read_b64_tr_b16 a[120:121], v16 offset:1536\n"
    "ds_read_b64_tr_b16 a[122:123], v17 offset:1536\n"
    "v_lshlrev_b32 v4, 16, v189\n"
    "v_and_b32 v5, s25, v189\n"
    "v_lshlrev_b32 v6, 16, v190\n"
    "v_and_b32 v7, s25, v190\n"
    "v_mfma_f32_32x32x16_bf16 v[96:111], a[136:139], v[116:119], v[96:111]\n"
    "s_add_u32 s6, s6, s7\n"
    "s_add_u32 s20, s69, 54272\n"
    "ds_read_b64_tr_b16 a[124:125], v16 offset:4096\n"
    "ds_read_b64_tr_b16 a[126:127], v17 offset:4096\n"
    "v_lshlrev_b32 v8, 16, v191\n"
    "v_and_b32 v9, s25, v191\n"
    "v_fma_f32 v2, v2, v30, v31\n"
    "v_mfma_f32_32x32x16_bf16 v[32:47], a[140:143], v[116:119], v[32:47]\n"
    "s_waitcnt lgkmcnt(6)\n"
    "ds_read_b64_tr_b16 a[128:129], v16 offset:4352\n"
    "ds_read_b64_tr_b16 a[130:131], v17 offset:4352\n"
    "buffer_load_dwordx4 v18, s[44:47], s20 offen offset:1024 lds\n"
    "v_fma_f32 v3, v3, v30, v31\n"
    "v_fma_f32 v4, v4, v30, v31\n"
    "v_mfma_f32_32x32x16_bf16 v[48:63], a[104:107], v[120:123], v[48:63]\n"
    "s_lshl_b32 s29, s34, 10\n"
    "ds_read_b64_tr_b16 a[132:133], v16 offset:4608\n"
    "ds_read_b64_tr_b16 a[134:135], v17 offset:4608\n"
    "v_fma_f32 v5, v5, v30, v31\n"
    "v_fma_f32 v6, v6, v30, v31\n"
    "v_fma_f32 v7, v7, v30, v31\n"
    "v_fma_f32 v8, v8, v30, v31\n"
    "v_mfma_f32_32x32x16_bf16 v[64:79], a[108:111], v[120:123], v[64:79]\n"
    "s_mul_i32 s86, s0, 294912\n"
    "ds_read_b64_tr_b16 a[136:137], v16 offset:4864\n"
    "ds_read_b64_tr_b16 a[138:139], v17 offset:4864\n"
    "v_fma_f32 v9, v9, v30, v31\n"
    "v_cvt_pk_bf16_f32 v188, v2, v3\n"
    "v_cvt_pk_bf16_f32 v189, v4, v5\n"
    "v_cvt_pk_bf16_f32 v190, v6, v7\n"
    "v_mfma_f32_32x32x16_bf16 v[80:95], a[112:115], v[120:123], v[80:95]\n"
    "s_waitcnt vmcnt(9) lgkmcnt(8)\n"
    "s_barrier\n"
    "v_cvt_pk_bf16_f32 v191, v8, v9\n"
    "v_mfma_f32_32x32x16_bf16 v[96:111], a[116:119], v[120:123], v[96:111]\n"
    "s_add_u32 s86, s86, s29\n"
    "s_add_u32 s20, s69, 108544\n"
    "ds_read_b64_tr_b16 a[140:141], v16 offset:1792\n"
    "ds_read_b64_tr_b16 a[142:143], v17 offset:1792\n"
    "ds_read_b64_tr_b16 a[104:105], v16 offset:5120\n"
    "ds_read_b64_tr_b16 a[106:107], v17 offset:5120\n"
    "v_lshlrev_b32 v2, 16, v192\n"
    "v_mfma_f32_32x32x16_bf16 v[32:47], a[120:123], v[120:123], v[32:47]\n"
    "s_waitcnt lgkmcnt(6)\n"
    "ds_read_b64_tr_b16 a[108:109], v16 offset:5376\n"
    "ds_read_b64_tr_b16 a[110:111], v17 offset:5376\n"
    "buffer_load_dwordx4 v18, s[44:47], s20 offen offset:2048 lds\n"
    "v_and_b32 v3, s25, v192\n"
    "v_lshlrev_b32 v4, 16, v193\n"
    "v_mfma_f32_32x32x16_bf16 v[48:63], a[124:127], v[124:127], v[48:63]\n"
    "s_add_u32 s86, s86, 16781312\n"
    "ds_read_b64_tr_b16 a[112:113], v16 offset:5632\n"
    "ds_read_b64_tr_b16 a[114:115], v17 offset:5632\n"
    "v_and_b32 v5, s25, v193\n"
    "v_lshlrev_b32 v6, 16, v194\n"
    "v_and_b32 v7, s25, v194\n"
    "v_lshlrev_b32 v8, 16, v195\n"
    "v_mfma_f32_32x32x16_bf16 v[64:79], a[128:131], v[124:127], v[64:79]\n"
    "s_mul_i32 s83, s3, 73728\n"
    "ds_read_b64_tr_b16 a[116:117], v16 offset:5888\n"
    "ds_read_b64_tr_b16 a[118:119], v17 offset:5888\n"
    "v_and_b32 v9, s25, v195\n"
    "v_fma_f32 v2, v2, v30, v31\n"
    "v_fma_f32 v3, v3, v30, v31\n"
    "v_fma_f32 v4, v4, v30, v31\n"
    "v_mfma_f32_32x32x16_bf16 v[80:95], a[132:135], v[124:127], v[80:95]\n"
    "s_waitcnt lgkmcnt(8)\n"
    "s_add_u32 s20, s69, 162816\n"
    "ds_read_b64_tr_b16 a[120:121], v16 offset:6144\n"
    "ds_read_b64_tr_b16 a[122:123], v17 offset:6144\n"
    "v_fma_f32 v5, v5, v30, v31\n"
    "v_fma_f32 v6, v6, v30, v31\n"
    "v_fma_f32 v7, v7, v30, v31\n"
    "v_mfma_f32_32x32x16_bf16 v[96:111], a[136:139], v[124:127], v[96:111]\n"
    "s_add_u32 s83, s83, s86\n"
    "ds_read_b64_tr_b16 a[124:125], v16 offset:7168\n"
    "ds_read_b64_tr_b16 a[126:127], v17 offset:7168\n"
    "buffer_load_dwordx4 v18, s[44:47], s20 offen offset:3072 lds\n"
    "v_fma_f32 v8, v8, v30, v31\n"
    "v_fma_f32 v9, v9, v30, v31\n"
    "v_mfma_f32_32x32x16_bf16 v[32:47], a[140:143], v[124:127], v[32:47]\n"
    "s_waitcnt lgkmcnt(6)\n"
    "ds_read_b64_tr_b16 a[128:129], v16 offset:7424\n"
    "ds_read_b64_tr_b16 a[130:131], v17 offset:7424\n"
    "v_cvt_pk_bf16_f32 v192, v2, v3\n"
    "v_cvt_pk_bf16_f32 v193, v4, v5\n"
    "v_cvt_pk_bf16_f32 v194, v6, v7\n"
    "v_cvt_pk_bf16_f32 v195, v8, v9\n"
    "v_mfma_f32_32x32x16_bf16 v[48:63], a[104:107], v[128:131], v[48:63]\n"
    "s_lshl2_add_u32 s1, s36, s34\n"
    "ds_read_b64_tr_b16 a[132:133], v16 offset:7680\n"
    "ds_read_b64_tr_b16 a[134:135], v17 offset:7680\n"
    "v_lshlrev_b32 v2, 16, v196\n"
    "v_and_b32 v3, s25, v196\n"
    "v_lshlrev_b32 v4, 16, v197\n"
    "v_and_b32 v5, s25, v197\n"
    "v_mfma_f32_32x32x16_bf16 v[64:79], a[108:111], v[128:131], v[64:79]\n"
    "s_lshl_b32 s9, s1, 12\n"
    "s_cmp_eq_u32 s11, 5\n"
    "s_cselect_b32 s20, 4293197824, s98\n"
    "s_add_u32 s69, s69, s20\n"
    "s_add_u32 m0, s68, 20480\n"
    "ds_read_b64_tr_b16 a[136:137], v16 offset:7936\n"
    "ds_read_b64_tr_b16 a[138:139], v17 offset:7936\n"
    "v_mfma_f32_32x32x16_bf16 v[80:95], a[112:115], v[128:131], v[80:95]\n"
    "s_waitcnt lgkmcnt(8)\n"
    "ds_read_b64_tr_b16 a[140:141], v16 offset:6400\n"
    "ds_read_b64_tr_b16 a[142:143], v17 offset:6400\n"
    "buffer_load_dwordx4 v18, s[44:47], s69 offen lds\n"
    "v_lshlrev_b32 v6, 16, v198\n"
    "v_and_b32 v7, s25, v198\n"
    "v_mfma_f32_32x32x16_bf16 v[96:111], a[116:119], v[128:131], v[96:111]\n"
    "s_add_u32 s9, s9, 1052672\n"
    "ds_read_b64_tr_b16 a[104:105], v16 offset:8192\n"
    "ds_read_b64_tr_b16 a[106:107], v17 offset:8192\n"
    "v_lshlrev_b32 v8, 16, v199\n"
    "v_and_b32 v9, s25, v199\n"
    "v_fma_f32 v2, v2, v30, v31\n"
    "v_fma_f32 v3, v3, v30, v31\n"
    "v_mfma_f32_32x32x16_bf16 v[32:47], a[120:123], v[128:131], v[32:47]\n"
    "s_waitcnt lgkmcnt(6)\n"
    "ds_read_b64_tr_b16 a[108:109], v16 offset:8448\n"
    "ds_read_b64_tr_b16 a[110:111], v17 offset:8448\n"
    "v_fma_f32 v4, v4, v30, v31\n"
    "v_fma_f32 v5, v5, v30, v31\n"
    "v_fma_f32 v6, v6, v30, v31\n"
    "v_fma_f32 v7, v7, v30, v31\n"
    "v_mfma_f32_32x32x16_bf16 v[48:63], a[124:127], v[132:135], v[48:63]\n"
    "s_lshl_b32 s8, s35, 7\n"
    "ds_read_b64_tr_b16 a[112:113], v16 offset:8704\n"
    "ds_read_b64_tr_b16 a[114:115], v17 offset:8704\n"
    "v_fma_f32 v8, v8, v30, v31\n"
    "v_fma_f32 v9, v9, v30, v31\n"
    "v_cvt_pk_bf16_f32 v196, v2, v3\n"
    "v_cvt_pk_bf16_f32 v197, v4, v5\n"
    "v_cvt_pk_bf16_f32 v198, v6, v7\n"
    "v_mfma_f32_32x32x16_bf16 v[64:79], a[128:131], v[132:135], v[64:79]\n"
    "s_add_u32 s8, s8, s9\n"
    "s_cmp_eq_u32 s11, 5\n"
    "s_cselect_b32 s20, 4290248704, s81\n"
    "s_add_u32 s72, s72, s20\n"
    "s_add_u32 m0, s68, 21504\n"
    "ds_read_b64_tr_b16 a[116:117], v16 offset:8960\n"
    "ds_read_b64_tr_b16 a[118:119], v17 offset:8960\n"
    "v_mfma_f32_32x32x16_bf16 v[80:95], a[132:135], v[132:135], v[80:95]\n"
    "s_waitcnt vmcnt(8) lgkmcnt(8)\n"
    "s_barrier\n"
    "v_mfma_f32_32x32x16_bf16 v[96:111], a[136:139], v[132:135], v[96:111]\n"
    "s_lshl4_add_u32 s94, s1, s35\n"
    "ds_read_b64_tr_b16 a[120:121], v16 offset:6656\n"
    "ds_read_b64_tr_b16 a[122:123], v17 offset:6656\n"
    "ds_read_b64_tr_b16 a[124:125], v16 offset:9216\n"
    "ds_read_b64_tr_b16 a[126:127], v17 offset:9216\n"
    "buffer_load_dwordx4 v20, s[48:51], s72 offen lds\n"
    "v_mfma_f32_32x32x16_bf16 v[32:47], a[140:143], v[132:135], v[32:47]\n"
    "s_waitcnt lgkmcnt(6)\n"
    "ds_read_b64_tr_b16 a[128:129], v16 offset:9472\n"
    "ds_read_b64_tr_b16 a[130:131], v17 offset:9472\n"
    "v_cvt_pk_bf16_f32 v199, v8, v9\n"
    "v_lshlrev_b32 v2, 16, v200\n"
    "v_and_b32 v3, s25, v200\n"
    "v_lshlrev_b32 v4, 16, v201\n"
    "v_mfma_f32_32x32x16_bf16 v[48:63], a[104:107], v[136:139], v[48:63]\n"
    "s_lshl_b32 s94, s94, 11\n"
    "s_add_u32 s20, s69, 54272\n"
    "ds_read_b64_tr_b16 a[132:133], v16 offset:9728\n"
    "ds_read_b64_tr_b16 a[134:135], v17 offset:9728\n"
    "v_and_b32 v5, s25, v201\n"
    "v_lshlrev_b32 v6, 16, v202\n"
    "v_and_b32 v7, s25, v202\n"
    "v_mfma_f32_32x32x16_bf16 v[64:79], a[108:111], v[136:139], v[64:79]\n"
    "s_add_u32 s94, s94, 1708032\n"
    "ds_read_b64_tr_b16 a[136:137], v16 offset:9984\n"
    "ds_read_b64_tr_b16 a[138:139], v17 offset:9984\n"
    "buffer_load_dwordx4 v18, s[44:47], s20 offen offset:1024 lds\n"
    "v_lshlrev_b32 v8, 16, v203\n"
    "v_and_b32 v9, s25, v203\n"
    "v_mfma_f32_32x32x16_bf16 v[80:95], a[112:115], v[136:139], v[80:95]\n"
    "s_waitcnt lgkmcnt(8)\n"
    "ds_read_b64_tr_b16 a[140:141], v16 offset:6912\n"
    "ds_read_b64_tr_b16 a[142:143], v17 offset:6912\n"
    "v_fma_f32 v2, v2, v30, v31\n"
    "v_fma_f32 v3, v3, v30, v31\n"
    "v_fma_f32 v4, v4, v30, v31\n"
    "v_fma_f32 v5, v5, v30, v31\n"
    "v_mfma_f32_32x32x16_bf16 v[96:111], a[116:119], v[136:139], v[96:111]\n"
    "s_lshl4_add_u32 s99, s35, s36\n"
    "ds_read_b64_tr_b16 a[104:105], v16 offset:10240\n"
    "ds_read_b64_tr_b16 a[106:107], v17 offset:10240\n"
    "v_fma_f32 v6, v6, v30, v31\n"
    "v_fma_f32 v7, v7, v30, v31\n"
    "v_fma_f32 v8, v8, v30, v31\n"
    "v_fma_f32 v9, v9, v30, v31\n"
    "v_mfma_f32_32x32x16_bf16 v[32:47], a[120:123], v[136:139], v[32:47]\n"
    "s_waitcnt lgkmcnt(6)\n"
    "s_add_u32 s20, s69, 108544\n"
    "ds_read_b64_tr_b16 a[108:109], v16 offset:10496\n"
    "ds_read_b64_tr_b16 a[110:111], v17 offset:10496\n"
    "v_cvt_pk_bf16_f32 v200, v2, v3\n"
    "v_cvt_pk_bf16_f32 v201, v4, v5\n"
    "v_cvt_pk_bf16_f32 v202, v6, v7\n"
    "v_mfma_f32_32x32x16_bf16 v[48:63], a[124:127], v[140:143], v[48:63]\n"
    "s_lshl2_add_u32 s99, s99, s34\n"
    "ds_read_b64_tr_b16 a[112:113], v16 offset:10752\n"
    "ds_read_b64_tr_b16 a[114:115], v17 offset:10752\n"
    "buffer_load_dwordx4 v18, s[44:47], s20 offen offset:2048 lds\n"
    "v_cvt_pk_bf16_f32 v203, v8, v9\n"
    "v_lshlrev_b32 v2, 16, v204\n"
    "v_mfma_f32_32x32x16_bf16 v[64:79], a[128:131], v[140:143], v[64:79]\n"
    "s_lshl_b32 s99, s99, 12\n"
    "ds_read_b64_tr_b16 a[116:117], v16 offset:11008\n"
    "ds_read_b64_tr_b16 a[118:119], v17 offset:11008\n"
    "v_and_b32 v3, s25, v204\n"
    "v_lshlrev_b32 v4, 16, v205\n"
    "v_and_b32 v5, s25, v205\n"
    "v_lshlrev_b32 v6, 16, v206\n"
    "v_mfma_f32_32x32x16_bf16 v[80:95], a[132:135], v[140:143], v[80:95]\n"
    "s_waitcnt lgkmcnt(8)\n"
    "ds_read_b64_tr_b16 a[120:121], v16 offset:11264\n"
    "ds_read_b64_tr_b16 a[122:123], v17 offset:11264\n"
    "v_and_b32 v7, s25, v206\n"
    "v_lshlrev_b32 v8, 16, v207\n"
    "v_and_b32 v9, s25, v207\n"
    "v_fma_f32 v2, v2, v30, v31\n"
    "v_fma_f32 v3, v3, v30, v31\n"
    "v_mfma_f32_32x32x16_bf16 v[96:111], a[136:139], v[140:143], v[96:111]\n"
    "s_add_u32 s99, s99, 12587008\n"
    "ds_read_b64_tr_b16 a[124:125], v16 offset:12288\n"
    "ds_read_b64_tr_b16 a[126:127], v17 offset:12288\n"
    "v_fma_f32 v4, v4, v30, v31\n"
    "v_fma_f32 v5, v5, v30, v31\n"
    "v_fma_f32 v6, v6, v30, v31\n"
    "v_fma_f32 v7, v7, v30, v31\n"
    "v_mfma_f32_32x32x16_bf16 v[32:47], a[140:143], v[140:143], v[32:47]\n"
    "s_waitcnt lgkmcnt(6)\n"
    "s_add_u32 s20, s69, 162816\n"
    "ds_read_b64_tr_b16 a[128:129], v16 offset:12544\n"
    "ds_read_b64_tr_b16 a[130:131], v17 offset:12544\n"
    "v_fma_f32 v8, v8, v30, v31\n"
    "v_fma_f32 v9, v9, v30, v31\n"
    "v_cvt_pk_bf16_f32 v204, v2, v3\n"
    "v_mfma_f32_32x32x16_bf16 v[48:63], a[104:107], v[144:147], v[48:63]\n"
    "s_lshl_b32 s29, s35, 6\n"
    "ds_read_b64_tr_b16 a[132:133], v16 offset:12800\n"
    "ds_read_b64_tr_b16 a[134:135], v17 offset:12800\n"
    "buffer_load_dwordx4 v18, s[44:47], s20 offen offset:3072 lds\n"
    "v_cvt_pk_bf16_f32 v205, v4, v5\n"
    "v_cvt_pk_bf16_f32 v206, v6, v7\n"
    "v_mfma_f32_32x32x16_bf16 v[64:79], a[108:111], v[144:147], v[64:79]\n"
    "s_lshl2_add_u32 s0, s3, s29\n"
    "ds_read_b64_tr_b16 a[136:137], v16 offset:13056\n"
    "ds_read_b64_tr_b16 a[138:139], v17 offset:13056\n"
    "v_cvt_pk_bf16_f32 v207, v8, v9\n"
    "v_lshlrev_b32 v2, 16, v208\n"
    "v_and_b32 v3, s25, v208\n"
    "v_lshlrev_b32 v4, 16, v209\n"
    "v_mfma_f32_32x32x16_bf16 v[80:95], a[112:115], v[144:147], v[80:95]\n"
    "s_waitcnt vmcnt(9) lgkmcnt(8)\n"
    "s_barrier\n"
    "v_mfma_f32_32x32x16_bf16 v[96:111], a[116:119], v[144:147], v[96:111]\n"
    "s_add_u32 s0, s0, s34\n"
    "s_cmp_eq_u32 s11, 4\n"
    "s_cselect_b32 s20, 4293197824, s98\n"
    "ds_read_b64_tr_b16 a[140:141], v16 offset:11520\n"
    "ds_read_b64_tr_b16 a[142:143], v17 offset:11520\n"
    "ds_read_b64_tr_b16 a[104:105], v16 offset:13312\n"
    "ds_read_b64_tr_b16 a[106:107], v17 offset:13312\n"
    "v_mfma_f32_32x32x16_bf16 v[32:47], a[120:123], v[144:147], v[32:47]\n"
    "s_waitcnt lgkmcnt(6)\n"
    "s_add_u32 s69, s69, s20\n"
    "s_add_u32 m0, s68, 25600\n"
    "ds_read_b64_tr_b16 a[108:109], v16 offset:13568\n"
    "ds_read_b64_tr_b16 a[110:111], v17 offset:13568\n"
    "buffer_load_dwordx4 v18, s[44:47], s69 offen lds\n"
    "v_mfma_f32_32x32x16_bf16 v[48:63], a[124:127], v[148:151], v[48:63]\n"
    "s_mul_i32 s0, s0, 6144\n"
    "ds_read_b64_tr_b16 a[112:113], v16 offset:13824\n"
    "ds_read_b64_tr_b16 a[114:115], v17 offset:13824\n"
    "v_and_b32 v5, s25, v209\n"
    "v_lshlrev_b32 v6, 16, v210\n"
    "v_and_b32 v7, s25, v210\n"
    "v_lshlrev_b32 v8, 16, v211\n"
    "v_mfma_f32_32x32x16_bf16 v[64:79], a[128:131], v[148:151], v[64:79]\n"
    "s_add_u32 s0, s0, 6295552\n"
    "ds_read_b64_tr_b16 a[116:117], v16 offset:14080\n"
    "ds_read_b64_tr_b16 a[118:119], v17 offset:14080\n"
    "v_and_b32 v9, s25, v211\n"
    "v_fma_f32 v2, v2, v30, v31\n"
    "v_fma_f32 v3, v3, v30, v31\n"
    "v_fma_f32 v4, v4, v30, v31\n"
    "v_mfma_f32_32x32x16_bf16 v[80:95], a[132:135], v[148:151], v[80:95]\n"
    "s_waitcnt lgkmcnt(8)\n"
    "ds_read_b64_tr_b16 a[120:121], v16 offset:11776\n"
    "ds_read_b64_tr_b16 a[122:123], v17 offset:11776\n"
    "v_fma_f32 v5, v5, v30, v31\n"
    "v_fma_f32 v6, v6, v30, v31\n"
    "v_fma_f32 v7, v7, v30, v31\n"
    "v_fma_f32 v8, v8, v30, v31\n"
    "v_mfma_f32_32x32x16_bf16 v[96:111], a[136:139], v[148:151], v[96:111]\n"
    "s_and_b32 s90, s39, 3\n"
    "s_cmp_eq_u32 s11, 4\n"
    "s_cselect_b32 s20, 4290248704, s81\n"
    "s_add_u32 s72, s72, s20\n"
    "s_add_u32 m0, s68, 26624\n"
    "ds_read_b64_tr_b16 a[124:125], v16 offset:14336\n"
    "ds_read_b64_tr_b16 a[126:127], v17 offset:14336\n"
    "v_mfma_f32_32x32x16_bf16 v[32:47], a[140:143], v[148:151], v[32:47]\n"
    "s_waitcnt lgkmcnt(6)\n"
    "ds_read_b64_tr_b16 a[128:129], v16 offset:14592\n"
    "ds_read_b64_tr_b16 a[130:131], v17 offset:14592\n"
    "buffer_load_dwordx4 v20, s[48:51], s72 offen lds\n"
    "v_fma_f32 v9, v9, v30, v31\n"
    "v_cvt_pk_bf16_f32 v208, v2, v3\n"
    "v_mfma_f32_32x32x16_bf16 v[48:63], a[104:107], v[152:155], v[48:63]\n"
    "s_mul_i32 s90, s90, 98304\n"
    "ds_read_b64_tr_b16 a[132:133], v16 offset:14848\n"
    "ds_read_b64_tr_b16 a[134:135], v17 offset:14848\n"
    "v_cvt_pk_bf16_f32 v209, v4, v5\n"
    "v_cvt_pk_bf16_f32 v210, v6, v7\n"
    "v_cvt_pk_bf16_f32 v211, v8, v9\n"
    "v_lshlrev_b32 v2, 16, v212\n"
    "v_and_b32 v3, s25, v212\n"
    "v_mfma_f32_32x32x16_bf16 v[64:79], a[108:111], v[152:155], v[64:79]\n"
    "s_add_u32 s90, s90, s0\n"
    "ds_read_b64_tr_b16 a[136:137], v16 offset:15104\n"
    "ds_read_b64_tr_b16 a[138:139], v17 offset:15104\n"
    "v_lshlrev_b32 v4, 16, v213\n"
    "v_and_b32 v5, s25, v213\n"
    "v_lshlrev_b32 v6, 16, v214\n"
    "v_and_b32 v7, s25, v214\n"
    "v_mfma_f32_32x32x16_bf16 v[80:95], a[112:115], v[152:155], v[80:95]\n"
    "s_waitcnt lgkmcnt(8)\n"
    "s_add_u32 s20, s69, 54272\n"
    "ds_read_b64_tr_b16 a[140:141], v16 offset:12032\n"
    "ds_read_b64_tr_b16 a[142:143], v17 offset:12032\n"
    "v_lshlrev_b32 v8, 16, v215\n"
    "v_and_b32 v9, s25, v215\n"
    "v_fma_f32 v2, v2, v30, v31\n"
    "v_mfma_f32_32x32x16_bf16 v[96:111], a[116:119], v[152:155], v[96:111]\n"
    "s_add_u32 s91, s39, 1\n"
    "ds_read_b64_tr_b16 a[104:105], v16 offset:15360\n"
    "ds_read_b64_tr_b16 a[106:107], v17 offset:15360\n"
    "buffer_load_dwordx4 v18, s[44:47], s20 offen offset:1024 lds\n"
    "v_fma_f32 v3, v3, v30, v31\n"
    "v_fma_f32 v4, v4, v30, v31\n"
    "v_mfma_f32_32x32x16_bf16 v[32:47], a[120:123], v[152:155], v[32:47]\n"
    "s_waitcnt lgkmcnt(6)\n"
    "ds_read_b64_tr_b16 a[108:109], v16 offset:15616\n"
    "ds_read_b64_tr_b16 a[110:111], v17 offset:15616\n"
    "v_fma_f32 v5, v5, v30, v31\n"
    "v_fma_f32 v6, v6, v30, v31\n"
    "v_fma_f32 v7, v7, v30, v31\n"
    "v_fma_f32 v8, v8, v30, v31\n"
    "v_mfma_f32_32x32x16_bf16 v[48:63], a[124:127], v[156:159], v[48:63]\n"
    "s_and_b32 s91, s91, 3\n"
    "ds_read_b64_tr_b16 a[112:113], v16 offset:15872\n"
    "ds_read_b64_tr_b16 a[114:115], v17 offset:15872\n"
    "v_fma_f32 v9, v9, v30, v31\n"
    "v_cvt_pk_bf16_f32 v212, v2, v3\n"
    "v_cvt_pk_bf16_f32 v213, v4, v5\n"
    "v_cvt_pk_bf16_f32 v214, v6, v7\n"
    "v_mfma_f32_32x32x16_bf16 v[64:79], a[128:131], v[156:159], v[64:79]\n"
    "s_mul_i32 s91, s91, 98304\n"
    "s_add_u32 s20, s69, 108544\n"
    "ds_read_b64_tr_b16 a[116:117], v16 offset:16128\n"
    "ds_read_b64_tr_b16 a[118:119], v17 offset:16128\n"
    "v_cvt_pk_bf16_f32 v215, v8, v9\n"
    "v_lshlrev_b32 v2, 16, v216\n"
    "v_and_b32 v3, s25, v216\n"
    "v_mfma_f32_32x32x16_bf16 v[80:95], a[132:135], v[156:159], v[80:95]\n"
    "s_waitcnt vmcnt(8) lgkmcnt(8)\n"
    "s_barrier\n"
    "v_mfma_f32_32x32x16_bf16 v[96:111], a[136:139], v[156:159], v[96:111]\n"
    "s_add_u32 s91, s91, s0\n"
    "ds_read_b64_tr_b16 a[120:121], v16 offset:16384\n"
    "ds_read_b64_tr_b16 a[122:123], v17 offset:16384\n"
    "ds_read_b64_tr_b16 a[124:125], v16 offset:17408\n"
    "ds_read_b64_tr_b16 a[126:127], v17 offset:17408\n"
    "buffer_load_dwordx4 v18, s[44:47], s20 offen offset:2048 lds\n"
    "v_mfma_f32_32x32x16_bf16 v[32:47], a[140:143], v[156:159], v[32:47]\n"
    "s_waitcnt lgkmcnt(6)\n"
    "ds_read_b64_tr_b16 a[128:129], v16 offset:17664\n"
    "ds_read_b64_tr_b16 a[130:131], v17 offset:17664\n"
    "v_lshlrev_b32 v4, 16, v217\n"
    "v_and_b32 v5, s25, v217\n"
    "v_lshlrev_b32 v6, 16, v218\n"
    "v_and_b32 v7, s25, v218\n"
    "v_mfma_f32_32x32x16_bf16 v[48:63], a[104:107], v[160:163], v[48:63]\n"
    "s_add_u32 s92, s39, 2\n"
    "ds_read_b64_tr_b16 a[132:133], v16 offset:17920\n"
    "ds_read_b64_tr_b16 a[134:135], v17 offset:17920\n"
    "v_lshlrev_b32 v8, 16, v219\n"
    "v_and_b32 v9, s25, v219\n"
    "v_fma_f32 v2, v2, v30, v31\n"
    "v_fma_f32 v3, v3, v30, v31\n"
    "v_mfma_f32_32x32x16_bf16 v[64:79], a[108:111], v[160:163], v[64:79]\n"
    "s_and_b32 s92, s92, 3\n"
    "s_add_u32 s20, s69, 162816\n"
    "ds_read_b64_tr_b16 a[136:137], v16 offset:18176\n"
    "ds_read_b64_tr_b16 a[138:139], v17 offset:18176\n"
    "v_fma_f32 v4, v4, v30, v31\n"
    "v_fma_f32 v5, v5, v30, v31\n"
    "v_fma_f32 v6, v6, v30, v31\n"
    "v_mfma_f32_32x32x16_bf16 v[80:95], a[112:115], v[160:163], v[80:95]\n"
    "s_waitcnt lgkmcnt(8)\n"
    "ds_read_b64_tr_b16 a[140:141], v16 offset:16640\n"
    "ds_read_b64_tr_b16 a[142:143], v17 offset:16640\n"
    "buffer_load_dwordx4 v18, s[44:47], s20 offen offset:3072 lds\n"
    "v_fma_f32 v7, v7, v30, v31\n"
    "v_fma_f32 v8, v8, v30, v31\n"
    "v_fma_f32 v9, v9, v30, v31\n"
    "v_mfma_f32_32x32x16_bf16 v[96:111], a[116:119], v[160:163], v[96:111]\n"
    "s_mul_i32 s92, s92, 98304\n"
    "ds_read_b64_tr_b16 a[104:105], v16 offset:18432\n"
    "ds_read_b64_tr_b16 a[106:107], v17 offset:18432\n"
    "v_cvt_pk_bf16_f32 v216, v2, v3\n"
    "v_cvt_pk_bf16_f32 v217, v4, v5\n"
    "v_cvt_pk_bf16_f32 v218, v6, v7\n"
    "v_cvt_pk_bf16_f32 v219, v8, v9\n"
    "v_mfma_f32_32x32x16_bf16 v[32:47], a[120:123], v[160:163], v[32:47]\n"
    "s_waitcnt lgkmcnt(6)\n"
    "ds_read_b64_tr_b16 a[108:109], v16 offset:18688\n"
    "ds_read_b64_tr_b16 a[110:111], v17 offset:18688\n"
    "v_lshlrev_b32 v2, 16, v220\n"
    "v_and_b32 v3, s25, v220\n"
    "v_lshlrev_b32 v4, 16, v221\n"
    "v_and_b32 v5, s25, v221\n"
    "v_mfma_f32_32x32x16_bf16 v[48:63], a[124:127], v[164:167], v[48:63]\n"
    "s_add_u32 s92, s92, s0\n"
    "s_cmp_eq_u32 s11, 3\n"
    "s_cselect_b32 s20, 4293197824, s98\n"
    "s_add_u32 s69, s69, s20\n"
    "s_add_u32 m0, s68, 30720\n"
    "ds_read_b64_tr_b16 a[112:113], v16 offset:18944\n"
    "ds_read_b64_tr_b16 a[114:115], v17 offset:18944\n"
    "v_mfma_f32_32x32x16_bf16 v[64:79], a[128:131], v[164:167], v[64:79]\n"
    "s_add_u32 s92, s92, 2048\n"
    "ds_read_b64_tr_b16 a[116:117], v16 offset:19200\n"
    "ds_read_b64_tr_b16 a[118:119], v17 offset:19200\n"
    "buffer_load_dwordx4 v18, s[44:47], s69 offen lds\n"
    "v_lshlrev_b32 v6, 16, v222\n"
    "v_and_b32 v7, s25, v222\n"
    "v_mfma_f32_32x32x16_bf16 v[80:95], a[132:135], v[164:167], v[80:95]\n"
    "s_waitcnt lgkmcnt(8)\n"
    "ds_read_b64_tr_b16 a[120:121], v16 offset:16896\n"
    "ds_read_b64_tr_b16 a[122:123], v17 offset:16896\n"
    "v_lshlrev_b32 v8, 16, v223\n"
    "v_and_b32 v9, s25, v223\n"
    "v_fma_f32 v2, v2, v30, v31\n"
    "v_fma_f32 v3, v3, v30, v31\n"
    "v_mfma_f32_32x32x16_bf16 v[96:111], a[136:139], v[164:167], v[96:111]\n"
    "s_add_u32 s93, s39, 3\n"
    "ds_read_b64_tr_b16 a[124:125], v16 offset:19456\n"
    "ds_read_b64_tr_b16 a[126:127], v17 offset:19456\n"
    "v_fma_f32 v4, v4, v30, v31\n"
    "v_fma_f32 v5, v5, v30, v31\n"
    "v_fma_f32 v6, v6, v30, v31\n"
    "v_fma_f32 v7, v7, v30, v31\n"
    "v_mfma_f32_32x32x16_bf16 v[32:47], a[140:143], v[164:167], v[32:47]\n"
    "s_waitcnt lgkmcnt(6)\n"
    "ds_read_b64_tr_b16 a[128:129], v16 offset:19712\n"
    "ds_read_b64_tr_b16 a[130:131], v17 offset:19712\n"
    "v_fma_f32 v8, v8, v30, v31\n"
    "v_fma_f32 v9, v9, v30, v31\n"
    "v_cvt_pk_bf16_f32 v220, v2, v3\n"
    "v_cvt_pk_bf16_f32 v221, v4, v5\n"
    "v_mfma_f32_32x32x16_bf16 v[48:63], a[104:107], v[168:171], v[48:63]\n"
    "s_and_b32 s93, s93, 3\n"
    "s_cmp_eq_u32 s11, 3\n"
    "s_cselect_b32 s20, 4290248704, s81\n"
    "s_add_u32 s72, s72, s20\n"
    "s_add_u32 m0, s68, 31744\n"
    "ds_read_b64_tr_b16 a[132:133], v16 offset:19968\n"
    "ds_read_b64_tr_b16 a[134:135], v17 offset:19968\n"
    "v_mfma_f32_32x32x16_bf16 v[64:79], a[108:111], v[168:171], v[64:79]\n"
    "s_mul_i32 s93, s93, 98304\n"
    "ds_read_b64_tr_b16 a[136:137], v16 offset:20224\n"
    "ds_read_b64_tr_b16 a[138:139], v17 offset:20224\n"
    "buffer_load_dwordx4 v20, s[48:51], s72 offen lds\n"
    "v_cvt_pk_bf16_f32 v222, v6, v7\n"
    "v_cvt_pk_bf16_f32 v223, v8, v9\n"
    "v_mfma_f32_32x32x16_bf16 v[80:95], a[112:115], v[168:171], v[80:95]\n"
    "s_waitcnt vmcnt(8) lgkmcnt(8)\n"
    "s_barrier\n"
    "v_mfma_f32_32x32x16_bf16 v[96:111], a[116:119], v[168:171], v[96:111]\n"
    "s_add_u32 s93, s93, s0\n"
    "ds_read_b64_tr_b16 a[140:141], v16 offset:17152\n"
    "ds_read_b64_tr_b16 a[142:143], v17 offset:17152\n"
    "ds_read_b64_tr_b16 a[104:105], v16 offset:20480\n"
    "ds_read_b64_tr_b16 a[106:107], v17 offset:20480\n"
    "v_lshlrev_b32 v2, 16, v224\n"
    "v_and_b32 v3, s25, v224\n"
    "v_mfma_f32_32x32x16_bf16 v[32:47], a[120:123], v[168:171], v[32:47]\n"
    "s_waitcnt lgkmcnt(6)\n"
    "s_add_u32 m0, s68, 0\n"
    "s_add_u32 s20, s69, 55296\n"
    "ds_read_b64_tr_b16 a[108:109], v16 offset:20736\n"
    "ds_read_b64_tr_b16 a[110:111], v17 offset:20736\n"
    "v_lshlrev_b32 v4, 16, v225\n"
    "v_and_b32 v5, s25, v225\n"
    "v_mfma_f32_32x32x16_bf16 v[48:63], a[124:127], v[172:175], v[48:63]\n"
    "s_add_u32 s93, s93, 4096\n"
    "ds_read_b64_tr_b16 a[112:113], v16 offset:20992\n"
    "ds_read_b64_tr_b16 a[114:115], v17 offset:20992\n"
    "buffer_load_dwordx4 v18, s[44:47], s20 offen lds\n"
    "v_lshlrev_b32 v6, 16, v226\n"
    "v_and_b32 v7, s25, v226\n"
    "v_lshlrev_b32 v8, 16, v227\n"
    "v_mfma_f32_32x32x16_bf16 v[64:79], a[128:131], v[172:175], v[64:79]\n"
    "s_lshl_b32 s0, s35, 7\n"
    "ds_read_b64_tr_b16 a[116:117], v16 offset:21248\n"
    "ds_read_b64_tr_b16 a[118:119], v17 offset:21248\n"
    "v_and_b32 v9, s25, v227\n"
    "v_fma_f32 v2, v2, v30, v31\n"
    "v_fma_f32 v3, v3, v30, v31\n"
    "v_fma_f32 v4, v4, v30, v31\n"
    "v_mfma_f32_32x32x16_bf16 v[80:95], a[132:135], v[172:175], v[80:95]\n"
    "s_waitcnt lgkmcnt(8)\n"
    "ds_read_b64_tr_b16 a[120:121], v16 offset:21504\n"
    "ds_read_b64_tr_b16 a[122:123], v17 offset:21504\n"
    "v_fma_f32 v5, v5, v30, v31\n"
    "v_fma_f32 v6, v6, v30, v31\n"
    "v_fma_f32 v7, v7, v30, v31\n"
    "v_fma_f32 v8, v8, v30, v31\n"
    "v_mfma_f32_32x32x16_bf16 v[96:111], a[136:139], v[172:175], v[96:111]\n"
    "s_lshl_b32 s95, s1, 15\n"
    "s_add_u32 s20, s69, 109568\n"
    "ds_read_b64_tr_b16 a[124:125], v16 offset:22528\n"
    "ds_read_b64_tr_b16 a[126:127], v17 offset:22528\n"
    "v_fma_f32 v9, v9, v30, v31\n"
    "v_cvt_pk_bf16_f32 v224, v2, v3\n"
    "v_cvt_pk_bf16_f32 v225, v4, v5\n"
    "v_mfma_f32_32x32x16_bf16 v[32:47], a[140:143], v[172:175], v[32:47]\n"
    "s_waitcnt lgkmcnt(6)\n"
    "ds_read_b64_tr_b16 a[128:129], v16 offset:22784\n"
    "ds_read_b64_tr_b16 a[130:131], v17 offset:22784\n"
    "buffer_load_dwordx4 v18, s[44:47], s20 offen offset:1024 lds\n"
    "v_cvt_pk_bf16_f32 v226, v6, v7\n"
    "v_cvt_pk_bf16_f32 v227, v8, v9\n"
    "v_mfma_f32_32x32x16_bf16 v[48:63], a[104:107], v[176:179], v[48:63]\n"
    "s_add_u32 s95, s95, s0\n"
    "ds_read_b64_tr_b16 a[132:133], v16 offset:23040\n"
    "ds_read_b64_tr_b16 a[134:135], v17 offset:23040\n"
    "v_lshlrev_b32 v2, 16, v228\n"
    "v_and_b32 v3, s25, v228\n"
    "v_lshlrev_b32 v4, 16, v229\n"
    "v_and_b32 v5, s25, v229\n"
    "v_mfma_f32_32x32x16_bf16 v[64:79], a[108:111], v[176:179], v[64:79]\n"
    "s_add_u32 s95, s95, 1708032\n"
    "ds_read_b64_tr_b16 a[136:137], v16 offset:23296\n"
    "ds_read_b64_tr_b16 a[138:139], v17 offset:23296\n"
    "v_lshlrev_b32 v6, 16, v230\n"
    "v_and_b32 v7, s25, v230\n"
    "v_lshlrev_b32 v8, 16, v231\n"
    "v_and_b32 v9, s25, v231\n"
    "v_mfma_f32_32x32x16_bf16 v[80:95], a[112:115], v[176:179], v[80:95]\n"
    "s_waitcnt lgkmcnt(8)\n"
    "ds_read_b64_tr_b16 a[140:141], v16 offset:21760\n"
    "ds_read_b64_tr_b16 a[142:143], v17 offset:21760\n"
    "v_fma_f32 v2, v2, v30, v31\n"
    "v_fma_f32 v3, v3, v30, v31\n"
    "v_fma_f32 v4, v4, v30, v31\n"
    "v_fma_f32 v5, v5, v30, v31\n"
    "v_mfma_f32_32x32x16_bf16 v[96:111], a[116:119], v[176:179], v[96:111]\n"
    "s_lshl_b32 s15, s1, 11\n"
    "s_add_u32 s20, s69, 163840\n"
    "ds_read_b64_tr_b16 a[104:105], v16 offset:23552\n"
    "ds_read_b64_tr_b16 a[106:107], v17 offset:23552\n"
    "v_fma_f32 v6, v6, v30, v31\n"
    "v_fma_f32 v7, v7, v30, v31\n"
    "v_fma_f32 v8, v8, v30, v31\n"
    "v_mfma_f32_32x32x16_bf16 v[32:47], a[120:123], v[176:179], v[32:47]\n"
    "s_waitcnt lgkmcnt(6)\n"
    "ds_read_b64_tr_b16 a[108:109], v16 offset:23808\n"
    "ds_read_b64_tr_b16 a[110:111], v17 offset:23808\n"
    "buffer_load_dwordx4 v18, s[44:47], s20 offen offset:2048 lds\n"
    "v_fma_f32 v9, v9, v30, v31\n"
    "v_cvt_pk_bf16_f32 v228, v2, v3\n"
    "v_mfma_f32_32x32x16_bf16 v[48:63], a[124:127], v[180:183], v[48:63]\n"
    "s_add_u32 s15, s15, s0\n"
    "ds_read_b64_tr_b16 a[112:113], v16 offset:24064\n"
    "ds_read_b64_tr_b16 a[114:115], v17 offset:24064\n"
    "v_cvt_pk_bf16_f32 v229, v4, v5\n"
    "v_cvt_pk_bf16_f32 v230, v6, v7\n"
    "v_cvt_pk_bf16_f32 v231, v8, v9\n"
    "v_lshlrev_b32 v2, 16, v232\n"
    "v_mfma_f32_32x32x16_bf16 v[64:79], a[128:131], v[180:183], v[64:79]\n"
    "s_add_u32 s15, s15, 3805184\n"
    "ds_read_b64_tr_b16 a[116:117], v16 offset:24320\n"
    "ds_read_b64_tr_b16 a[118:119], v17 offset:24320\n"
    "v_and_b32 v3, s25, v232\n"
    "v_lshlrev_b32 v4, 16, v233\n"
    "v_and_b32 v5, s25, v233\n"
    "v_lshlrev_b32 v6, 16, v234\n"
    "v_mfma_f32_32x32x16_bf16 v[80:95], a[132:135], v[180:183], v[80:95]\n"
    "s_waitcnt vmcnt(7) lgkmcnt(8)\n"
    "s_barrier\n"
    "v_and_b32 v7, s25, v234\n"
    "v_mfma_f32_32x32x16_bf16 v[96:111], a[136:139], v[180:183], v[96:111]\n"
    "s_xor_b32 s1, s1, 4\n"
    "s_cmp_eq_u32 s11, 2\n"
    "s_cselect_b32 s20, 4293197824, s98\n"
    "ds_read_b64_tr_b16 a[120:121], v16 offset:22016\n"
    "ds_read_b64_tr_b16 a[122:123], v17 offset:22016\n"
    "ds_read_b64_tr_b16 a[124:125], v16 offset:24576\n"
    "ds_read_b64_tr_b16 a[126:127], v17 offset:24576\n"
    "v_mfma_f32_32x32x16_bf16 v[32:47], a[140:143], v[180:183], v[32:47]\n"
    "s_waitcnt lgkmcnt(6)\n"
    "s_add_u32 s69, s69, s20\n"
    "s_add_u32 m0, s68, 3072\n"
    "ds_read_b64_tr_b16 a[128:129], v16 offset:24832\n"
    "ds_read_b64_tr_b16 a[130:131], v17 offset:24832\n"
    "buffer_load_dwordx4 v18, s[44:47], s69 offen lds\n"
    "v_mfma_f32_32x32x16_bf16 v[48:63], a[104:107], v[184:187], v[48:63]\n"
    "s_lshl_b32 s16, s1, 11\n"
    "ds_read_b64_tr_b16 a[132:133], v16 offset:25088\n"
    "ds_read_b64_tr_b16 a[134:135], v17 offset:25088\n"
    "v_lshlrev_b32 v8, 16, v235\n"
    "v_and_b32 v9, s25, v235\n"
    "v_fma_f32 v2, v2, v30, v31\n"
    "v_fma_f32 v3, v3, v30, v31\n"
    "v_mfma_f32_32x32x16_bf16 v[64:79], a[108:111], v[184:187], v[64:79]\n"
    "s_add_u32 s16, s16, 3805184\n"
    "ds_read_b64_tr_b16 a[136:137], v16 offset:25344\n"
    "ds_read_b64_tr_b16 a[138:139], v17 offset:25344\n"
    "v_fma_f32 v4, v4, v30, v31\n"
    "v_fma_f32 v5, v5, v30, v31\n"
    "v_fma_f32 v6, v6, v30, v31\n"
    "v_fma_f32 v7, v7, v30, v31\n"
    "v_mfma_f32_32x32x16_bf16 v[80:95], a[112:115], v[184:187], v[80:95]\n"
    "s_waitcnt lgkmcnt(8)\n"
    "s_cmp_eq_u32 s11, 2\n"
    "s_cselect_b32 s20, 4290248704, s81\n"
    "s_add_u32 s72, s72, s20\n"
    "s_add_u32 m0, s68, 4096\n"
    "ds_read_b64_tr_b16 a[140:141], v16 offset:22272\n"
    "ds_read_b64_tr_b16 a[142:143], v17 offset:22272\n"
    "v_mfma_f32_32x32x16_bf16 v[96:111], a[116:119], v[184:187], v[96:111]\n"
    "s_lshl_b32 s17, s1, 12\n"
    "ds_read_b64_tr_b16 a[104:105], v16 offset:25600\n"
    "ds_read_b64_tr_b16 a[106:107], v17 offset:25600\n"
    "buffer_load_dwordx4 v20, s[48:51], s72 offen lds\n"
    "v_fma_f32 v8, v8, v30, v31\n"
    "v_fma_f32 v9, v9, v30, v31\n"
    "v_mfma_f32_32x32x16_bf16 v[32:47], a[120:123], v[184:187], v[32:47]\n"
    "s_waitcnt lgkmcnt(6)\n"
    "ds_read_b64_tr_b16 a[108:109], v16 offset:25856\n"
    "ds_read_b64_tr_b16 a[110:111], v17 offset:25856\n"
    "v_cvt_pk_bf16_f32 v232, v2, v3\n"
    "v_cvt_pk_bf16_f32 v233, v4, v5\n"
    "v_cvt_pk_bf16_f32 v234, v6, v7\n"
    "v_cvt_pk_bf16_f32 v235, v8, v9\n"
    "v_mfma_f32_32x32x16_bf16 v[48:63], a[124:127], v[188:191], v[48:63]\n"
    "s_add_u32 s17, s17, 1054720\n"
    "ds_read_b64_tr_b16 a[112:113], v16 offset:26112\n"
    "ds_read_b64_tr_b16 a[114:115], v17 offset:26112\n"
    "v_lshlrev_b32 v2, 16, v236\n"
    "v_and_b32 v3, s25, v236\n"
    "v_lshlrev_b32 v4, 16, v237\n"
    "v_and_b32 v5, s25, v237\n"
    "v_mfma_f32_32x32x16_bf16 v[64:79], a[128:131], v[188:191], v[64:79]\n"
    "s_lshl_b32 s29, s3, 8\n"
    "s_add_u32 s20, s69, 54272\n"
    "ds_read_b64_tr_b16 a[116:117], v16 offset:26368\n"
    "ds_read_b64_tr_b16 a[118:119], v17 offset:26368\n"
    "v_lshlrev_b32 v6, 16, v238\n"
    "v_and_b32 v7, s25, v238\n"
    "v_lshlrev_b32 v8, 16, v239\n"
    "v_mfma_f32_32x32x16_bf16 v[80:95], a[132:135], v[188:191], v[80:95]\n"
    "s_waitcnt lgkmcnt(8)\n"
    "ds_read_b64_tr_b16 a[120:121], v16 offset:26624\n"
    "ds_read_b64_tr_b16 a[122:123], v17 offset:26624\n"
    "buffer_load_dwordx4 v18, s[44:47], s20 offen offset:1024 lds\n"
    "v_and_b32 v9, s25, v239\n"
    "v_fma_f32 v2, v2, v30, v31\n"
    "v_mfma_f32_32x32x16_bf16 v[96:111], a[136:139], v[188:191], v[96:111]\n"
    "s_mul_i32 s19, s34, 4608\n"
    "ds_read_b64_tr_b16 a[124:125], v16 offset:27648\n"
    "ds_read_b64_tr_b16 a[126:127], v17 offset:27648\n"
    "v_fma_f32 v3, v3, v30, v31\n"
    "v_fma_f32 v4, v4, v30, v31\n"
    "v_fma_f32 v5, v5, v30, v31\n"
    "v_fma_f32 v6, v6, v30, v31\n"
    "v_mfma_f32_32x32x16_bf16 v[32:47], a[140:143], v[188:191], v[32:47]\n"
    "s_waitcnt lgkmcnt(6)\n"
    "ds_read_b64_tr_b16 a[128:129], v16 offset:27904\n"
    "ds_read_b64_tr_b16 a[130:131], v17 offset:27904\n"
    "v_fma_f32 v7, v7, v30, v31\n"
    "v_fma_f32 v8, v8, v30, v31\n"
    "v_fma_f32 v9, v9, v30, v31\n"
    "v_cvt_pk_bf16_f32 v236, v2, v3\n"
    "v_mfma_f32_32x32x16_bf16 v[48:63], a[104:107], v[192:195], v[48:63]\n"
    "s_mul_i32 s76, s39, 1327104\n"
    "ds_read_b64_tr_b16 a[132:133], v16 offset:28160\n"
    "ds_read_b64_tr_b16 a[134:135], v17 offset:28160\n"
    "v_cvt_pk_bf16_f32 v237, v4, v5\n"
    "v_cvt_pk_bf16_f32 v238, v6, v7\n"
    "v_cvt_pk_bf16_f32 v239, v8, v9\n"
    "v_lshlrev_b32 v2, 16, v240\n"
    "v_and_b32 v3, s25, v240\n"
    "v_mfma_f32_32x32x16_bf16 v[64:79], a[108:111], v[192:195], v[64:79]\n"
    "s_add_u32 s76, s76, s19\n"
    "s_add_u32 s20, s69, 108544\n"
    "ds_read_b64_tr_b16 a[136:137], v16 offset:28416\n"
    "ds_read_b64_tr_b16 a[138:139], v17 offset:28416\n"
    "buffer_load_dwordx4 v18, s[44:47], s20 offen offset:2048 lds\n"
    "v_lshlrev_b32 v4, 16, v241\n"
    "v_mfma_f32_32x32x16_bf16 v[80:95], a[112:115], v[192:195], v[80:95]\n"
    "s_waitcnt vmcnt(8) lgkmcnt(8)\n"
    "s_barrier\n"
    "v_mfma_f32_32x32x16_bf16 v[96:111], a[116:119], v[192:195], v[96:111]\n"
    "s_add_u32 s76, s76, s29\n"
    "ds_read_b64_tr_b16 a[140:141], v16 offset:26880\n"
    "ds_read_b64_tr_b16 a[142:143], v17 offset:26880\n"
    "ds_read_b64_tr_b16 a[104:105], v16 offset:28672\n"
    "ds_read_b64_tr_b16 a[106:107], v17 offset:28672\n"
    "v_and_b32 v5, s25, v241\n"
    "v_lshlrev_b32 v6, 16, v242\n"
    "v_mfma_f32_32x32x16_bf16 v[32:47], a[120:123], v[192:195], v[32:47]\n"
    "s_waitcnt lgkmcnt(6)\n"
    "ds_read_b64_tr_b16 a[108:109], v16 offset:28928\n"
    "ds_read_b64_tr_b16 a[110:111], v17 offset:28928\n"
    "v_and_b32 v7, s25, v242\n"
    "v_lshlrev_b32 v8, 16, v243\n"
    "v_and_b32 v9, s25, v243\n"
    "v_fma_f32 v2, v2, v30, v31\n"
    "v_mfma_f32_32x32x16_bf16 v[48:63], a[124:127], v[196:199], v[48:63]\n"
    "s_mov_b32 s0, s3\n"
    "s_add_u32 s20, s69, 162816\n"
    "ds_read_b64_tr_b16 a[112:113], v16 offset:29184\n"
    "ds_read_b64_tr_b16 a[114:115], v17 offset:29184\n"
    "v_fma_f32 v3, v3, v30, v31\n"
    "v_fma_f32 v4, v4, v30, v31\n"
    "v_fma_f32 v5, v5, v30, v31\n"
    "v_mfma_f32_32x32x16_bf16 v[64:79], a[128:131], v[196:199], v[64:79]\n"
    "s_and_b32 s1, s0, 1\n"
    "ds_read_b64_tr_b16 a[116:117], v16 offset:29440\n"
    "ds_read_b64_tr_b16 a[118:119], v17 offset:29440\n"
    "buffer_load_dwordx4 v18, s[44:47], s20 offen offset:3072 lds\n"
    "v_fma_f32 v6, v6, v30, v31\n"
    "v_fma_f32 v7, v7, v30, v31\n"
    "v_mfma_f32_32x32x16_bf16 v[80:95], a[132:135], v[196:199], v[80:95]\n"
    "s_waitcnt lgkmcnt(8)\n"
    "ds_read_b64_tr_b16 a[120:121], v16 offset:27136\n"
    "ds_read_b64_tr_b16 a[122:123], v17 offset:27136\n"
    "v_fma_f32 v8, v8, v30, v31\n"
    "v_fma_f32 v9, v9, v30, v31\n"
    "v_cvt_pk_bf16_f32 v240, v2, v3\n"
    "v_cvt_pk_bf16_f32 v241, v4, v5\n"
    "v_mfma_f32_32x32x16_bf16 v[96:111], a[136:139], v[196:199], v[96:111]\n"
    "s_mul_i32 s73, s0, 9\n"
    "ds_read_b64_tr_b16 a[124:125], v16 offset:29696\n"
    "ds_read_b64_tr_b16 a[126:127], v17 offset:29696\n"
    "v_cvt_pk_bf16_f32 v242, v6, v7\n"
    "v_cvt_pk_bf16_f32 v243, v8, v9\n"
    "v_lshlrev_b32 v2, 16, v244\n"
    "v_and_b32 v3, s25, v244\n"
    "v_mfma_f32_32x32x16_bf16 v[32:47], a[140:143], v[196:199], v[32:47]\n"
    "s_waitcnt lgkmcnt(6)\n"
    "s_cmp_eq_u32 s11, 1\n"
    "s_cselect_b32 s20, 4293197824, s98\n"
    "s_add_u32 s69, s69, s20\n"
    "s_add_u32 m0, s68, 8192\n"
    "ds_read_b64_tr_b16 a[128:129], v16 offset:29952\n"
    "ds_read_b64_tr_b16 a[130:131], v17 offset:29952\n"
    "v_mfma_f32_32x32x16_bf16 v[48:63], a[104:107], v[200:203], v[48:63]\n"
    "s_add_u32 s73, s73, s1\n"
    "ds_read_b64_tr_b16 a[132:133], v16 offset:30208\n"
    "ds_read_b64_tr_b16 a[134:135], v17 offset:30208\n"
    "buffer_load_dwordx4 v18, s[44:47], s69 offen lds\n"
    "v_lshlrev_b32 v4, 16, v245\n"
    "v_and_b32 v5, s25, v245\n"
    "v_mfma_f32_32x32x16_bf16 v[64:79], a[108:111], v[200:203], v[64:79]\n"
    "s_mul_i32 s73, s73, 36864\n"
    "ds_read_b64_tr_b16 a[136:137], v16 offset:30464\n"
    "ds_read_b64_tr_b16 a[138:139], v17 offset:30464\n"
    "v_lshlrev_b32 v6, 16, v246\n"
    "v_and_b32 v7, s25, v246\n"
    "v_lshlrev_b32 v8, 16, v247\n"
    "v_and_b32 v9, s25, v247\n"
    "v_mfma_f32_32x32x16_bf16 v[80:95], a[112:115], v[200:203], v[80:95]\n"
    "s_waitcnt lgkmcnt(8)\n"
    "ds_read_b64_tr_b16 a[140:141], v16 offset:27392\n"
    "ds_read_b64_tr_b16 a[142:143], v17 offset:27392\n"
    "v_fma_f32 v2, v2, v30, v31\n"
    "v_fma_f32 v3, v3, v30, v31\n"
    "v_fma_f32 v4, v4, v30, v31\n"
    "v_fma_f32 v5, v5, v30, v31\n"
    "v_fma_f32 v6, v6, v30, v31\n"
    "v_mfma_f32_32x32x16_bf16 v[96:111], a[116:119], v[200:203], v[96:111]\n"
    "s_add_u32 s73, s73, s76\n"
    "ds_read_b64_tr_b16 a[104:105], v16 offset:30720\n"
    "ds_read_b64_tr_b16 a[106:107], v17 offset:30720\n"
    "v_fma_f32 v7, v7, v30, v31\n"
    "v_fma_f32 v8, v8, v30, v31\n"
    "v_fma_f32 v9, v9, v30, v31\n"
    "v_cvt_pk_bf16_f32 v244, v2, v3\n"
    "v_mfma_f32_32x32x16_bf16 v[32:47], a[120:123], v[200:203], v[32:47]\n"
    "s_waitcnt lgkmcnt(6)\n"
    "s_cmp_eq_u32 s11, 1\n"
    "s_cselect_b32 s20, 4290248704, s81\n"
    "s_add_u32 s72, s72, s20\n"
    "s_add_u32 m0, s68, 9216\n"
    "ds_read_b64_tr_b16 a[108:109], v16 offset:30976\n"
    "ds_read_b64_tr_b16 a[110:111], v17 offset:30976\n"
    "v_mfma_f32_32x32x16_bf16 v[48:63], a[124:127], v[204:207], v[48:63]\n"
    "s_mul_i32 s29, s1, 331776\n"
    "ds_read_b64_tr_b16 a[112:113], v16 offset:31232\n"
    "ds_read_b64_tr_b16 a[114:115], v17 offset:31232\n"
    "buffer_load_dwordx4 v20, s[48:51], s72 offen lds\n"
    "v_cvt_pk_bf16_f32 v245, v4, v5\n"
    "v_cvt_pk_bf16_f32 v246, v6, v7\n"
    "v_mfma_f32_32x32x16_bf16 v[64:79], a[128:131], v[204:207], v[64:79]\n"
    "s_add_u32 s77, s73, 294912\n"
    "ds_read_b64_tr_b16 a[116:117], v16 offset:31488\n"
    "ds_read_b64_tr_b16 a[118:119], v17 offset:31488\n"
    "v_cvt_pk_bf16_f32 v247, v8, v9\n"
    "v_lshlrev_b32 v2, 16, v248\n"
    "v_and_b32 v3, s25, v248\n"
    "v_lshlrev_b32 v4, 16, v249\n"
    "v_mfma_f32_32x32x16_bf16 v[80:95], a[132:135], v[204:207], v[80:95]\n"
    "s_waitcnt vmcnt(7) lgkmcnt(8)\n"
    "s_barrier\n"
    "v_mfma_f32_32x32x16_bf16 v[96:111], a[136:139], v[204:207], v[96:111]\n"
    "s_sub_u32 s77, s77, s29\n"
    "s_add_u32 s20, s69, 54272\n"
    "ds_read_b64_tr_b16 a[120:121], v16 offset:31744\n"
    "ds_read_b64_tr_b16 a[122:123], v17 offset:31744\n"
    "ds_read_b64_tr_b16 a[124:125], v16\n"
    "ds_read_b64_tr_b16 a[126:127], v17\n"
    "v_and_b32 v5, s25, v249\n"
    "v_mfma_f32_32x32x16_bf16 v[32:47], a[140:143], v[204:207], v[32:47]\n"
    "s_waitcnt lgkmcnt(6)\n"
    "ds_read_b64_tr_b16 a[128:129], v16 offset:256\n"
    "ds_read_b64_tr_b16 a[130:131], v17 offset:256\n"
    "buffer_load_dwordx4 v18, s[44:47], s20 offen offset:1024 lds\n"
    "v_lshlrev_b32 v6, 16, v250\n"
    "v_and_b32 v7, s25, v250\n"
    "v_mfma_f32_32x32x16_bf16 v[48:63], a[104:107], v[208:211], v[48:63]\n"
    "s_xor_b32 s0, s3, 1\n"
    "ds_read_b64_tr_b16 a[132:133], v16 offset:512\n"
    "ds_read_b64_tr_b16 a[134:135], v17 offset:512\n"
    "v_lshlrev_b32 v8, 16, v251\n"
    "v_and_b32 v9, s25, v251\n"
    "v_fma_f32 v2, v2, v30, v31\n"
    "v_fma_f32 v3, v3, v30, v31\n"
    "v_mfma_f32_32x32x16_bf16 v[64:79], a[108:111], v[208:211], v[64:79]\n"
    "s_and_b32 s1, s0, 1\n"
    "ds_read_b64_tr_b16 a[136:137], v16 offset:768\n"
    "ds_read_b64_tr_b16 a[138:139], v17 offset:768\n"
    "v_fma_f32 v4, v4, v30, v31\n"
    "v_fma_f32 v5, v5, v30, v31\n"
    "v_fma_f32 v6, v6, v30, v31\n"
    "v_fma_f32 v7, v7, v30, v31\n"
    "v_mfma_f32_32x32x16_bf16 v[80:95], a[112:115], v[208:211], v[80:95]\n"
    "s_waitcnt lgkmcnt(8)\n"
    "s_add_u32 s20, s69, 108544\n"
    "ds_read_b64_tr_b16 a[140:141], v16 offset:32000\n"
    "ds_read_b64_tr_b16 a[142:143], v17 offset:32000\n"
    "v_fma_f32 v8, v8, v30, v31\n"
    "v_fma_f32 v9, v9, v30, v31\n"
    "v_cvt_pk_bf16_f32 v248, v2, v3\n"
    "v_mfma_f32_32x32x16_bf16 v[96:111], a[116:119], v[208:211], v[96:111]\n"
    "s_mul_i32 s74, s0, 9\n"
    "ds_read_b64_tr_b16 a[104:105], v16 offset:1024\n"
    "ds_read_b64_tr_b16 a[106:107], v17 offset:1024\n"
    "buffer_load_dwordx4 v18, s[44:47], s20 offen offset:2048 lds\n"
    "v_cvt_pk_bf16_f32 v249, v4, v5\n"
    "v_cvt_pk_bf16_f32 v250, v6, v7\n"
    "v_mfma_f32_32x32x16_bf16 v[32:47], a[120:123], v[208:211], v[32:47]\n"
    "s_waitcnt lgkmcnt(6)\n"
    "ds_read_b64_tr_b16 a[108:109], v16 offset:1280\n"
    "ds_read_b64_tr_b16 a[110:111], v17 offset:1280\n"
    "v_cvt_pk_bf16_f32 v251, v8, v9\n"
    "v_lshlrev_b32 v2, 16, v252\n"
    "v_and_b32 v3, s25, v252\n"
    "v_lshlrev_b32 v4, 16, v253\n"
    "v_mfma_f32_32x32x16_bf16 v[48:63], a[124:127], v[212:215], v[48:63]\n"
    "s_add_u32 s74, s74, s1\n"
    "ds_read_b64_tr_b16 a[112:113], v16 offset:1536\n"
    "ds_read_b64_tr_b16 a[114:115], v17 offset:1536\n"
    "v_and_b32 v5, s25, v253\n"
    "v_lshlrev_b32 v6, 16, v254\n"
    "v_and_b32 v7, s25, v254\n"
    "v_lshlrev_b32 v8, 16, v255\n"
    "v_and_b32 v9, s25, v255\n"
    "v_mfma_f32_32x32x16_bf16 v[64:79], a[128:131], v[212:215], v[64:79]\n"
    "s_mul_i32 s74, s74, 36864\n"
    "ds_read_b64_tr_b16 a[116:117], v16 offset:1792\n"
    "ds_read_b64_tr_b16 a[118:119], v17 offset:1792\n"
    "v_fma_f32 v2, v2, v30, v31\n"
    "v_fma_f32 v3, v3, v30, v31\n"
    "v_fma_f32 v4, v4, v30, v31\n"
    "v_fma_f32 v5, v5, v30, v31\n"
    "v_mfma_f32_32x32x16_bf16 v[80:95], a[132:135], v[212:215], v[80:95]\n"
    "s_waitcnt lgkmcnt(8)\n"
    "s_add_u32 s20, s69, 162816\n"
    "ds_read_b64_tr_b16 a[120:121], v16 offset:32256\n"
    "ds_read_b64_tr_b16 a[122:123], v17 offset:32256\n"
    "v_fma_f32 v6, v6, v30, v31\n"
    "v_fma_f32 v7, v7, v30, v31\n"
    "v_fma_f32 v8, v8, v30, v31\n"
    "v_mfma_f32_32x32x16_bf16 v[96:111], a[136:139], v[212:215], v[96:111]\n"
    "s_add_u32 s74, s74, s76\n"
    "ds_read_b64_tr_b16 a[124:125], v16 offset:2048\n"
    "ds_read_b64_tr_b16 a[126:127], v17 offset:2048\n"
    "buffer_load_dwordx4 v18, s[44:47], s20 offen offset:3072 lds\n"
    "v_fma_f32 v9, v9, v30, v31\n"
    "v_cvt_pk_bf16_f32 v252, v2, v3\n"
    "v_mfma_f32_32x32x16_bf16 v[32:47], a[140:143], v[212:215], v[32:47]\n"
    "s_waitcnt lgkmcnt(6)\n"
    "ds_read_b64_tr_b16 a[128:129], v16 offset:2304\n"
    "ds_read_b64_tr_b16 a[130:131], v17 offset:2304\n"
    "v_cvt_pk_bf16_f32 v253, v4, v5\n"
    "v_cvt_pk_bf16_f32 v254, v6, v7\n"
    "v_cvt_pk_bf16_f32 v255, v8, v9\n"
    "v_add_u32 v29, 2654435761, v29\n"
    "v_mfma_f32_32x32x16_bf16 v[48:63], a[104:107], v[216:219], v[48:63]\n"
    "s_mul_i32 s29, s1, 331776\n"
    "ds_read_b64_tr_b16 a[132:133], v16 offset:2560\n"
    "ds_read_b64_tr_b16 a[134:135], v17 offset:2560\n"
    "v_lshrrev_b32 v13, 3, v25\n"
    "v_add_u32 v13, s68, v13\n"
    "v_accvgpr_write_b32 a72, v184\n"
    "v_accvgpr_write_b32 a73, v185\n"
    "v_mfma_f32_32x32x16_bf16 v[64:79], a[108:111], v[216:219], v[64:79]\n"
    "s_add_u32 s78, s74, 294912\n"
    "s_add_u32 m0, s68, 13312\n"
    "ds_read_b64_tr_b16 a[136:137], v16 offset:2816\n"
    "ds_read_b64_tr_b16 a[138:139], v17 offset:2816\n"
    "ds_write_b16 v13, v15 offset:40832\n"
    "v_mfma_f32_32x32x16_bf16 v[80:95], a[112:115], v[216:219], v[80:95]\n"
    "s_waitcnt vmcnt(6) lgkmcnt(9)\n"
    "s_barrier\n"
    "v_mfma_f32_32x32x16_bf16 v[96:111], a[116:119], v[216:219], v[96:111]\n"
    "s_sub_u32 s78, s78, s29\n"
    "ds_read_b64_tr_b16 a[140:141], v16 offset:32512\n"
    "ds_read_b64_tr_b16 a[142:143], v17 offset:32512\n"
    "ds_read_b64_tr_b16 a[104:105], v16 offset:3072\n"
    "ds_read_b64_tr_b16 a[106:107], v17 offset:3072\n"
    "buffer_load_dwordx4 v19, s[48:51], s70 offen lds\n"
    "v_mfma_f32_32x32x16_bf16 v[32:47], a[120:123], v[216:219], v[32:47]\n"
    "s_waitcnt lgkmcnt(7)\n"
    "s_add_u32 s20, s70, 146432\n"
    "ds_read_b64_tr_b16 a[108:109], v16 offset:3328\n"
    "ds_read_b64_tr_b16 a[110:111], v17 offset:3328\n"
    "buffer_load_dwordx4 v19, s[48:51], s20 offen offset:1024 lds\n"
    "v_accvgpr_write_b32 a74, v186\n"
    "v_mfma_f32_32x32x16_bf16 v[48:63], a[124:127], v[220:223], v[48:63]\n"
    "s_mul_i32 s84, s0, 73728\n"
    "s_add_u32 s20, s70, 292864\n"
    "ds_read_b64_tr_b16 a[112:113], v16 offset:3584\n"
    "ds_read_b64_tr_b16 a[114:115], v17 offset:3584\n"
    "v_accvgpr_write_b32 a75, v187\n"
    "v_accvgpr_write_b32 a76, v188\n"
    "v_accvgpr_write_b32 a77, v189\n"
    "v_mfma_f32_32x32x16_bf16 v[64:79], a[128:131], v[220:223], v[64:79]\n"
    "s_add_u32 s84, s84, s86\n"
    "ds_read_b64_tr_b16 a[116:117], v16 offset:3840\n"
    "ds_read_b64_tr_b16 a[118:119], v17 offset:3840\n"
    "buffer_load_dwordx4 v19, s[48:51], s20 offen offset:2048 lds\n"
    "v_accvgpr_write_b32 a78, v190\n"
    "v_accvgpr_write_b32 a79, v191\n"
    "v_mfma_f32_32x32x16_bf16 v[80:95], a[132:135], v[220:223], v[80:95]\n"
    "s_waitcnt lgkmcnt(8)\n"
    "s_add_u32 s20, s70, 439296\n"
    "ds_read_b64_tr_b16 a[120:121], v16 offset:4096\n"
    "ds_read_b64_tr_b16 a[122:123], v17 offset:4096\n"
    "v_accvgpr_write_b32 a0, v112\n"
    "v_accvgpr_write_b32 a1, v113\n"
    "v_accvgpr_write_b32 a2, v114\n"
    "v_accvgpr_write_b32 a3, v115\n"
    "v_mfma_f32_32x32x16_bf16 v[96:111], a[136:139], v[220:223], v[96:111]\n"
    "s_xor_b32 s0, s3, 2\n"
    "ds_read_b64_tr_b16 a[124:125], v16 offset:5120\n"
    "ds_read_b64_tr_b16 a[126:127], v17 offset:5120\n"
    "buffer_load_dwordx4 v19, s[48:51], s20 offen offset:3072 lds\n"
    "v_accvgpr_write_b32 a4, v116\n"
    "v_accvgpr_write_b32 a5, v117\n"
    "v_mfma_f32_32x32x16_bf16 v[32:47], a[140:143], v[220:223], v[32:47]\n"
    "s_waitcnt lgkmcnt(6)\n"
    "s_cmp_eq_u32 s11, 8\n"
    "s_cselect_b32 s20, 4290248704, s81\n"
    "s_add_u32 s70, s70, s20\n"
    "s_add_u32 m0, s68, 17408\n"
    "ds_read_b64_tr_b16 a[128:129], v16 offset:5376\n"
    "ds_read_b64_tr_b16 a[130:131], v17 offset:5376\n"
    "v_mfma_f32_32x32x16_bf16 v[48:63], a[104:107], v[224:227], v[48:63]\n"
    "s_and_b32 s1, s0, 1\n"
    "ds_read_b64_tr_b16 a[132:133], v16 offset:5632\n"
    "ds_read_b64_tr_b16 a[134:135], v17 offset:5632\n"
    "buffer_load_dwordx4 v19, s[48:51], s70 offen lds\n"
    "v_accvgpr_write_b32 a6, v118\n"
    "v_accvgpr_write_b32 a7, v119\n"
    "v_mfma_f32_32x32x16_bf16 v[64:79], a[108:111], v[224:227], v[64:79]\n"
    "s_mul_i32 s75, s0, 9\n"
    "s_add_u32 s20, s70, 146432\n"
    "ds_read_b64_tr_b16 a[136:137], v16 offset:5888\n"
    "ds_read_b64_tr_b16 a[138:139], v17 offset:5888\n"
    "v_accvgpr_write_b32 a8, v120\n"
    "v_accvgpr_write_b32 a9, v121\n"
    "v_accvgpr_write_b32 a10, v122\n"
    "v_mfma_f32_32x32x16_bf16 v[80:95], a[112:115], v[224:227], v[80:95]\n"
    "s_waitcnt lgkmcnt(8)\n"
    "ds_read_b64_tr_b16 a[140:141], v16 offset:4352\n"
    "ds_read_b64_tr_b16 a[142:143], v17 offset:4352\n"
    "buffer_load_dwordx4 v19, s[48:51], s20 offen offset:1024 lds\n"
    "v_accvgpr_write_b32 a11, v123\n"
    "v_accvgpr_write_b32 a12, v124\n"
    "v_mfma_f32_32x32x16_bf16 v[96:111], a[116:119], v[224:227], v[96:111]\n"
    "s_add_u32 s75, s75, s1\n"
    "s_add_u32 s20, s70, 292864\n"
    "ds_read_b64_tr_b16 a[104:105], v16 offset:6144\n"
    "ds_read_b64_tr_b16 a[106:107], v17 offset:6144\n"
    "v_accvgpr_write_b32 a13, v125\n"
    "v_accvgpr_write_b32 a14, v126\n"
    "v_accvgpr_write_b32 a15, v127\n"
    "v_mfma_f32_32x32x16_bf16 v[32:47], a[120:123], v[224:227], v[32:47]\n"
    "s_waitcnt lgkmcnt(6)\n"
    "ds_read_b64_tr_b16 a[108:109], v16 offset:6400\n"
    "ds_read_b64_tr_b16 a[110:111], v17 offset:6400\n"
    "buffer_load_dwordx4 v19, s[48:51], s20 offen offset:2048 lds\n"
    "v_accvgpr_write_b32 a16, v128\n"
    "v_accvgpr_write_b32 a17, v129\n"
    "v_mfma_f32_32x32x16_bf16 v[48:63], a[124:127], v[228:231], v[48:63]\n"
    "s_mul_i32 s75, s75, 36864\n"
    "s_add_u32 s20, s70, 439296\n"
    "ds_read_b64_tr_b16 a[112:113], v16 offset:6656\n"
    "ds_read_b64_tr_b16 a[114:115], v17 offset:6656\n"
    "v_accvgpr_write_b32 a18, v130\n"
    "v_accvgpr_write_b32 a19, v131\n"
    "v_accvgpr_write_b32 a20, v132\n"
    "v_mfma_f32_32x32x16_bf16 v[64:79], a[128:131], v[228:231], v[64:79]\n"
    "s_add_u32 s75, s75, s76\n"
    "ds_read_b64_tr_b16 a[116:117], v16 offset:6912\n"
    "ds_read_b64_tr_b16 a[118:119], v17 offset:6912\n"
    "buffer_load_dwordx4 v19, s[48:51], s20 offen offset:3072 lds\n"
    "v_accvgpr_write_b32 a21, v133\n"
    "v_accvgpr_write_b32 a22, v134\n"
    "v_mfma_f32_32x32x16_bf16 v[80:95], a[132:135], v[228:231], v[80:95]\n"
    "s_waitcnt vmcnt(9) lgkmcnt(8)\n"
    "s_barrier\n"
    "v_mfma_f32_32x32x16_bf16 v[96:111], a[136:139], v[228:231], v[96:111]\n"
    "s_mul_i32 s29, s1, 331776\n"
    "s_cmp_eq_u32 s11, 7\n"
    "s_cselect_b32 s20, 4290248704, s81\n"
    "ds_read_b64_tr_b16 a[120:121], v16 offset:4608\n"
    "ds_read_b64_tr_b16 a[122:123], v17 offset:4608\n"
    "ds_read_b64_tr_b16 a[124:125], v16 offset:7168\n"
    "ds_read_b64_tr_b16 a[126:127], v17 offset:7168\n"
    "v_mfma_f32_32x32x16_bf16 v[32:47], a[140:143], v[228:231], v[32:47]\n"
    "s_waitcnt lgkmcnt(6)\n"
    "s_add_u32 s70, s70, s20\n"
    "s_add_u32 m0, s68, 21504\n"
    "ds_read_b64_tr_b16 a[128:129], v16 offset:7424\n"
    "ds_read_b64_tr_b16 a[130:131], v17 offset:7424\n"
    "buffer_load_dwordx4 v19, s[48:51], s70 offen lds\n"
    "v_mfma_f32_32x32x16_bf16 v[48:63], a[104:107], v[232:235], v[48:63]\n"
    "s_add_u32 s79, s75, 294912\n"
    "s_add_u32 s20, s70, 146432\n"
    "ds_read_b64_tr_b16 a[132:133], v16 offset:7680\n"
    "ds_read_b64_tr_b16 a[134:135], v17 offset:7680\n"
    "buffer_load_dwordx4 v19, s[48:51], s20 offen offset:1024 lds\n"
    "v_accvgpr_write_b32 a23, v135\n"
    "v_accvgpr_write_b32 a24, v136\n"
    "v_mfma_f32_32x32x16_bf16 v[64:79], a[108:111], v[232:235], v[64:79]\n"
    "s_sub_u32 s79, s79, s29\n"
    "s_add_u32 s20, s70, 292864\n"
    "ds_read_b64_tr_b16 a[136:137], v16 offset:7936\n"
    "ds_read_b64_tr_b16 a[138:139], v17 offset:7936\n"
    "v_accvgpr_write_b32 a25, v137\n"
    "v_accvgpr_write_b32 a26, v138\n"
    "v_accvgpr_write_b32 a27, v139\n"
    "v_mfma_f32_32x32x16_bf16 v[80:95], a[112:115], v[232:235], v[80:95]\n"
    "s_waitcnt lgkmcnt(8)\n"
    "ds_read_b64_tr_b16 a[140:141], v16 offset:4864\n"
    "ds_read_b64_tr_b16 a[142:143], v17 offset:4864\n"
    "buffer_load_dwordx4 v19, s[48:51], s20 offen offset:2048 lds\n"
    "v_accvgpr_write_b32 a28, v140\n"
    "v_accvgpr_write_b32 a29, v141\n"
    "v_mfma_f32_32x32x16_bf16 v[96:111], a[116:119], v[232:235], v[96:111]\n"
    "s_mul_i32 s85, s0, 73728\n"
    "s_add_u32 s20, s70, 439296\n"
    "ds_read_b64_tr_b16 a[104:105], v16 offset:8192\n"
    "ds_read_b64_tr_b16 a[106:107], v17 offset:8192\n"
    "v_accvgpr_write_b32 a30, v142\n"
    "v_accvgpr_write_b32 a31, v143\n"
    "v_accvgpr_write_b32 a32, v144\n"
    "v_mfma_f32_32x32x16_bf16 v[32:47], a[120:123], v[232:235], v[32:47]\n"
    "s_waitcnt lgkmcnt(6)\n"
    "ds_read_b64_tr_b16 a[108:109], v16 offset:8448\n"
    "ds_read_b64_tr_b16 a[110:111], v17 offset:8448\n"
    "buffer_load_dwordx4 v19, s[48:51], s20 offen offset:3072 lds\n"
    "v_accvgpr_write_b32 a33, v145\n"
    "v_accvgpr_write_b32 a34, v146\n"
    "v_mfma_f32_32x32x16_bf16 v[48:63], a[124:127], v[236:239], v[48:63]\n"
    "s_add_u32 s85, s85, s86\n"
    "s_cmp_eq_u32 s11, 6\n"
    "s_cselect_b32 s20, 4290248704, s81\n"
    "s_add_u32 s70, s70, s20\n"
    "s_add_u32 m0, s68, 25600\n"
    "ds_read_b64_tr_b16 a[112:113], v16 offset:8704\n"
    "ds_read_b64_tr_b16 a[114:115], v17 offset:8704\n"
    "v_mfma_f32_32x32x16_bf16 v[64:79], a[128:131], v[236:239], v[64:79]\n"
    "s_xor_b32 s0, s3, 3\n"
    "ds_read_b64_tr_b16 a[116:117], v16 offset:8960\n"
    "ds_read_b64_tr_b16 a[118:119], v17 offset:8960\n"
    "buffer_load_dwordx4 v19, s[48:51], s70 offen lds\n"
    "v_accvgpr_write_b32 a35, v147\n"
    "v_accvgpr_write_b32 a36, v148\n"
    "v_mfma_f32_32x32x16_bf16 v[80:95], a[132:135], v[236:239], v[80:95]\n"
    "s_waitcnt lgkmcnt(8)\n"
    "s_add_u32 s20, s70, 146432\n"
    "ds_read_b64_tr_b16 a[120:121], v16 offset:9216\n"
    "ds_read_b64_tr_b16 a[122:123], v17 offset:9216\n"
    "v_accvgpr_write_b32 a37, v149\n"
    "v_accvgpr_write_b32 a38, v150\n"
    "v_accvgpr_write_b32 a39, v151\n"
    "v_mfma_f32_32x32x16_bf16 v[96:111], a[136:139], v[236:239], v[96:111]\n"
    "s_and_b32 s1, s0, 1\n"
    "ds_read_b64_tr_b16 a[124:125], v16 offset:10240\n"
    "ds_read_b64_tr_b16 a[126:127], v17 offset:10240\n"
    "buffer_load_dwordx4 v19, s[48:51], s20 offen offset:1024 lds\n"
    "v_accvgpr_write_b32 a40, v152\n"
    "v_accvgpr_write_b32 a41, v153\n"
    "v_mfma_f32_32x32x16_bf16 v[32:47], a[140:143], v[236:239], v[32:47]\n"
    "s_waitcnt lgkmcnt(6)\n"
    "s_add_u32 s20, s70, 292864\n"
    "ds_read_b64_tr_b16 a[128:129], v16 offset:10496\n"
    "ds_read_b64_tr_b16 a[130:131], v17 offset:10496\n"
    "v_accvgpr_write_b32 a42, v154\n"
    "v_accvgpr_write_b32 a43, v155\n"
    "v_accvgpr_write_b32 a44, v156\n"
    "v_mfma_f32_32x32x16_bf16 v[48:63], a[104:107], v[240:243], v[48:63]\n"
    "s_mul_i32 s29, s0, 9\n"
    "ds_read_b64_tr_b16 a[132:133], v16 offset:10752\n"
    "ds_read_b64_tr_b16 a[134:135], v17 offset:10752\n"
    "buffer_load_dwordx4 v19, s[48:51], s20 offen offset:2048 lds\n"
    "v_accvgpr_write_b32 a45, v157\n"
    "v_accvgpr_write_b32 a46, v158\n"
    "v_mfma_f32_32x32x16_bf16 v[64:79], a[108:111], v[240:243], v[64:79]\n"
    "s_add_u32 s29, s29, s1\n"
    "s_add_u32 s20, s70, 439296\n"
    "ds_read_b64_tr_b16 a[136:137], v16 offset:11008\n"
    "ds_read_b64_tr_b16 a[138:139], v17 offset:11008\n"
    "v_accvgpr_write_b32 a47, v159\n"
    "v_accvgpr_write_b32 a48, v160\n"
    "v_accvgpr_write_b32 a49, v161\n"
    "v_mfma_f32_32x32x16_bf16 v[80:95], a[112:115], v[240:243], v[80:95]\n"
    "s_waitcnt lgkmcnt(8)\n"
    "ds_read_b64_tr_b16 a[140:141], v16 offset:9472\n"
    "ds_read_b64_tr_b16 a[142:143], v17 offset:9472\n"
    "buffer_load_dwordx4 v19, s[48:51], s20 offen offset:3072 lds\n"
    "v_accvgpr_write_b32 a50, v162\n"
    "v_accvgpr_write_b32 a51, v163\n"
    "v_accvgpr_write_b32 a52, v164\n"
    "v_mfma_f32_32x32x16_bf16 v[96:111], a[116:119], v[240:243], v[96:111]\n"
    "s_mul_i32 s29, s29, 36864\n"
    "s_cmp_eq_u32 s11, 5\n"
    "s_cselect_b32 s20, 4290248704, s81\n"
    "s_add_u32 s70, s70, s20\n"
    "s_add_u32 m0, s68, 29696\n"
    "ds_read_b64_tr_b16 a[104:105], v16 offset:11264\n"
    "ds_read_b64_tr_b16 a[106:107], v17 offset:11264\n"
    "v_mfma_f32_32x32x16_bf16 v[32:47], a[120:123], v[240:243], v[32:47]\n"
    "s_waitcnt lgkmcnt(6)\n"
    "ds_read_b64_tr_b16 a[108:109], v16 offset:11520\n"
    "ds_read_b64_tr_b16 a[110:111], v17 offset:11520\n"
    "buffer_load_dwordx4 v19, s[48:51], s70 offen lds\n"
    "v_accvgpr_write_b32 a53, v165\n"
    "v_accvgpr_write_b32 a54, v166\n"
    "v_mfma_f32_32x32x16_bf16 v[48:63], a[124:127], v[244:247], v[48:63]\n"
    "s_add_u32 s76, s29, s76\n"
    "s_add_u32 s20, s70, 146432\n"
    "ds_read_b64_tr_b16 a[112:113], v16 offset:11776\n"
    "ds_read_b64_tr_b16 a[114:115], v17 offset:11776\n"
    "v_accvgpr_write_b32 a55, v167\n"
    "v_accvgpr_write_b32 a56, v168\n"
    "v_accvgpr_write_b32 a57, v169\n"
    "v_mfma_f32_32x32x16_bf16 v[64:79], a[128:131], v[244:247], v[64:79]\n"
    "s_mul_i32 s29, s1, 331776\n"
    "ds_read_b64_tr_b16 a[116:117], v16 offset:12032\n"
    "ds_read_b64_tr_b16 a[118:119], v17 offset:12032\n"
    "buffer_load_dwordx4 v19, s[48:51], s20 offen offset:1024 lds\n"
    "v_accvgpr_write_b32 a58, v170\n"
    "v_accvgpr_write_b32 a59, v171\n"
    "v_mfma_f32_32x32x16_bf16 v[80:95], a[132:135], v[244:247], v[80:95]\n"
    "s_waitcnt vmcnt(13) lgkmcnt(8)\n"
    "s_barrier\n"
    "v_mfma_f32_32x32x16_bf16 v[96:111], a[136:139], v[244:247], v[96:111]\n"
    "s_add_u32 s80, s76, 294912\n"
    "s_add_u32 s20, s70, 292864\n"
    "ds_read_b64_tr_b16 a[120:121], v16 offset:9728\n"
    "ds_read_b64_tr_b16 a[122:123], v17 offset:9728\n"
    "ds_read_b64_tr_b16 a[124:125], v16 offset:12288\n"
    "ds_read_b64_tr_b16 a[126:127], v17 offset:12288\n"
    "v_accvgpr_write_b32 a60, v172\n"
    "v_mfma_f32_32x32x16_bf16 v[32:47], a[140:143], v[244:247], v[32:47]\n"
    "s_waitcnt lgkmcnt(6)\n"
    "ds_read_b64_tr_b16 a[128:129], v16 offset:12544\n"
    "ds_read_b64_tr_b16 a[130:131], v17 offset:12544\n"
    "buffer_load_dwordx4 v19, s[48:51], s20 offen offset:2048 lds\n"
    "v_accvgpr_write_b32 a61, v173\n"
    "v_accvgpr_write_b32 a62, v174\n"
    "v_mfma_f32_32x32x16_bf16 v[48:63], a[104:107], v[248:251], v[48:63]\n"
    "s_sub_u32 s80, s80, s29\n"
    "s_add_u32 m0, s68, 0\n"
    "s_add_u32 s20, s70, 442368\n"
    "ds_read_b64_tr_b16 a[132:133], v16 offset:12800\n"
    "ds_read_b64_tr_b16 a[134:135], v17 offset:12800\n"
    "buffer_load_dwordx4 v19, s[48:51], s20 offen lds\n"
    "v_mfma_f32_32x32x16_bf16 v[64:79], a[108:111], v[248:251], v[64:79]\n"
    "s_mul_i32 s29, s0, 73728\n"
    "s_cmp_eq_u32 s11, 4\n"
    "s_cselect_b32 s20, 4290248704, s81\n"
    "s_add_u32 s70, s70, s20\n"
    "s_add_u32 m0, s68, 1024\n"
    "ds_read_b64_tr_b16 a[136:137], v16 offset:13056\n"
    "ds_read_b64_tr_b16 a[138:139], v17 offset:13056\n"
    "v_mfma_f32_32x32x16_bf16 v[80:95], a[112:115], v[248:251], v[80:95]\n"
    "s_waitcnt lgkmcnt(8)\n"
    "ds_read_b64_tr_b16 a[140:141], v16 offset:9984\n"
    "ds_read_b64_tr_b16 a[142:143], v17 offset:9984\n"
    "buffer_load_dwordx4 v19, s[48:51], s70 offen lds\n"
    "v_accvgpr_write_b32 a63, v175\n"
    "v_accvgpr_write_b32 a64, v176\n"
    "v_mfma_f32_32x32x16_bf16 v[96:111], a[116:119], v[248:251], v[96:111]\n"
    "s_add_u32 s86, s29, s86\n"
    "s_add_u32 s20, s70, 146432\n"
    "ds_read_b64_tr_b16 a[104:105], v16 offset:13312\n"
    "ds_read_b64_tr_b16 a[106:107], v17 offset:13312\n"
    "buffer_load_dwordx4 v19, s[48:51], s20 offen offset:1024 lds\n"
    "v_accvgpr_write_b32 a65, v177\n"
    "v_mfma_f32_32x32x16_bf16 v[32:47], a[120:123], v[248:251], v[32:47]\n"
    "s_waitcnt lgkmcnt(6)\n"
    "s_add_u32 s20, s70, 292864\n"
    "ds_read_b64_tr_b16 a[108:109], v16 offset:13568\n"
    "ds_read_b64_tr_b16 a[110:111], v17 offset:13568\n"
    "v_accvgpr_write_b32 a66, v178\n"
    "v_accvgpr_write_b32 a67, v179\n"
    "v_accvgpr_write_b32 a68, v180\n"
    "v_mfma_f32_32x32x16_bf16 v[48:63], a[124:127], v[252:255], v[48:63]\n"
    "ds_read_b64_tr_b16 a[112:113], v16 offset:13824\n"
    "ds_read_b64_tr_b16 a[114:115], v17 offset:13824\n"
    "buffer_load_dwordx4 v19, s[48:51], s20 offen offset:2048 lds\n"
    "v_accvgpr_write_b32 a69, v181\n"
    "v_accvgpr_write_b32 a70, v182\n"
    "v_accvgpr_write_b32 a71, v183\n"
    "v_mfma_f32_32x32x16_bf16 v[64:79], a[128:131], v[252:255], v[64:79]\n"
    "s_add_u32 s20, s70, 439296\n"
    "ds_read_b64_tr_b16 a[116:117], v16 offset:14080\n"
    "ds_read_b64_tr_b16 a[118:119], v17 offset:14080\n"
    "v_mfma_f32_32x32x16_bf16 v[80:95], a[132:135], v[252:255], v[80:95]\n"
    "s_waitcnt lgkmcnt(8)\n"
    "ds_read_b64_tr_b16 a[120:121], v16 offset:14336\n"
    "ds_read_b64_tr_b16 a[122:123], v17 offset:14336\n"
    "buffer_load_dwordx4 v19, s[48:51], s20 offen offset:3072 lds\n"
    "v_mfma_f32_32x32x16_bf16 v[96:111], a[136:139], v[252:255], v[96:111]\n"
    "s_cmp_eq_u32 s11, 3\n"
    "s_cselect_b32 s20, 4290248704, s81\n"
    "s_add_u32 s70, s70, s20\n"
    "s_add_u32 m0, s68, 5120\n"
    "ds_read_b64_tr_b16 a[124:125], v16 offset:14592\n"
    "ds_read_b64_tr_b16 a[126:127], v17 offset:14592\n"
    "v_mfma_f32_32x32x16_bf16 v[32:47], a[140:143], v[252:255], v[32:47]\n"
    "s_waitcnt lgkmcnt(4)\n"
    "ds_read_b64_tr_b16 a[128:129], v16 offset:14848\n"
    "ds_read_b64_tr_b16 a[130:131], v17 offset:14848\n"
    "buffer_load_dwordx4 v19, s[48:51], s70 offen lds\n"
    "v_mfma_f32_32x32x16_bf16 v[112:127], a[104:107], a[0:3], 0\n"
    "s_add_u32 s20, s70, 146432\n"
    "ds_read_b64_tr_b16 a[132:133], v16 offset:15104\n"
    "ds_read_b64_tr_b16 a[134:135], v17 offset:15104\n"
    "v_accvgpr_read_b32 v14, a158\n"
    "v_accvgpr_read_b32 v18, a159\n"
    "v_mfma_f32_32x32x16_bf16 v[128:143], a[108:111], a[0:3], 0\n"
    "ds_read_b64_tr_b16 a[136:137], v16 offset:15360\n"
    "ds_read_b64_tr_b16 a[138:139], v17 offset:15360\n"
    "buffer_load_dwordx4 v19, s[48:51], s20 offen offset:1024 lds\n"
    "v_cvt_pk_bf16_f32 v176, v48, v49\n"
    "v_cvt_pk_bf16_f32 v177, v50, v51\n"
    "v_cvt_pk_bf16_f32 v178, v52, v53\n"
    "v_cvt_pk_bf16_f32 v179, v54, v55\n"
    "v_cvt_pk_bf16_f32 v180, v56, v57\n"
    "v_mfma_f32_32x32x16_bf16 v[144:159], a[112:115], a[0:3], 0\n"
    "s_add_u32 s20, s70, 292864\n"
    "ds_read_b64_tr_b16 a[140:141], v16 offset:15616\n"
    "ds_read_b64_tr_b16 a[142:143], v17 offset:15616\n"
    "v_cvt_pk_bf16_f32 v181, v58, v59\n"
    "v_cvt_pk_bf16_f32 v182, v60, v61\n"
    "v_cvt_pk_bf16_f32 v183, v62, v63\n"
    "v_cvt_pk_bf16_f32 v184, v64, v65\n"
    "v_cvt_pk_bf16_f32 v185, v66, v67\n"
    "v_mfma_f32_32x32x16_bf16 v[160:175], a[116:119], a[0:3], 0\n"
    "s_waitcnt lgkmcnt(4)\n"
    "ds_read_b64_tr_b16 a[104:105], v16 offset:15872\n"
    "ds_read_b64_tr_b16 a[106:107], v17 offset:15872\n"
    "buffer_load_dwordx4 v19, s[48:51], s20 offen offset:2048 lds\n"
    "v_cvt_pk_bf16_f32 v186, v68, v69\n"
    "v_cvt_pk_bf16_f32 v187, v70, v71\n"
    "v_cvt_pk_bf16_f32 v60, v72, v73\n"
    "v_cvt_pk_bf16_f32 v61, v74, v75\n"
    "v_mfma_f32_32x32x16_bf16 v[112:127], a[120:123], a[4:7], v[112:127]\n"
    "s_add_u32 s20, s70, 439296\n"
    "ds_read_b64_tr_b16 a[108:109], v16 offset:16128\n"
    "ds_read_b64_tr_b16 a[110:111], v17 offset:16128\n"
    "buffer_load_dwordx4 v19, s[48:51], s20 offen offset:3072 lds\n"
    "v_cvt_pk_bf16_f32 v62, v76, v77\n"
    "v_cvt_pk_bf16_f32 v63, v78, v79\n"
    "v_cvt_pk_bf16_f32 v64, v80, v81\n"
    "v_mfma_f32_32x32x16_bf16 v[128:143], a[124:127], a[4:7], v[128:143]\n"
    "ds_read_b64_tr_b16 a[112:113], v16 offset:16384\n"
    "ds_read_b64_tr_b16 a[114:115], v17 offset:16384\n"
    "v_cvt_pk_bf16_f32 v65, v82, v83\n"
    "v_cvt_pk_bf16_f32 v66, v84, v85\n"
    "v_cvt_pk_bf16_f32 v67, v86, v87\n"
    "v_cvt_pk_bf16_f32 v68, v88, v89\n"
    "v_cvt_pk_bf16_f32 v69, v90, v91\n"
    "v_cvt_pk_bf16_f32 v70, v92, v93\n"
    "v_mfma_f32_32x32x16_bf16 v[144:159], a[128:131], a[4:7], v[144:159]\n"
    "ds_read_b64_tr_b16 a[116:117], v16 offset:16640\n"
    "ds_read_b64_tr_b16 a[118:119], v17 offset:16640\n"
    "v_cvt_pk_bf16_f32 v71, v94, v95\n"
    "v_cvt_pk_bf16_f32 v72, v96, v97\n"
    "v_cvt_pk_bf16_f32 v73, v98, v99\n"
    "v_cvt_pk_bf16_f32 v74, v100, v101\n"
    "v_cvt_pk_bf16_f32 v75, v102, v103\n"
    "v_mfma_f32_32x32x16_bf16 v[160:175], a[132:135], a[4:7], v[160:175]\n"
    "s_waitcnt lgkmcnt(4)\n"
    "ds_read_b64_tr_b16 a[120:121], v16 offset:16896\n"
    "ds_read_b64_tr_b16 a[122:123], v17 offset:16896\n"
    "v_cvt_pk_bf16_f32 v76, v104, v105\n"
    "v_cvt_pk_bf16_f32 v77, v106, v107\n"
    "v_accvgpr_read_b32 v188, a145\n"
    "v_accvgpr_read_b32 v189, a146\n"
    "v_accvgpr_read_b32 v190, a144\n"
    "v_mov_b32 v191, 1065369472\n"
    "v_mfma_f32_32x32x16_bf16 v[112:127], a[136:139], a[8:11], v[112:127]\n"
    "ds_read_b64_tr_b16 a[124:125], v16 offset:17152\n"
    "ds_read_b64_tr_b16 a[126:127], v17 offset:17152\n"
    "v_cndmask_b32 v76, v191, v76, s[26:27]\n"
    "v_cndmask_b32 v77, v191, v77, s[26:27]\n"
    "v_exp_f32 v2, v32\n"
    "v_mfma_f32_32x32x16_bf16 v[128:143], a[140:143], a[8:11], v[128:143]\n"
    "ds_read_b64_tr_b16 a[128:129], v16 offset:17408\n"
    "ds_read_b64_tr_b16 a[130:131], v17 offset:17408\n"
    "v_exp_f32 v3, v33\n"
    "v_exp_f32 v4, v34\n"
    "v_mfma_f32_32x32x16_bf16 v[80:95], a[80:83], v[60:63], 0\n"
    "v_exp_f32 v5, v35\n"
    "v_add_f32 v2, 1.0, v2\n"
    "v_add_f32 v3, 1.0, v3\n"
    "v_add_f32 v4, 1.0, v4\n"
    "v_add_f32 v5, 1.0, v5\n"
    "v_mfma_f32_32x32x16_bf16 v[144:159], a[104:107], a[8:11], v[144:159]\n"
    "ds_read_b64_tr_b16 a[132:133], v16 offset:17664\n"
    "ds_read_b64_tr_b16 a[134:135], v17 offset:17664\n"
    "v_rcp_f32 v2, v2\n"
    "v_rcp_f32 v3, v3\n"
    "v_mfma_f32_32x32x16_bf16 v[80:95], a[84:87], v[64:67], v[80:95]\n"
    "v_rcp_f32 v4, v4\n"
    "v_rcp_f32 v5, v5\n"
    "v_mul_f32 v2, v32, v2\n"
    "v_mul_f32 v3, v33, v3\n"
    "v_mfma_f32_32x32x16_bf16 v[160:175], a[108:111], a[8:11], v[160:175]\n"
    "s_waitcnt vmcnt(17) lgkmcnt(4)\n"
    "s_barrier\n"
    "s_cmp_eq_u32 s11, 2\n"
    "s_cselect_b32 s20, 4290248704, s81\n"
    "s_add_u32 s70, s70, s20\n"
    "s_add_u32 m0, s68, 9216\n"
    "v_mfma_f32_32x32x16_bf16 v[80:95], a[88:91], v[68:71], v[80:95]\n"
    "buffer_load_dwordx4 v19, s[48:51], s70 offen lds\n"
    "v_mul_f32 v4, v34, v4\n"
    "v_mul_f32 v5, v35, v5\n"
    "v_cvt_pk_bf16_f32 v32, v2, v3\n"
    "v_cvt_pk_bf16_f32 v33, v4, v5\n"
    "v_mfma_f32_32x32x16_bf16 v[112:127], a[112:115], a[12:15], v[112:127]\n"
    "s_add_u32 s20, s70, 146432\n"
    "ds_read_b64_tr_b16 a[136:137], v16 offset:17920\n"
    "ds_read_b64_tr_b16 a[138:139], v17 offset:17920\n"
    "ds_read_b64_tr_b16 a[140:141], v16 offset:18176\n"
    "ds_read_b64_tr_b16 a[142:143], v17 offset:18176\n"
    "buffer_load_dwordx4 v19, s[48:51], s20 offen offset:1024 lds\n"
    "v_mfma_f32_32x32x16_bf16 v[96:111], a[92:95], v[60:63], 0\n"
    "v_exp_f32 v6, v36\n"
    "v_exp_f32 v7, v37\n"
    "v_exp_f32 v8, v38\n"
    "v_mfma_f32_32x32x16_bf16 v[128:143], a[116:119], a[12:15], v[128:143]\n"
    "s_add_u32 s20, s70, 292864\n"
    "ds_read_b64_tr_b16 a[104:105], v16 offset:18432\n"
    "ds_read_b64_tr_b16 a[106:107], v17 offset:18432\n"
    "buffer_load_dwordx4 v19, s[48:51], s20 offen offset:2048 lds\n"
    "v_exp_f32 v9, v39\n"
    "v_mfma_f32_32x32x16_bf16 v[96:111], a[96:99], v[64:67], v[96:111]\n"
    "v_add_f32 v6, 1.0, v6\n"
    "v_add_f32 v7, 1.0, v7\n"
    "v_add_f32 v8, 1.0, v8\n"
    "v_add_f32 v9, 1.0, v9\n"
    "v_rcp_f32 v6, v6\n"
    "v_mfma_f32_32x32x16_bf16 v[144:159], a[120:123], a[12:15], v[144:159]\n"
    "s_add_u32 s20, s70, 439296\n"
    "ds_read_b64_tr_b16 a[108:109], v16 offset:18688\n"
    "ds_read_b64_tr_b16 a[110:111], v17 offset:18688\n"
    "buffer_load_dword a252, v0, s[56:59], s82 offen\n"
    "buffer_load_dwordx4 v19, s[48:51], s20 offen offset:3072 lds\n"
    "v_mfma_f32_32x32x16_bf16 v[96:111], a[100:103], v[68:71], v[96:111]\n"
    "v_rcp_f32 v7, v7\n"
    "v_rcp_f32 v8, v8\n"
    "v_rcp_f32 v9, v9\n"
    "v_mfma_f32_32x32x16_bf16 v[160:175], a[124:127], a[12:15], v[160:175]\n"
    "s_waitcnt lgkmcnt(4)\n"
    "s_cmp_eq_u32 s11, 1\n"
    "s_cselect_b32 s20, 4290248704, s81\n"
    "s_add_u32 s70, s70, s20\n"
    "s_add_u32 m0, s68, 13312\n"
    "ds_read_b64_tr_b16 a[112:113], v16 offset:18944\n"
    "ds_read_b64_tr_b16 a[114:115], v17 offset:18944\n"
    "buffer_load_dword a253, v0, s[52:55], s13 offen\n"
    "buffer_load_dwordx4 v19, s[48:51], s70 offen lds\n"
    "v_mfma_f32_32x32x16_bf16 v[112:127], a[128:131], a[16:19], v[112:127]\n"
    "s_add_u32 s13, s13, 110592\n"
    "s_add_u32 s20, s70, 146432\n"
    "ds_read_b64_tr_b16 a[116:117], v16 offset:19200\n"
    "ds_read_b64_tr_b16 a[118:119], v17 offset:19200\n"
    "buffer_load_dwordx4 v19, s[48:51], s20 offen offset:1024 lds\n"
    "ds_write_b64 v188, v[186:187]\n"
    "v_mfma_f32_32x32x16_bf16 v[128:143], a[132:135], a[16:19], v[128:143]\n"
    "s_add_u32 s20, s70, 292864\n"
    "ds_read_b64_tr_b16 a[120:121], v16 offset:19456\n"
    "ds_read_b64_tr_b16 a[122:123], v17 offset:19456\n"
    "buffer_load_dword a254, v0, s[52:55], s13 offen\n"
    "buffer_load_dwordx4 v19, s[48:51], s20 offen offset:2048 lds\n"
    "v_max3_f32 v100, v80, v81, v82\n"
    "v_mfma_f32_32x32x16_bf16 v[144:159], a[136:139], a[16:19], v[144:159]\n"
    "s_add_u32 s13, s13, 110592\n"
    "ds_read_b64_tr_b16 a[124:125], v16 offset:19712\n"
    "ds_read_b64_tr_b16 a[126:127], v17 offset:19712\n"
    "v_min3_f32 v101, v80, v81, v82\n"
    "v_max3_f32 v100, v100, v83, v84\n"
    "v_min3_f32 v101, v101, v83, v84\n"
    "v_max3_f32 v100, v100, v85, v86\n"
    "v_min3_f32 v101, v101, v85, v86\n"
    "v_max3_f32 v100, v100, v87, v88\n"
    "v_mfma_f32_32x32x16_bf16 v[160:175], a[140:143], a[16:19], v[160:175]\n"
    "s_waitcnt lgkmcnt(5)\n"
    "ds_read_b64_tr_b16 a[128:129], v16 offset:19968\n"
    "ds_read_b64_tr_b16 a[130:131], v17 offset:19968\n"
    "buffer_load_dword a255, v0, s[52:55], s13 offen\n"
    "v_min3_f32 v101, v101, v87, v88\n"
    "v_max3_f32 v100, v100, v89, v90\n"
    "v_min3_f32 v101, v101, v89, v90\n"
    "v_mfma_f32_32x32x16_bf16 v[112:127], a[104:107], a[20:23], v[112:127]\n"
    "s_add_u32 s13, s13, 110592\n"
    "ds_read_b64_tr_b16 a[132:133], v16 offset:20224\n"
    "ds_read_b64_tr_b16 a[134:135], v17 offset:20224\n"
    "ds_write_b64 v188, v[70:71] offset:16\n"
    "v_max3_f32 v100, v100, v91, v92\n"
    "v_min3_f32 v101, v101, v91, v92\n"
    "v_max3_f32 v100, v100, v93, v94\n"
    "v_mfma_f32_32x32x16_bf16 v[128:143], a[108:111], a[20:23], v[128:143]\n"
    "s_lshl2_add_u32 s0, s3, s39\n"
    "ds_read_b64_tr_b16 a[136:137], v16 offset:20480\n"
    "ds_read_b64_tr_b16 a[138:139], v17 offset:20480\n"
    "v_min3_f32 v101, v101, v93, v94\n"
    "v_max3_f32 v100, v100, v95, v96\n"
    "v_min3_f32 v101, v101, v95, v96\n"
    "v_max_f32 v100, v100, v97\n"
    "v_min_f32 v101, v101, v97\n"
    "v_mfma_f32_32x32x16_bf16 v[144:159], a[112:115], a[20:23], v[144:159]\n"
    "s_lshl_b32 s0, s0, 6\n"
    "ds_read_b64_tr_b16 a[140:141], v16 offset:20736\n"
    "ds_read_b64_tr_b16 a[142:143], v17 offset:20736\n"
    "v_sub_f32 v48, v80, v100\n"
    "v_sub_f32 v80, v101, v80\n"
    "v_exp_f32 v48, v48\n"
    "v_exp_f32 v80, v80\n"
    "v_mfma_f32_32x32x16_bf16 v[160:175], a[116:119], a[20:23], v[160:175]\n"
    "s_waitcnt lgkmcnt(5)\n"
    "ds_read_b64_tr_b16 a[104:105], v16 offset:20992\n"
    "ds_read_b64_tr_b16 a[106:107], v17 offset:20992\n"
    "v_sub_f32 v49, v81, v100\n"
    "v_sub_f32 v81, v101, v81\n"
    "v_exp_f32 v49, v49\n"
    "v_mfma_f32_32x32x16_bf16 v[112:127], a[120:123], a[24:27], v[112:127]\n"
    "s_and_b32 s29, s34, 1\n"
    "ds_read_b64_tr_b16 a[108:109], v16 offset:21248\n"
    "ds_read_b64_tr_b16 a[110:111], v17 offset:21248\n"
    "ds_write_b64 v188, v[72:73] offset:32\n"
    "v_exp_f32 v81, v81\n"
    "v_sub_f32 v50, v82, v100\n"
    "v_sub_f32 v82, v101, v82\n"
    "v_mfma_f32_32x32x16_bf16 v[128:143], a[124:127], a[24:27], v[128:143]\n"
    "s_mul_i32 s29, s29, 18\n"
    "ds_read_b64_tr_b16 a[112:113], v16 offset:21504\n"
    "ds_read_b64_tr_b16 a[114:115], v17 offset:21504\n"
    "v_exp_f32 v50, v50\n"
    "v_exp_f32 v82, v82\n"
    "v_add_f32 v102, v48, v49\n"
    "v_mfma_f32_32x32x16_bf16 v[144:159], a[128:131], a[24:27], v[144:159]\n"
    "s_lshr_b32 s1, s34, 1\n"
    "ds_read_b64_tr_b16 a[116:117], v16 offset:21760\n"
    "ds_read_b64_tr_b16 a[118:119], v17 offset:21760\n"
    "v_add_f32 v103, v80, v81\n"
    "v_sub_f32 v51, v83, v100\n"
    "v_sub_f32 v83, v101, v83\n"
    "v_exp_f32 v51, v51\n"
    "v_mfma_f32_32x32x16_bf16 v[160:175], a[132:135], a[24:27], v[160:175]\n"
    "s_waitcnt lgkmcnt(5)\n"
    "ds_read_b64_tr_b16 a[120:121], v16 offset:22016\n"
    "ds_read_b64_tr_b16 a[122:123], v17 offset:22016\n"
    "v_exp_f32 v83, v83\n"
    "v_add_f32 v102, v102, v50\n"
    "v_add_f32 v103, v103, v82\n"
    "v_sub_f32 v52, v84, v100\n"
    "v_sub_f32 v84, v101, v84\n"
    "v_mfma_f32_32x32x16_bf16 v[112:127], a[136:139], a[28:31], v[112:127]\n"
    "s_lshl2_add_u32 s1, s1, s29\n"
    "ds_read_b64_tr_b16 a[124:125], v16 offset:22272\n"
    "ds_read_b64_tr_b16 a[126:127], v17 offset:22272\n"
    "ds_write_b64 v188, v[74:75] offset:48\n"
    "v_exp_f32 v52, v52\n"
    "v_mfma_f32_32x32x16_bf16 v[128:143], a[140:143], a[28:31], v[128:143]\n"
    "s_mul_i32 s1, s1, 1152\n"
    "ds_read_b64_tr_b16 a[128:129], v16 offset:22528\n"
    "ds_read_b64_tr_b16 a[130:131], v17 offset:22528\n"
    "v_exp_f32 v84, v84\n"
    "v_add_f32 v102, v102, v51\n"
    "v_add_f32 v103, v103, v83\n"
    "v_sub_f32 v53, v85, v100\n"
    "v_sub_f32 v85, v101, v85\n"
    "v_mfma_f32_32x32x16_bf16 v[144:159], a[104:107], a[28:31], v[144:159]\n"
    "s_lshr_b32 s10, s36, 1\n"
    "ds_read_b64_tr_b16 a[132:133], v16 offset:22784\n"
    "ds_read_b64_tr_b16 a[134:135], v17 offset:22784\n"
    "v_exp_f32 v53, v53\n"
    "v_exp_f32 v85, v85\n"
    "v_add_f32 v102, v102, v52\n"
    "v_add_f32 v103, v103, v84\n"
    "v_mfma_f32_32x32x16_bf16 v[160:175], a[108:111], a[28:31], v[160:175]\n"
    "s_waitcnt lgkmcnt(5)\n"
    "ds_read_b64_tr_b16 a[136:137], v16 offset:23040\n"
    "ds_read_b64_tr_b16 a[138:139], v17 offset:23040\n"
    "v_sub_f32 v54, v86, v100\n"
    "v_sub_f32 v86, v101, v86\n"
    "v_exp_f32 v54, v54\n"
    "v_mfma_f32_32x32x16_bf16 v[112:127], a[112:115], a[32:35], v[112:127]\n"
    "s_add_u32 s72, s1, s0\n"
    "ds_read_b64_tr_b16 a[140:141], v16 offset:23296\n"
    "ds_read_b64_tr_b16 a[142:143], v17 offset:23296\n"
    "ds_write_b64 v189, v[76:77]\n"
    "v_exp_f32 v86, v86\n"
    "v_add_f32 v102, v102, v53\n"
    "v_add_f32 v103, v103, v85\n"
    "v_mfma_f32_32x32x16_bf16 v[128:143], a[116:119], a[32:35], v[128:143]\n"
    "s_mov_b32 s82, 1\n"
    "ds_read_b64_tr_b16 a[104:105], v16 offset:23552\n"
    "ds_read_b64_tr_b16 a[106:107], v17 offset:23552\n"
    "v_sub_f32 v55, v87, v100\n"
    "v_sub_f32 v87, v101, v87\n"
    "v_exp_f32 v55, v55\n"
    "v_mfma_f32_32x32x16_bf16 v[144:159], a[120:123], a[32:35], v[144:159]\n"
    "s_mul_i32 s29, s10, 82944\n"
    "ds_read_b64_tr_b16 a[108:109], v16 offset:23808\n"
    "ds_read_b64_tr_b16 a[110:111], v17 offset:23808\n"
    "v_exp_f32 v87, v87\n"
    "v_add_f32 v102, v102, v54\n"
    "v_add_f32 v103, v103, v86\n"
    "v_sub_f32 v56, v88, v100\n"
    "v_sub_f32 v88, v101, v88\n"
    "v_mfma_f32_32x32x16_bf16 v[160:175], a[124:127], a[32:35], v[160:175]\n"
    "s_waitcnt vmcnt(22) lgkmcnt(5)\n"
    "s_barrier\n"
    "v_exp_f32 v56, v56\n"
    "v_mfma_f32_32x32x16_bf16 v[112:127], a[128:131], a[36:39], v[112:127]\n"
    "s_add_u32 s1, s29, s1\n"
    "ds_read_b64_tr_b16 a[112:113], v16 offset:24064\n"
    "ds_read_b64_tr_b16 a[114:115], v17 offset:24064\n"
    "ds_read_b64_tr_b16 a[116:117], v16 offset:24320\n"
    "ds_read_b64_tr_b16 a[118:119], v17 offset:24320\n"
    "v_exp_f32 v88, v88\n"
    "v_add_f32 v102, v102, v55\n"
    "v_add_f32 v103, v103, v87\n"
    "v_mfma_f32_32x32x16_bf16 v[128:143], a[132:135], a[36:39], v[128:143]\n"
    "s_and_b32 s89, s3, 1\n"
    "ds_read_b64_tr_b16 a[120:121], v16 offset:24576\n"
    "ds_read_b64_tr_b16 a[122:123], v17 offset:24576\n"
    "v_sub_f32 v57, v89, v100\n"
    "v_sub_f32 v89, v101, v89\n"
    "v_exp_f32 v57, v57\n"
    "v_mfma_f32_32x32x16_bf16 v[144:159], a[136:139], a[36:39], v[144:159]\n"
    "s_lshl_b32 s89, s89, 6\n"
    "ds_read_b64_tr_b16 a[124:125], v16 offset:24832\n"
    "ds_read_b64_tr_b16 a[126:127], v17 offset:24832\n"
    "v_exp_f32 v89, v89\n"
    "v_add_f32 v102, v102, v56\n"
    "v_add_f32 v103, v103, v88\n"
    "v_sub_f32 v58, v90, v100\n"
    "v_sub_f32 v90, v101, v90\n"
    "v_mfma_f32_32x32x16_bf16 v[160:175], a[140:143], a[36:39], v[160:175]\n"
    "s_waitcnt lgkmcnt(4)\n"
    "ds_read_b64_tr_b16 a[128:129], v16 offset:25088\n"
    "ds_read_b64_tr_b16 a[130:131], v17 offset:25088\n"
    "v_exp_f32 v58, v58\n"
    "v_exp_f32 v90, v90\n"
    "v_add_f32 v102, v102, v57\n"
    "v_add_f32 v103, v103, v89\n"
    "v_sub_f32 v59, v91, v100\n"
    "v_mfma_f32_32x32x16_bf16 v[112:127], a[104:107], a[40:43], v[112:127]\n"
    "s_add_u32 s89, s89, s1\n"
    "ds_read_b64_tr_b16 a[132:133], v16 offset:25344\n"
    "ds_read_b64_tr_b16 a[134:135], v17 offset:25344\n"
    "v_sub_f32 v91, v101, v91\n"
    "v_exp_f32 v59, v59\n"
    "v_exp_f32 v91, v91\n"
    "v_mfma_f32_32x32x16_bf16 v[128:143], a[108:111], a[40:43], v[128:143]\n"
    "s_add_u32 s89, s89, 1024\n"
    "ds_read_b64_tr_b16 a[136:137], v16 offset:25600\n"
    "ds_read_b64_tr_b16 a[138:139], v17 offset:25600\n"
    "v_add_f32 v102, v102, v58\n"
    "v_add_f32 v103, v103, v90\n"
    "v_sub_f32 v106, v92, v100\n"
    "v_sub_f32 v92, v101, v92\n"
    "v_exp_f32 v106, v106\n"
    "v_mfma_f32_32x32x16_bf16 v[144:159], a[112:115], a[40:43], v[144:159]\n"
    "s_add_u32 s87, s1, s0\n"
    "ds_read_b64_tr_b16 a[140:141], v16 offset:25856\n"
    "ds_read_b64_tr_b16 a[142:143], v17 offset:25856\n"
    "v_exp_f32 v92, v92\n"
    "v_add_f32 v102, v102, v59\n"
    "v_add_f32 v103, v103, v91\n"
    "v_sub_f32 v107, v93, v100\n"
    "v_mfma_f32_32x32x16_bf16 v[160:175], a[116:119], a[40:43], v[160:175]\n"
    "s_waitcnt lgkmcnt(4)\n"
    "ds_read_b64_tr_b16 a[104:105], v16 offset:26112\n"
    "ds_read_b64_tr_b16 a[106:107], v17 offset:26112\n"
    "v_sub_f32 v93, v101, v93\n"
    "v_exp_f32 v107, v107\n"
    "v_exp_f32 v93, v93\n"
    "v_add_f32 v102, v102, v106\n"
    "v_mfma_f32_32x32x16_bf16 v[112:127], a[120:123], a[44:47], v[112:127]\n"
    "s_and_b32 s88, s34, 1\n"
    "ds_read_b64_tr_b16 a[108:109], v16 offset:26368\n"
    "ds_read_b64_tr_b16 a[110:111], v17 offset:26368\n"
    "v_add_f32 v103, v103, v92\n"
    "v_sub_f32 v108, v94, v100\n"
    "v_sub_f32 v94, v101, v94\n"
    "v_exp_f32 v108, v108\n"
    "v_mfma_f32_32x32x16_bf16 v[128:143], a[124:127], a[44:47], v[128:143]\n"
    "s_mul_i32 s88, s88, 18\n"
    "ds_read_b64_tr_b16 a[112:113], v16 offset:26624\n"
    "ds_read_b64_tr_b16 a[114:115], v17 offset:26624\n"
    "v_exp_f32 v94, v94\n"
    "v_add_f32 v102, v102, v107\n"
    "v_add_f32 v103, v103, v93\n"
    "v_sub_f32 v109, v95, v100\n"
    "v_sub_f32 v95, v101, v95\n"
    "v_mfma_f32_32x32x16_bf16 v[144:159], a[128:131], a[44:47], v[144:159]\n"
    "s_add_u32 s88, s88, 16\n"
    "ds_read_b64_tr_b16 a[116:117], v16 offset:26880\n"
    "ds_read_b64_tr_b16 a[118:119], v17 offset:26880\n"
    "v_exp_f32 v109, v109\n"
    "v_exp_f32 v95, v95\n"
    "v_add_f32 v102, v102, v108\n"
    "v_mfma_f32_32x32x16_bf16 v[160:175], a[132:135], a[44:47], v[160:175]\n"
    "s_waitcnt lgkmcnt(4)\n"
    "ds_read_b64_tr_b16 a[120:121], v16 offset:27136\n"
    "ds_read_b64_tr_b16 a[122:123], v17 offset:27136\n"
    "v_add_f32 v103, v103, v94\n"
    "v_sub_f32 v110, v96, v100\n"
    "v_sub_f32 v96, v101, v96\n"
    "v_exp_f32 v110, v110\n"
    "v_mfma_f32_32x32x16_bf16 v[112:127], a[136:139], a[48:51], v[112:127]\n"
    "s_mul_i32 s88, s88, 1152\n"
    "ds_read_b64_tr_b16 a[124:125], v16 offset:27392\n"
    "ds_read_b64_tr_b16 a[126:127], v17 offset:27392\n"
    "v_exp_f32 v96, v96\n"
    "v_add_f32 v102, v102, v109\n"
    "v_add_f32 v103, v103, v95\n"
    "v_sub_f32 v111, v97, v100\n"
    "v_sub_f32 v97, v101, v97\n"
    "v_mfma_f32_32x32x16_bf16 v[128:143], a[140:143], a[48:51], v[128:143]\n"
    "s_add_u32 s88, s88, s0\n"
    "ds_read_b64_tr_b16 a[128:129], v16 offset:27648\n"
    "ds_read_b64_tr_b16 a[130:131], v17 offset:27648\n"
    "v_exp_f32 v111, v111\n"
    "v_exp_f32 v97, v97\n"
    "v_add_f32 v102, v102, v110\n"
    "v_add_f32 v103, v103, v96\n"
    "v_mfma_f32_32x32x16_bf16 v[144:159], a[104:107], a[48:51], v[144:159]\n"
    "s_lshl2_add_u32 s1, s35, s34\n"
    "ds_read_b64_tr_b16 a[132:133], v16 offset:27904\n"
    "ds_read_b64_tr_b16 a[134:135], v17 offset:27904\n"
    "v_add_f32 v102, v102, v111\n"
    "v_add_f32 v103, v103, v97\n"
    "v_rcp_f32 v101, v103\n"
    "v_mfma_f32_32x32x16_bf16 v[160:175], a[108:111], a[48:51], v[160:175]\n"
    "s_waitcnt lgkmcnt(4)\n"
    "ds_read_b64_tr_b16 a[136:137], v16 offset:28160\n"
    "ds_read_b64_tr_b16 a[138:139], v17 offset:28160\n"
    "v_rcp_f32 v100, v102\n"
    "s_nop 0\n"
    "v_mul_f32 v48, v48, v100\n"
    "v_mul_f32 v49, v49, v100\n"
    "v_mul_f32 v50, v50, v100\n"
    "v_mul_f32 v51, v51, v100\n"
    "v_mul_f32 v52, v52, v100\n"
    "v_mfma_f32_32x32x16_bf16 v[112:127], a[112:115], a[52:55], v[112:127]\n"
    "s_mul_i32 s4, s1, 36864\n"
    "ds_read_b64_tr_b16 a[140:141], v16 offset:28416\n"
    "ds_read_b64_tr_b16 a[142:143], v17 offset:28416\n"
    "v_mul_f32 v53, v53, v100\n"
    "v_mul_f32 v54, v54, v100\n"
    "v_mul_f32 v55, v55, v100\n"
    "v_mul_f32 v56, v56, v100\n"
    "v_mul_f32 v57, v57, v100\n"
    "v_mfma_f32_32x32x16_bf16 v[128:143], a[116:119], a[52:55], v[128:143]\n"
    "s_add_u32 s4, s4, s0\n"
    "ds_read_b64_tr_b16 a[104:105], v16 offset:28672\n"
    "ds_read_b64_tr_b16 a[106:107], v17 offset:28672\n"
    "v_mul_f32 v58, v58, v100\n"
    "v_mul_f32 v59, v59, v100\n"
    "v_mul_f32 v106, v106, v100\n"
    "v_mul_f32 v107, v107, v100\n"
    "v_mul_f32 v108, v108, v100\n"
    "v_mul_f32 v109, v109, v100\n"
    "v_mfma_f32_32x32x16_bf16 v[144:159], a[120:123], a[52:55], v[144:159]\n"
    "s_mul_i32 s97, s1, 36864\n"
    "ds_read_b64_tr_b16 a[108:109], v16 offset:28928\n"
    "ds_read_b64_tr_b16 a[110:111], v17 offset:28928\n"
    "v_mul_f32 v110, v110, v100\n"
    "v_mul_f32 v111, v111, v100\n"
    "v_mul_f32 v6, v36, v6\n"
    "v_mul_f32 v7, v37, v7\n"
    "v_mfma_f32_32x32x16_bf16 v[160:175], a[124:127], a[52:55], v[160:175]\n"
    "s_waitcnt lgkmcnt(4)\n"
    "ds_read_b64_tr_b16 a[112:113], v16 offset:29184\n"
    "ds_read_b64_tr_b16 a[114:115], v17 offset:29184\n"
    "v_cvt_pk_bf16_f32 v60, v48, v49\n"
    "v_cvt_pk_bf16_f32 v61, v50, v51\n"
    "v_cvt_pk_bf16_f32 v62, v52, v53\n"
    "v_cvt_pk_bf16_f32 v63, v54, v55\n"
    "v_cvt_pk_bf16_f32 v64, v56, v57\n"
    "v_cvt_pk_bf16_f32 v65, v58, v59\n"
    "v_cvt_pk_bf16_f32 v66, v106, v107\n"
    "v_mfma_f32_32x32x16_bf16 v[112:127], a[128:131], a[56:59], v[112:127]\n"
    "s_add_u32 s97, s97, 3936256\n"
    "ds_read_b64_tr_b16 a[116:117], v16 offset:29440\n"
    "ds_read_b64_tr_b16 a[118:119], v17 offset:29440\n"
    "v_cvt_pk_bf16_f32 v67, v108, v109\n"
    "v_cvt_pk_bf16_f32 v68, v110, v111\n"
    "v_mul_f32 v8, v38, v8\n"
    "v_mul_f32 v9, v39, v9\n"
    "v_mfma_f32_32x32x16_bf16 v[128:143], a[132:135], a[56:59], v[128:143]\n"
    "s_xor_b32 s96, s36, 1\n"
    "ds_read_b64_tr_b16 a[120:121], v16 offset:29696\n"
    "ds_read_b64_tr_b16 a[122:123], v17 offset:29696\n"
    "ds_write_b128 v190, v[60:63]\n"
    "v_mul_f32 v80, v80, v101\n"
    "v_mul_f32 v81, v81, v101\n"
    "v_mul_f32 v82, v82, v101\n"
    "v_mfma_f32_32x32x16_bf16 v[144:159], a[136:139], a[56:59], v[144:159]\n"
    "s_mul_i32 s96, s96, 2304\n"
    "ds_read_b64_tr_b16 a[124:125], v16 offset:29952\n"
    "ds_read_b64_tr_b16 a[126:127], v17 offset:29952\n"
    "v_mul_f32 v83, v83, v101\n"
    "v_mul_f32 v84, v84, v101\n"
    "v_mul_f32 v85, v85, v101\n"
    "v_mul_f32 v86, v86, v101\n"
    "v_mul_f32 v87, v87, v101\n"
    "v_mfma_f32_32x32x16_bf16 v[160:175], a[140:143], a[56:59], v[160:175]\n"
    "s_waitcnt vmcnt(16) lgkmcnt(5)\n"
    "s_barrier\n"
    "v_mul_f32 v88, v88, v101\n"
    "v_mul_f32 v89, v89, v101\n"
    "v_mfma_f32_32x32x16_bf16 v[112:127], a[104:107], a[60:63], v[112:127]\n"
    "s_add_u32 s96, s96, s97\n"
    "ds_read_b64_tr_b16 a[128:129], v16 offset:30208\n"
    "ds_read_b64_tr_b16 a[130:131], v17 offset:30208\n"
    "ds_read_b64_tr_b16 a[132:133], v16 offset:30464\n"
    "ds_read_b64_tr_b16 a[134:135], v17 offset:30464\n"
    "v_mul_f32 v90, v90, v101\n"
    "v_mul_f32 v91, v91, v101\n"
    "v_mfma_f32_32x32x16_bf16 v[128:143], a[108:111], a[60:63], v[128:143]\n"
    "s_lshl_b32 s12, s1, 12\n"
    "ds_read_b64_tr_b16 a[136:137], v16 offset:30720\n"
    "ds_read_b64_tr_b16 a[138:139], v17 offset:30720\n"
    "ds_write_b128 v190, v[64:67] offset:16\n"
    "v_mul_f32 v92, v92, v101\n"
    "v_mul_f32 v93, v93, v101\n"
    "v_mul_f32 v94, v94, v101\n"
    "v_mfma_f32_32x32x16_bf16 v[144:159], a[112:115], a[60:63], v[144:159]\n"
    "s_add_u32 s12, s12, 1316864\n"
    "ds_read_b64_tr_b16 a[140:141], v16 offset:30976\n"
    "ds_read_b64_tr_b16 a[142:143], v17 offset:30976\n"
    "v_mul_f32 v95, v95, v101\n"
    "v_mul_f32 v96, v96, v101\n"
    "v_mul_f32 v97, v97, v101\n"
    "v_cvt_pk_bf16_f32 v70, v80, v81\n"
    "v_cvt_pk_bf16_f32 v71, v82, v83\n"
    "v_mfma_f32_32x32x16_bf16 v[160:175], a[116:119], a[60:63], v[160:175]\n"
    "s_waitcnt lgkmcnt(5)\n"
    "s_add_u32 s20, s70, 439296\n"
    "ds_read_b64_tr_b16 a[104:105], v16 offset:31232\n"
    "ds_read_b64_tr_b16 a[106:107], v17 offset:31232\n"
    "ds_write_b128 v190, v[68:71] offset:32\n"
    "v_cvt_pk_bf16_f32 v72, v84, v85\n"
    "v_mfma_f32_32x32x16_bf16 v[112:127], a[120:123], a[64:67], v[112:127]\n"
    "s_xor_b32 s18, s36, 1\n"
    "ds_read_b64_tr_b16 a[108:109], v16 offset:31488\n"
    "ds_read_b64_tr_b16 a[110:111], v17 offset:31488\n"
    "buffer_load_dwordx4 v19, s[48:51], s20 offen offset:3072 lds\n"
    "v_cvt_pk_bf16_f32 v73, v86, v87\n"
    "v_cvt_pk_bf16_f32 v74, v88, v89\n"
    "v_cvt_pk_bf16_f32 v75, v90, v91\n"
    "v_mfma_f32_32x32x16_bf16 v[128:143], a[124:127], a[64:67], v[128:143]\n"
    "s_lshl_b32 s18, s18, 7\n"
    "s_add_u32 m0, s68, 17408\n"
    "ds_read_b64_tr_b16 a[112:113], v16 offset:31744\n"
    "ds_read_b64_tr_b16 a[114:115], v17 offset:31744\n"
    "v_cvt_pk_bf16_f32 v34, v6, v7\n"
    "v_cvt_pk_bf16_f32 v35, v8, v9\n"
    "v_accvgpr_write_b32 a196, v32\n"
    "v_mfma_f32_32x32x16_bf16 v[144:159], a[128:131], a[64:67], v[144:159]\n"
    "s_add_u32 s18, s18, s12\n"
    "ds_read_b64_tr_b16 a[116:117], v16 offset:32000\n"
    "ds_read_b64_tr_b16 a[118:119], v17 offset:32000\n"
    "buffer_load_dwordx4 v19, s[48:51], s71 offen lds\n"
    "ds_write_b128 v190, v[72:75] offset:48\n"
    "v_cvt_pk_bf16_f32 v76, v92, v93\n"
    "v_cvt_pk_bf16_f32 v77, v94, v95\n"
    "v_mfma_f32_32x32x16_bf16 v[160:175], a[132:135], a[64:67], v[160:175]\n"
    "s_waitcnt lgkmcnt(5)\n"
    "s_add_u32 s20, s71, 146432\n"
    "ds_read_b64_tr_b16 a[120:121], v16 offset:32256\n"
    "ds_read_b64_tr_b16 a[122:123], v17 offset:32256\n"
    "buffer_load_dwordx4 v19, s[48:51], s20 offen offset:1024 lds\n"
    "v_cvt_pk_bf16_f32 v78, v96, v97\n"
    "v_accvgpr_read_b32 v188, a147\n"
    "v_mfma_f32_32x32x16_bf16 v[112:127], a[136:139], a[68:71], v[112:127]\n"
    "s_lshl_b32 s29, s3, 5\n"
    "s_add_u32 s20, s71, 292864\n"
    "ds_read_b64_tr_b16 a[124:125], v16 offset:32512\n"
    "ds_read_b64_tr_b16 a[126:127], v17 offset:32512\n"
    "v_accvgpr_read_b32 v189, a148\n"
    "v_add_u32 v30, 81920, v188\n"
    "v_add_u32 v31, 81920, v189\n"
    "v_accvgpr_read_b32 v10, a149\n"
    "v_mfma_f32_32x32x16_bf16 v[128:143], a[140:143], a[68:71], v[128:143]\n"
    "s_lshl2_add_u32 s5, s35, s39\n"
    "ds_read_b64_tr_b16 a[128:129], v16\n"
    "ds_read_b64_tr_b16 a[130:131], v17\n"
    "buffer_load_dwordx4 v19, s[48:51], s20 offen offset:2048 lds\n"
    "v_accvgpr_write_b32 a197, v33\n"
    "v_accvgpr_write_b32 a198, v34\n"
    "v_mfma_f32_32x32x16_bf16 v[144:159], a[104:107], a[68:71], v[144:159]\n"
    "s_mul_i32 s5, s5, 36864\n"
    "s_add_u32 s20, s71, 439296\n"
    "ds_read_b64_tr_b16 a[132:133], v16 offset:256\n"
    "ds_read_b64_tr_b16 a[134:135], v17 offset:256\n"
    "ds_write_b128 v190, v[76:79] offset:64\n"
    "v_accvgpr_read_b32 v11, a150\n"
    "v_mfma_f32_32x32x16_bf16 v[160:175], a[108:111], a[68:71], v[160:175]\n"
    "s_waitcnt lgkmcnt(5)\n"
    "ds_read_b64_tr_b16 a[136:137], v16 offset:512\n"
    "ds_read_b64_tr_b16 a[138:139], v17 offset:512\n"
    "buffer_load_dwordx4 v19, s[48:51], s20 offen offset:3072 lds\n"
    "v_accvgpr_write_b32 a199, v35\n"
    "v_mfma_f32_32x32x16_bf16 v[48:63], a[220:223], v[176:179], 0\n"
    "s_add_u32 s5, s5, s29\n"
    "v_exp_f32 v2, v40\n"
    "v_exp_f32 v3, v41\n"
    "v_exp_f32 v4, v42\n"
    "v_mfma_f32_32x32x16_bf16 v[112:127], a[112:115], a[72:75], v[112:127]\n"
    "s_waitcnt lgkmcnt(2)\n"
    "s_barrier\n"
    "s_add_u32 s21, s83, 32768\n"
    "ds_read_b64_tr_b16 a[140:141], v16 offset:768\n"
    "ds_read_b64_tr_b16 a[142:143], v17 offset:768\n"
    "buffer_store_dwordx4 a[196:199], v25, s[60:63], s21 offen sc1\n"
    "v_mfma_f32_32x32x16_bf16 v[48:63], a[224:227], v[180:183], v[48:63]\n"
    "s_add_u32 s5, s5, 1024\n"
    "ds_read_b64_tr_b16 a[80:81], v188\n"
    "ds_read_b64_tr_b16 a[82:83], v188 offset:640\n"
    "ds_read_b64_tr_b16 a[84:85], v189\n"
    "ds_read_b64_tr_b16 a[86:87], v189 offset:320\n"
    "ds_read_b64_tr_b16 a[88:89], v188 offset:2560\n"
    "ds_read_b64_tr_b16 a[90:91], v188 offset:3200\n"
    "v_mfma_f32_32x32x16_bf16 v[128:143], a[116:119], a[72:75], v[128:143]\n"
    "s_xor_b32 s13, 0, 1\n"
    "ds_read_b64_tr_b16 a[104:105], v16 offset:1024\n"
    "ds_read_b64_tr_b16 a[106:107], v17 offset:1024\n"
    "ds_read_b64_tr_b16 a[92:93], v189 offset:1280\n"
    "ds_read_b64_tr_b16 a[94:95], v189 offset:1600\n"
    "v_exp_f32 v5, v43\n"
    "v_mfma_f32_32x32x16_bf16 v[48:63], a[228:231], v[184:187], v[48:63]\n"
    "s_sub_u32 s13, s13, 0\n"
    "v_add_f32 v2, 1.0, v2\n"
    "v_add_f32 v3, 1.0, v3\n"
    "v_add_f32 v4, 1.0, v4\n"
    "v_add_f32 v5, 1.0, v5\n"
    "v_rcp_f32 v2, v2\n"
    "v_mfma_f32_32x32x16_bf16 v[144:159], a[120:123], a[72:75], v[144:159]\n"
    "s_mul_i32 s13, s13, 73728\n"
    "s_cmp_eq_u32 s11, 8\n"
    "s_cselect_b32 s20, 4290248704, s81\n"
    "s_add_u32 s71, s71, s20\n"
    "s_add_u32 m0, s68, 21504\n"
    "ds_read_b64_tr_b16 a[108:109], v16 offset:1280\n"
    "ds_read_b64_tr_b16 a[110:111], v17 offset:1280\n"
    "v_mfma_f32_32x32x16_bf16 v[64:79], a[232:235], v[176:179], 0\n"
    "s_xor_b32 s29, 0, 1\n"
    "buffer_load_dwordx4 v19, s[48:51], s71 offen lds\n"
    "v_rcp_f32 v3, v3\n"
    "v_rcp_f32 v4, v4\n"
    "v_mfma_f32_32x32x16_bf16 v[160:175], a[124:127], a[72:75], v[160:175]\n"
    "s_waitcnt lgkmcnt(8)\n"
    "s_add_u32 s20, s71, 146432\n"
    "ds_read_b64_tr_b16 a[112:113], v16 offset:1536\n"
    "ds_read_b64_tr_b16 a[114:115], v17 offset:1536\n"
    "v_rcp_f32 v5, v5\n"
    "v_mul_f32 v2, v40, v2\n"
    "v_mfma_f32_32x32x16_bf16 v[80:95], a[80:83], a[84:87], 0\n"
    "s_xor_b32 s69, 0, 2\n"
    "ds_read_b64_tr_b16 a[96:97], v188 offset:40960\n"
    "ds_read_b64_tr_b16 a[98:99], v188 offset:41600\n"
    "ds_read_b64_tr_b16 a[100:101], v189 offset:40960\n"
    "ds_read_b64_tr_b16 a[102:103], v189 offset:41280\n"
    "buffer_load_dwordx4 v19, s[48:51], s20 offen offset:1024 lds\n"
    "v_mfma_f32_32x32x16_bf16 v[64:79], a[236:239], v[180:183], v[64:79]\n"
    "s_sub_u32 s69, s69, s29\n"
    "v_mul_f32 v3, v41, v3\n"
    "v_mul_f32 v4, v42, v4\n"
    "v_mul_f32 v5, v43, v5\n"
    "v_cvt_pk_bf16_f32 v36, v2, v3\n"
    "v_cvt_pk_bf16_f32 v37, v4, v5\n"
    "v_mfma_f32_32x32x16_bf16 v[112:127], a[128:131], a[76:79], v[112:127]\n"
    "s_waitcnt lgkmcnt(8)\n"
    "s_mul_i32 s69, s69, 73728\n"
    "s_add_u32 s20, s71, 292864\n"
    "ds_read_b64_tr_b16 a[116:117], v16 offset:1792\n"
    "ds_read_b64_tr_b16 a[118:119], v17 offset:1792\n"
    "buffer_load_dwordx4 v19, s[48:51], s20 offen offset:2048 lds\n"
    "v_mfma_f32_32x32x16_bf16 v[80:95], a[88:91], a[92:95], v[80:95]\n"
    "ds_read_b64_tr_b16 a[80:81], v188 offset:43520\n"
    "ds_read_b64_tr_b16 a[82:83], v188 offset:44160\n"
    "ds_read_b64_tr_b16 a[84:85], v189 offset:42240\n"
    "ds_read_b64_tr_b16 a[86:87], v189 offset:42560\n"
    "v_exp_f32 v6, v44\n"
    "v_mfma_f32_32x32x16_bf16 v[64:79], a[240:243], v[184:187], v[64:79]\n"
    "v_exp_f32 v7, v45\n"
    "v_exp_f32 v8, v46\n"
    "v_exp_f32 v9, v47\n"
    "v_mfma_f32_32x32x16_bf16 v[128:143], a[132:135], a[76:79], v[128:143]\n"
    "s_waitcnt lgkmcnt(6)\n"
    "s_add_u32 s20, s71, 439296\n"
    "ds_read_b64_tr_b16 a[120:121], v16 offset:2048\n"
    "ds_read_b64_tr_b16 a[122:123], v17 offset:2048\n"
    "buffer_load_dwordx4 v19, s[48:51], s20 offen offset:3072 lds\n"
    "v_add_f32 v6, 1.0, v6\n"
    "v_mfma_f32_32x32x16_bf16 v[80:95], a[96:99], a[100:103], v[80:95]\n"
    "ds_read_b64_tr_b16 a[88:89], v30\n"
    "ds_read_b64_tr_b16 a[90:91], v30 offset:640\n"
    "ds_read_b64_tr_b16 a[92:93], v31\n"
    "ds_read_b64_tr_b16 a[94:95], v31 offset:320\n"
    "v_add_f32 v7, 1.0, v7\n"
    "v_add_f32 v8, 1.0, v8\n"
    "v_mfma_f32_32x32x16_bf16 v[144:159], a[136:139], a[76:79], v[144:159]\n"
    "s_waitcnt lgkmcnt(6)\n"
    "s_cmp_eq_u32 s11, 7\n"
    "s_cselect_b32 s20, 4290248704, s81\n"
    "s_add_u32 s71, s71, s20\n"
    "s_add_u32 m0, s68, 25600\n"
    "ds_read_b64_tr_b16 a[124:125], v16 offset:2304\n"
    "ds_read_b64_tr_b16 a[126:127], v17 offset:2304\n"
    "buffer_load_dwordx4 v19, s[48:51], s71 offen lds\n"
    "v_mfma_f32_32x32x16_bf16 v[80:95], a[80:83], a[84:87], v[80:95]\n"
    "ds_read_b64_tr_b16 a[96:97], v30 offset:2560\n"
    "ds_read_b64_tr_b16 a[98:99], v30 offset:3200\n"
    "ds_read_b64_tr_b16 a[100:101], v31 offset:1280\n"
    "ds_read_b64_tr_b16 a[102:103], v31 offset:1600\n"
    "v_add_f32 v9, 1.0, v9\n"
    "v_mfma_f32_32x32x16_bf16 v[160:175], a[140:143], a[76:79], v[160:175]\n"
    "s_waitcnt lgkmcnt(6)\n"
    "s_add_u32 s20, s71, 146432\n"
    "ds_read_b64_tr_b16 a[128:129], v16 offset:2560\n"
    "ds_read_b64_tr_b16 a[130:131], v17 offset:2560\n"
    "buffer_load_dwordx4 v19, s[48:51], s20 offen offset:1024 lds\n"
    "v_max3_f32 v66, v48, v49, v50\n"
    "v_min3_f32 v67, v48, v49, v50\n"
    "v_max3_f32 v66, v66, v51, v52\n"
    "v_min3_f32 v67, v67, v51, v52\n"
    "v_max3_f32 v66, v66, v53, v54\n"
    "v_mfma_f32_32x32x16_bf16 v[80:95], a[88:91], a[92:95], v[80:95]\n"
    "ds_read_b64_tr_b16 a[80:81], v30 offset:40960\n"
    "ds_read_b64_tr_b16 a[82:83], v30 offset:41600\n"
    "ds_read_b64_tr_b16 a[84:85], v31 offset:40960\n"
    "ds_read_b64_tr_b16 a[86:87], v31 offset:41280\n"
    "v_min3_f32 v67, v67, v53, v54\n"
    "v_max3_f32 v66, v66, v55, v56\n"
    "v_mfma_f32_32x32x16_bf16 v[112:127], a[104:107], v[192:195], v[112:127]\n"
    "s_waitcnt lgkmcnt(6)\n"
    "s_add_u32 s20, s71, 292864\n"
    "ds_read_b64_tr_b16 a[132:133], v16 offset:2816\n"
    "ds_read_b64_tr_b16 a[134:135], v17 offset:2816\n"
    "buffer_load_dwordx4 v19, s[48:51], s20 offen offset:2048 lds\n"
    "v_min3_f32 v67, v67, v55, v56\n"
    "v_max3_f32 v66, v66, v57, v58\n"
    "v_mfma_f32_32x32x16_bf16 v[80:95], a[96:99], a[100:103], v[80:95]\n"
    "ds_read_b64_tr_b16 a[88:89], v30 offset:43520\n"
    "ds_read_b64_tr_b16 a[90:91], v30 offset:44160\n"
    "ds_read_b64_tr_b16 a[92:93], v31 offset:42240\n"
    "ds_read_b64_tr_b16 a[94:95], v31 offset:42560\n"
    "v_min3_f32 v67, v67, v57, v58\n"
    "v_max3_f32 v66, v66, v59, v60\n"
    "v_mfma_f32_32x32x16_bf16 v[128:143], a[108:111], v[192:195], v[128:143]\n"
    "s_waitcnt lgkmcnt(6)\n"
    "ds_read_b64_tr_b16 a[136:137], v16 offset:3072\n"
    "ds_read_b64_tr_b16 a[138:139], v17 offset:3072\n"
    "v_min3_f32 v67, v67, v59, v60\n"
    "v_max3_f32 v66, v66, v61, v62\n"
    "v_min3_f32 v67, v67, v61, v62\n"
    "v_max3_f32 v66, v66, v63, v64\n"
    "v_min3_f32 v67, v67, v63, v64\n"
    "v_max_f32 v66, v66, v65\n"
    "v_mfma_f32_32x32x16_bf16 v[80:95], a[80:83], a[84:87], v[80:95]\n"
    "v_min_f32 v67, v67, v65\n"
    "v_sub_f32 v96, v48, v66\n"
    "v_sub_f32 v48, v67, v48\n"
    "v_exp_f32 v96, v96\n"
    "v_mfma_f32_32x32x16_bf16 v[144:159], a[112:115], v[192:195], v[144:159]\n"
    "s_waitcnt lgkmcnt(2)\n"
    "ds_read_b64_tr_b16 a[140:141], v16 offset:3328\n"
    "ds_read_b64_tr_b16 a[142:143], v17 offset:3328\n"
    "v_exp_f32 v48, v48\n"
    "v_sub_f32 v97, v49, v66\n"
    "v_sub_f32 v49, v67, v49\n"
    "v_mfma_f32_32x32x16_bf16 v[80:95], a[88:91], a[92:95], v[80:95]\n"
    "v_exp_f32 v97, v97\n"
    "v_exp_f32 v49, v49\n"
    "v_sub_f32 v98, v50, v66\n"
    "v_sub_f32 v50, v67, v50\n"
    "v_mfma_f32_32x32x16_bf16 v[160:175], a[116:119], v[192:195], v[160:175]\n"
    "s_waitcnt vmcnt(23)\n"
    "s_barrier\n"
    "s_add_u32 s20, s71, 439296\n"
    "buffer_load_dwordx4 v19, s[48:51], s20 offen offset:3072 lds\n"
    "v_mfma_f32_32x32x16_bf16 v[112:127], a[120:123], v[196:199], v[112:127]\n"
    "s_cmp_eq_u32 s11, 6\n"
    "s_cselect_b32 s20, 4290248704, s81\n"
    "s_add_u32 s71, s71, s20\n"
    "s_add_u32 m0, s68, 29696\n"
    "ds_read_b64_tr_b16 a[104:105], v16 offset:3584\n"
    "ds_read_b64_tr_b16 a[106:107], v17 offset:3584\n"
    "ds_read_b64_tr_b16 a[108:109], v16 offset:3840\n"
    "ds_read_b64_tr_b16 a[110:111], v17 offset:3840\n"
    "v_mfma_f32_32x32x16_bf16 v[128:143], a[124:127], v[196:199], v[128:143]\n"
    "ds_read_b64_tr_b16 a[112:113], v16 offset:4096\n"
    "ds_read_b64_tr_b16 a[114:115], v17 offset:4096\n"
    "buffer_store_dwordx4 v[80:83], v10, s[60:63], s94 offen sc1\n"
    "buffer_store_dwordx4 v[84:87], v10, s[60:63], s94 offen offset:768 sc1\n"
    "v_mfma_f32_32x32x16_bf16 v[144:159], a[128:131], v[196:199], v[144:159]\n"
    "ds_read_b64_tr_b16 a[116:117], v16 offset:4352\n"
    "ds_read_b64_tr_b16 a[118:119], v17 offset:4352\n"
    "buffer_store_dwordx2 v[88:89], v11, s[60:63], s94 offen offset:1536 sc1\n"
    "v_exp_f32 v98, v98\n"
    "v_mfma_f32_32x32x16_bf16 v[160:175], a[132:135], v[196:199], v[160:175]\n"
    "s_waitcnt lgkmcnt(4)\n"
    "ds_read_b64_tr_b16 a[120:121], v16 offset:4608\n"
    "ds_read_b64_tr_b16 a[122:123], v17 offset:4608\n"
    "v_exp_f32 v50, v50\n"
    "v_add_f32 v68, v96, v97\n"
    "v_add_f32 v69, v48, v49\n"
    "v_sub_f32 v99, v51, v66\n"
    "v_sub_f32 v51, v67, v51\n"
    "v_mfma_f32_32x32x16_bf16 v[112:127], a[136:139], v[200:203], v[112:127]\n"
    "ds_read_b64_tr_b16 a[124:125], v16 offset:4864\n"
    "ds_read_b64_tr_b16 a[126:127], v17 offset:4864\n"
    "v_exp_f32 v99, v99\n"
    "v_exp_f32 v51, v51\n"
    "v_add_f32 v68, v68, v98\n"
    "v_mfma_f32_32x32x16_bf16 v[128:143], a[140:143], v[200:203], v[128:143]\n"
    "ds_read_b64_tr_b16 a[128:129], v16 offset:5120\n"
    "ds_read_b64_tr_b16 a[130:131], v17 offset:5120\n"
    "v_add_f32 v69, v69, v50\n"
    "v_sub_f32 v100, v52, v66\n"
    "v_sub_f32 v52, v67, v52\n"
    "v_exp_f32 v100, v100\n"
    "v_mfma_f32_32x32x16_bf16 v[144:159], a[104:107], v[200:203], v[144:159]\n"
    "ds_read_b64_tr_b16 a[132:133], v16 offset:5376\n"
    "ds_read_b64_tr_b16 a[134:135], v17 offset:5376\n"
    "v_exp_f32 v52, v52\n"
    "v_add_f32 v68, v68, v99\n"
    "v_add_f32 v69, v69, v51\n"
    "v_sub_f32 v101, v53, v66\n"
    "v_mfma_f32_32x32x16_bf16 v[160:175], a[108:111], v[200:203], v[160:175]\n"
    "s_waitcnt lgkmcnt(4)\n"
    "ds_read_b64_tr_b16 a[136:137], v16 offset:5632\n"
    "ds_read_b64_tr_b16 a[138:139], v17 offset:5632\n"
    "v_sub_f32 v53, v67, v53\n"
    "v_exp_f32 v101, v101\n"
    "v_exp_f32 v53, v53\n"
    "v_mfma_f32_32x32x16_bf16 v[112:127], a[112:115], v[204:207], v[112:127]\n"
    "ds_read_b64_tr_b16 a[140:141], v16 offset:5888\n"
    "ds_read_b64_tr_b16 a[142:143], v17 offset:5888\n"
    "v_rcp_f32 v6, v6\n"
    "v_rcp_f32 v7, v7\n"
    "v_add_f32 v68, v68, v100\n"
    "v_mfma_f32_32x32x16_bf16 v[128:143], a[116:119], v[204:207], v[128:143]\n"
    "ds_read_b64_tr_b16 a[104:105], v16 offset:6144\n"
    "ds_read_b64_tr_b16 a[106:107], v17 offset:6144\n"
    "v_rcp_f32 v8, v8\n"
    "v_rcp_f32 v9, v9\n"
    "v_mul_f32 v6, v44, v6\n"
    "v_mfma_f32_32x32x16_bf16 v[144:159], a[120:123], v[204:207], v[144:159]\n"
    "ds_read_b64_tr_b16 a[108:109], v16 offset:6400\n"
    "ds_read_b64_tr_b16 a[110:111], v17 offset:6400\n"
    "v_mul_f32 v7, v45, v7\n"
    "v_mul_f32 v8, v46, v8\n"
    "v_mul_f32 v9, v47, v9\n"
    "v_cvt_pk_bf16_f32 v38, v6, v7\n"
    "v_cvt_pk_bf16_f32 v39, v8, v9\n"
    "v_mfma_f32_32x32x16_bf16 v[160:175], a[124:127], v[204:207], v[160:175]\n"
    "s_waitcnt lgkmcnt(4)\n"
    "ds_read_b64_tr_b16 a[112:113], v16 offset:6656\n"
    "ds_read_b64_tr_b16 a[114:115], v17 offset:6656\n"
    "v_accvgpr_write_b32 a200, v36\n"
    "v_accvgpr_write_b32 a201, v37\n"
    "v_accvgpr_write_b32 a202, v38\n"
    "v_accvgpr_write_b32 a203, v39\n"
    "v_mfma_f32_32x32x16_bf16 v[112:127], a[128:131], v[208:211], v[112:127]\n"
    "s_add_u32 s21, s83, 36864\n"
    "ds_read_b64_tr_b16 a[116:117], v16 offset:6912\n"
    "ds_read_b64_tr_b16 a[118:119], v17 offset:6912\n"
    "buffer_store_dwordx4 a[200:203], v25, s[60:63], s21 offen sc1\n"
    "v_add_f32 v69, v69, v52\n"
    "v_sub_f32 v102, v54, v66\n"
    "v_mfma_f32_32x32x16_bf16 v[128:143], a[132:135], v[208:211], v[128:143]\n"
    "ds_read_b64_tr_b16 a[120:121], v16 offset:7168\n"
    "ds_read_b64_tr_b16 a[122:123], v17 offset:7168\n"
    "v_sub_f32 v54, v67, v54\n"
    "v_exp_f32 v102, v102\n"
    "v_exp_f32 v54, v54\n"
    "v_mfma_f32_32x32x16_bf16 v[144:159], a[136:139], v[208:211], v[144:159]\n"
    "ds_read_b64_tr_b16 a[124:125], v16 offset:7424\n"
    "ds_read_b64_tr_b16 a[126:127], v17 offset:7424\n"
    "v_add_f32 v68, v68, v101\n"
    "v_add_f32 v69, v69, v53\n"
    "v_sub_f32 v103, v55, v66\n"
    "v_sub_f32 v55, v67, v55\n"
    "v_mfma_f32_32x32x16_bf16 v[160:175], a[140:143], v[208:211], v[160:175]\n"
    "s_waitcnt lgkmcnt(4)\n"
    "ds_read_b64_tr_b16 a[128:129], v16 offset:7680\n"
    "ds_read_b64_tr_b16 a[130:131], v17 offset:7680\n"
    "v_exp_f32 v103, v103\n"
    "v_exp_f32 v55, v55\n"
    "v_add_f32 v68, v68, v102\n"
    "v_add_f32 v69, v69, v54\n"
    "v_mfma_f32_32x32x16_bf16 v[112:127], a[104:107], v[212:215], v[112:127]\n"
    "ds_read_b64_tr_b16 a[132:133], v16 offset:7936\n"
    "ds_read_b64_tr_b16 a[134:135], v17 offset:7936\n"
    "v_sub_f32 v104, v56, v66\n"
    "v_sub_f32 v56, v67, v56\n"
    "v_exp_f32 v104, v104\n"
    "v_mfma_f32_32x32x16_bf16 v[128:143], a[108:111], v[212:215], v[128:143]\n"
    "ds_read_b64_tr_b16 a[136:137], v16 offset:8192\n"
    "ds_read_b64_tr_b16 a[138:139], v17 offset:8192\n"
    "v_exp_f32 v56, v56\n"
    "v_add_f32 v68, v68, v103\n"
    "v_add_f32 v69, v69, v55\n"
    "v_sub_f32 v105, v57, v66\n"
    "v_sub_f32 v57, v67, v57\n"
    "v_mfma_f32_32x32x16_bf16 v[144:159], a[112:115], v[212:215], v[144:159]\n"
    "ds_read_b64_tr_b16 a[140:141], v16 offset:8448\n"
    "ds_read_b64_tr_b16 a[142:143], v17 offset:8448\n"
    "buffer_load_dwordx4 v19, s[48:51], s71 offen lds\n"
    "v_exp_f32 v105, v105\n"
    "v_mfma_f32_32x32x16_bf16 v[160:175], a[116:119], v[212:215], v[160:175]\n"
    "s_waitcnt lgkmcnt(4)\n"
    "s_add_u32 s20, s71, 146432\n"
    "ds_read_b64_tr_b16 a[104:105], v16 offset:8704\n"
    "ds_read_b64_tr_b16 a[106:107], v17 offset:8704\n"
    "v_exp_f32 v57, v57\n"
    "v_add_f32 v68, v68, v104\n"
    "v_add_f32 v69, v69, v56\n"
    "v_mfma_f32_32x32x16_bf16 v[112:127], a[120:123], v[216:219], v[112:127]\n"
    "ds_read_b64_tr_b16 a[108:109], v16 offset:8960\n"
    "ds_read_b64_tr_b16 a[110:111], v17 offset:8960\n"
    "buffer_load_dwordx4 v19, s[48:51], s20 offen offset:1024 lds\n"
    "v_sub_f32 v106, v58, v66\n"
    "v_sub_f32 v58, v67, v58\n"
    "v_mfma_f32_32x32x16_bf16 v[128:143], a[124:127], v[216:219], v[128:143]\n"
    "s_waitcnt vmcnt(3)\n"
    "ds_read_b64_tr_b16 a[112:113], v16 offset:9216\n"
    "ds_read_b64_tr_b16 a[114:115], v17 offset:9216\n"
    "buffer_store_dword v29, v26, s[60:63], s8 offen sc1\n"
    "v_exp_f32 v106, v106\n"
    "v_mfma_f32_32x32x16_bf16 v[144:159], a[128:131], v[216:219], v[144:159]\n"
    "s_add_u32 s20, s71, 292864\n"
    "ds_read_b64_tr_b16 a[116:117], v16 offset:9472\n"
    "ds_read_b64_tr_b16 a[118:119], v17 offset:9472\n"
    "v_exp_f32 v58, v58\n"
    "v_add_f32 v68, v68, v105\n"
    "v_add_f32 v69, v69, v57\n"
    "v_sub_f32 v107, v59, v66\n"
    "v_sub_f32 v59, v67, v59\n"
    "v_mfma_f32_32x32x16_bf16 v[160:175], a[132:135], v[216:219], v[160:175]\n"
    "s_waitcnt lgkmcnt(4)\n"
    "s_barrier\n"
    "v_mfma_f32_32x32x16_bf16 v[112:127], a[136:139], v[220:223], v[112:127]\n"
    "ds_read_b64_tr_b16 a[120:121], v16 offset:9728\n"
    "ds_read_b64_tr_b16 a[122:123], v17 offset:9728\n"
    "ds_read_b64_tr_b16 a[124:125], v16 offset:9984\n"
    "ds_read_b64_tr_b16 a[126:127], v17 offset:9984\n"
    "buffer_load_dwordx4 v19, s[48:51], s20 offen offset:2048 lds\n"
    "v_exp_f32 v107, v107\n"
    "v_mfma_f32_32x32x16_bf16 v[128:143], a[140:143], v[220:223], v[128:143]\n"
    "s_add_u32 m0, s68, 0\n"
    "s_add_u32 s20, s71, 442368\n"
    "ds_read_b64_tr_b16 a[128:129], v16 offset:10240\n"
    "ds_read_b64_tr_b16 a[130:131], v17 offset:10240\n"
    "buffer_load_dwordx4 v19, s[48:51], s20 offen lds\n"
    "v_exp_f32 v59, v59\n"
    "v_mfma_f32_32x32x16_bf16 v[144:159], a[104:107], v[220:223], v[144:159]\n"
    "s_cmp_eq_u32 s11, 5\n"
    "s_cselect_b32 s20, 4290248704, s81\n"
    "s_add_u32 s71, s71, s20\n"
    "s_add_u32 m0, s68, 1024\n"
    "ds_read_b64_tr_b16 a[132:133], v16 offset:10496\n"
    "ds_read_b64_tr_b16 a[134:135], v17 offset:10496\n"
    "buffer_load_dwordx4 v19, s[48:51], s71 offen lds\n"
    "v_mfma_f32_32x32x16_bf16 v[160:175], a[108:111], v[220:223], v[160:175]\n"
    "s_waitcnt lgkmcnt(4)\n"
    "s_add_u32 s20, s71, 146432\n"
    "ds_read_b64_tr_b16 a[136:137], v16 offset:10752\n"
    "ds_read_b64_tr_b16 a[138:139], v17 offset:10752\n"
    "v_add_f32 v68, v68, v106\n"
    "v_add_f32 v69, v69, v58\n"
    "v_sub_f32 v108, v60, v66\n"
    "v_mfma_f32_32x32x16_bf16 v[112:127], a[112:115], v[224:227], v[112:127]\n"
    "ds_read_b64_tr_b16 a[140:141], v16 offset:11008\n"
    "ds_read_b64_tr_b16 a[142:143], v17 offset:11008\n"
    "buffer_load_dwordx4 v19, s[48:51], s20 offen offset:1024 lds\n"
    "v_sub_f32 v60, v67, v60\n"
    "v_exp_f32 v108, v108\n"
    "v_mfma_f32_32x32x16_bf16 v[128:143], a[116:119], v[224:227], v[128:143]\n"
    "s_add_u32 s20, s71, 292864\n"
    "ds_read_b64_tr_b16 a[104:105], v16 offset:11264\n"
    "ds_read_b64_tr_b16 a[106:107], v17 offset:11264\n"
    "v_exp_f32 v60, v60\n"
    "v_add_f32 v68, v68, v107\n"
    "v_add_f32 v69, v69, v59\n"
    "v_sub_f32 v109, v61, v66\n"
    "v_mfma_f32_32x32x16_bf16 v[144:159], a[120:123], v[224:227], v[144:159]\n"
    "ds_read_b64_tr_b16 a[108:109], v16 offset:11520\n"
    "ds_read_b64_tr_b16 a[110:111], v17 offset:11520\n"
    "buffer_load_dwordx4 v19, s[48:51], s20 offen offset:2048 lds\n"
    "v_sub_f32 v61, v67, v61\n"
    "v_exp_f32 v109, v109\n"
    "v_mfma_f32_32x32x16_bf16 v[160:175], a[124:127], v[224:227], v[160:175]\n"
    "s_waitcnt lgkmcnt(4)\n"
    "s_add_u32 s20, s71, 439296\n"
    "ds_read_b64_tr_b16 a[112:113], v16 offset:11776\n"
    "ds_read_b64_tr_b16 a[114:115], v17 offset:11776\n"
    "v_exp_f32 v61, v61\n"
    "v_add_f32 v68, v68, v108\n"
    "v_add_f32 v69, v69, v60\n"
    "v_mfma_f32_32x32x16_bf16 v[112:127], a[128:131], v[228:231], v[112:127]\n"
    "ds_read_b64_tr_b16 a[116:117], v16 offset:12032\n"
    "ds_read_b64_tr_b16 a[118:119], v17 offset:12032\n"
    "buffer_load_dwordx4 v19, s[48:51], s20 offen offset:3072 lds\n"
    "v_sub_f32 v110, v62, v66\n"
    "v_sub_f32 v62, v67, v62\n"
    "v_mfma_f32_32x32x16_bf16 v[128:143], a[132:135], v[228:231], v[128:143]\n"
    "s_cmp_eq_u32 s11, 4\n"
    "s_cselect_b32 s20, 4290248704, s81\n"
    "s_add_u32 s71, s71, s20\n"
    "s_add_u32 m0, s68, 5120\n"
    "ds_read_b64_tr_b16 a[120:121], v16 offset:12288\n"
    "ds_read_b64_tr_b16 a[122:123], v17 offset:12288\n"
    "v_exp_f32 v110, v110\n"
    "v_mfma_f32_32x32x16_bf16 v[144:159], a[136:139], v[228:231], v[144:159]\n"
    "ds_read_b64_tr_b16 a[124:125], v16 offset:12544\n"
    "ds_read_b64_tr_b16 a[126:127], v17 offset:12544\n"
    "buffer_load_dwordx4 v19, s[48:51], s71 offen lds\n"
    "v_exp_f32 v62, v62\n"
    "v_add_f32 v68, v68, v109\n"
    "v_add_f32 v69, v69, v61\n"
    "v_mfma_f32_32x32x16_bf16 v[160:175], a[140:143], v[228:231], v[160:175]\n"
    "s_waitcnt lgkmcnt(4)\n"
    "s_add_u32 s20, s71, 146432\n"
    "ds_read_b64_tr_b16 a[128:129], v16 offset:12800\n"
    "ds_read_b64_tr_b16 a[130:131], v17 offset:12800\n"
    "buffer_load_dwordx4 v19, s[48:51], s20 offen offset:1024 lds\n"
    "v_sub_f32 v111, v63, v66\n"
    "v_sub_f32 v63, v67, v63\n"
    "v_mfma_f32_32x32x16_bf16 v[112:127], a[104:107], v[232:235], v[112:127]\n"
    "s_add_u32 s20, s71, 292864\n"
    "ds_read_b64_tr_b16 a[132:133], v16 offset:13056\n"
    "ds_read_b64_tr_b16 a[134:135], v17 offset:13056\n"
    "v_exp_f32 v111, v111\n"
    "v_exp_f32 v63, v63\n"
    "v_mfma_f32_32x32x16_bf16 v[128:143], a[108:111], v[232:235], v[128:143]\n"
    "ds_read_b64_tr_b16 a[136:137], v16 offset:13312\n"
    "ds_read_b64_tr_b16 a[138:139], v17 offset:13312\n"
    "buffer_load_dwordx4 v19, s[48:51], s20 offen offset:2048 lds\n"
    "v_add_f32 v68, v68, v110\n"
    "v_add_f32 v69, v69, v62\n"
    "v_sub_f32 v72, v64, v66\n"
    "v_mfma_f32_32x32x16_bf16 v[144:159], a[112:115], v[232:235], v[144:159]\n"
    "ds_read_b64_tr_b16 a[140:141], v16 offset:13568\n"
    "ds_read_b64_tr_b16 a[142:143], v17 offset:13568\n"
    "v_sub_f32 v64, v67, v64\n"
    "v_exp_f32 v72, v72\n"
    "v_exp_f32 v64, v64\n"
    "v_mfma_f32_32x32x16_bf16 v[160:175], a[116:119], v[232:235], v[160:175]\n"
    "s_waitcnt lgkmcnt(4)\n"
    "ds_read_b64_tr_b16 a[104:105], v16 offset:13824\n"
    "ds_read_b64_tr_b16 a[106:107], v17 offset:13824\n"
    "v_add_f32 v68, v68, v111\n"
    "v_add_f32 v69, v69, v63\n"
    "v_sub_f32 v73, v65, v66\n"
    "v_sub_f32 v65, v67, v65\n"
    "v_mfma_f32_32x32x16_bf16 v[112:127], a[120:123], v[236:239], v[112:127]\n"
    "ds_read_b64_tr_b16 a[108:109], v16 offset:14080\n"
    "ds_read_b64_tr_b16 a[110:111], v17 offset:14080\n"
    "v_exp_f32 v73, v73\n"
    "v_exp_f32 v65, v65\n"
    "v_add_f32 v68, v68, v72\n"
    "v_add_f32 v69, v69, v64\n"
    "v_mfma_f32_32x32x16_bf16 v[128:143], a[124:127], v[236:239], v[128:143]\n"
    "ds_read_b64_tr_b16 a[112:113], v16 offset:14336\n"
    "ds_read_b64_tr_b16 a[114:115], v17 offset:14336\n"
    "v_add_f32 v68, v68, v73\n"
    "v_add_f32 v69, v69, v65\n"
    "v_rcp_f32 v67, v69\n"
    "s_nop 0\n"
    "v_mul_f32 v67, v67, v68\n"
    "v_mfma_f32_32x32x16_bf16 v[144:159], a[128:131], v[236:239], v[144:159]\n"
    "ds_read_b64_tr_b16 a[116:117], v16 offset:14592\n"
    "ds_read_b64_tr_b16 a[118:119], v17 offset:14592\n"
    "v_mul_f32 v30, 897988541, v68\n"
    "v_cvt_pk_bf16_f32 v176, v96, v97\n"
    "v_cvt_pk_bf16_f32 v177, v98, v99\n"
    "v_cvt_pk_bf16_f32 v178, v100, v101\n"
    "v_cvt_pk_bf16_f32 v179, v102, v103\n"
    "v_mfma_f32_32x32x16_bf16 v[160:175], a[132:135], v[236:239], v[160:175]\n"
    "s_waitcnt vmcnt(12) lgkmcnt(4)\n"
    "ds_read_b64_tr_b16 a[120:121], v16 offset:14848\n"
    "ds_read_b64_tr_b16 a[122:123], v17 offset:14848\n"
    "buffer_store_dword v29, v26, s[60:63], s6 offen offset:2048 sc1\n"
    "v_cvt_pk_bf16_f32 v180, v104, v105\n"
    "v_cvt_pk_bf16_f32 v181, v106, v107\n"
    "v_mfma_f32_32x32x16_bf16 v[112:127], a[136:139], v[240:243], v[112:127]\n"
    "ds_read_b64_tr_b16 a[124:125], v16 offset:15104\n"
    "ds_read_b64_tr_b16 a[126:127], v17 offset:15104\n"
    "v_cvt_pk_bf16_f32 v182, v108, v109\n"
    "v_cvt_pk_bf16_f32 v183, v110, v111\n"
    "v_cvt_pk_bf16_f32 v184, v72, v73\n"
    "v_mov_b32 v185, 0\n"
    "v_mfma_f32_32x32x16_bf16 v[128:143], a[140:143], v[240:243], v[128:143]\n"
    "ds_read_b64_tr_b16 a[128:129], v16 offset:15360\n"
    "ds_read_b64_tr_b16 a[130:131], v17 offset:15360\n"
    "v_mov_b32 v47, 0\n"
    "v_mul_f32 v48, v48, v67\n"
    "v_mul_f32 v49, v49, v67\n"
    "v_mul_f32 v50, v50, v67\n"
    "v_mul_f32 v51, v51, v67\n"
    "v_mfma_f32_32x32x16_bf16 v[144:159], a[104:107], v[240:243], v[144:159]\n"
    "ds_read_b64_tr_b16 a[132:133], v16 offset:15616\n"
    "ds_read_b64_tr_b16 a[134:135], v17 offset:15616\n"
    "v_mul_f32 v52, v52, v67\n"
    "v_mul_f32 v53, v53, v67\n"
    "v_mul_f32 v54, v54, v67\n"
    "v_mul_f32 v55, v55, v67\n"
    "v_mul_f32 v56, v56, v67\n"
    "v_mfma_f32_32x32x16_bf16 v[160:175], a[108:111], v[240:243], v[160:175]\n"
    "s_waitcnt lgkmcnt(4)\n"
    "s_barrier\n"
    "s_add_u32 s20, s71, 439296\n"
    "v_mfma_f32_32x32x16_bf16 v[112:127], a[112:115], v[244:247], v[112:127]\n"
    "ds_read_b64_tr_b16 a[136:137], v16 offset:15872\n"
    "ds_read_b64_tr_b16 a[138:139], v17 offset:15872\n"
    "ds_read_b64_tr_b16 a[140:141], v16 offset:16128\n"
    "ds_read_b64_tr_b16 a[142:143], v17 offset:16128\n"
    "buffer_load_dwordx4 v19, s[48:51], s20 offen offset:3072 lds\n"
    "v_mfma_f32_32x32x16_bf16 v[128:143], a[116:119], v[244:247], v[128:143]\n"
    "s_cmp_eq_u32 s11, 3\n"
    "s_cselect_b32 s20, 4290248704, s81\n"
    "s_add_u32 s71, s71, s20\n"
    "s_add_u32 m0, s68, 9216\n"
    "ds_read_b64_tr_b16 a[104:105], v16 offset:16384\n"
    "ds_read_b64_tr_b16 a[106:107], v17 offset:16384\n"
    "v_mul_f32 v57, v57, v67\n"
    "v_mfma_f32_32x32x16_bf16 v[144:159], a[120:123], v[244:247], v[144:159]\n"
    "ds_read_b64_tr_b16 a[108:109], v16 offset:16640\n"
    "ds_read_b64_tr_b16 a[110:111], v17 offset:16640\n"
    "buffer_load_dwordx4 v19, s[48:51], s71 offen lds\n"
    "v_mul_f32 v58, v58, v67\n"
    "v_mul_f32 v59, v59, v67\n"
    "v_mul_f32 v60, v60, v67\n"
    "v_mfma_f32_32x32x16_bf16 v[160:175], a[124:127], v[244:247], v[160:175]\n"
    "s_waitcnt lgkmcnt(4)\n"
    "s_add_u32 s20, s71, 146432\n"
    "ds_read_b64_tr_b16 a[112:113], v16 offset:16896\n"
    "ds_read_b64_tr_b16 a[114:115], v17 offset:16896\n"
    "buffer_load_dwordx4 v19, s[48:51], s20 offen offset:1024 lds\n"
    "v_mul_f32 v61, v61, v67\n"
    "v_mfma_f32_32x32x16_bf16 v[112:127], a[128:131], v[248:251], v[112:127]\n"
    "s_add_u32 m0, s68, 32768\n"
    "ds_read_b64_tr_b16 a[116:117], v16 offset:17152\n"
    "ds_read_b64_tr_b16 a[118:119], v17 offset:17152\n"
    "buffer_load_dwordx4 v14, s[56:59], s87 offen lds\n"
    "v_mul_f32 v62, v62, v67\n"
    "v_mul_f32 v63, v63, v67\n"
    "v_mfma_f32_32x32x16_bf16 v[128:143], a[132:135], v[248:251], v[128:143]\n"
    "ds_read_b64_tr_b16 a[120:121], v16 offset:17408\n"
    "ds_read_b64_tr_b16 a[122:123], v17 offset:17408\n"
    "v_mul_f32 v64, v64, v67\n"
    "v_mul_f32 v65, v65, v67\n"
    "v_cvt_pk_bf16_f32 v186, v48, v49\n"
    "v_cvt_pk_bf16_f32 v187, v50, v51\n"
    "v_mfma_f32_32x32x16_bf16 v[144:159], a[136:139], v[248:251], v[144:159]\n"
    "s_add_u32 m0, s68, 11264\n"
    "s_add_u32 s20, s71, 294912\n"
    "ds_read_b64_tr_b16 a[124:125], v16 offset:17664\n"
    "ds_read_b64_tr_b16 a[126:127], v17 offset:17664\n"
    "v_cvt_pk_bf16_f32 v188, v52, v53\n"
    "v_cvt_pk_bf16_f32 v189, v54, v55\n"
    "v_cvt_pk_bf16_f32 v190, v56, v57\n"
    "v_mfma_f32_32x32x16_bf16 v[160:175], a[140:143], v[248:251], v[160:175]\n"
    "s_waitcnt lgkmcnt(4)\n"
    "ds_read_b64_tr_b16 a[128:129], v16 offset:17920\n"
    "ds_read_b64_tr_b16 a[130:131], v17 offset:17920\n"
    "buffer_load_dwordx4 v19, s[48:51], s20 offen lds\n"
    "v_cvt_pk_bf16_f32 v191, v58, v59\n"
    "v_cvt_pk_bf16_f32 v44, v60, v61\n"
    "v_mfma_f32_32x32x16_bf16 v[112:127], a[104:107], v[252:255], v[112:127]\n"
    "s_add_u32 s20, s71, 441344\n"
    "ds_read_b64_tr_b16 a[132:133], v16 offset:18176\n"
    "ds_read_b64_tr_b16 a[134:135], v17 offset:18176\n"
    "buffer_load_dwordx4 v19, s[48:51], s20 offen offset:1024 lds\n"
    "v_cvt_pk_bf16_f32 v45, v62, v63\n"
    "v_cvt_pk_bf16_f32 v46, v64, v65\n"
    "v_mfma_f32_32x32x16_bf16 v[128:143], a[108:111], v[252:255], v[128:143]\n"
    "s_add_u32 s10, s10, s82\n"
    "s_and_b32 s10, s10, 7\n"
    "s_mul_i32 s20, s10, 82944\n"
    "s_add_u32 s87, s72, s20\n"
    "s_add_u32 m0, s68, 33792\n"
    "ds_read_b64_tr_b16 a[136:137], v16 offset:18432\n"
    "ds_read_b64_tr_b16 a[138:139], v17 offset:18432\n"
    "v_mfma_f32_32x32x16_bf16 v[144:159], a[112:115], v[252:255], v[144:159]\n"
    "ds_read_b64_tr_b16 a[140:141], v16 offset:18688\n"
    "ds_read_b64_tr_b16 a[142:143], v17 offset:18688\n"
    "buffer_load_dwordx4 v14, s[56:59], s87 offen lds\n"
    "v_mfma_f32_32x32x16_bf16 v[160:175], a[116:119], v[252:255], v[160:175]\n"
    "s_waitcnt lgkmcnt(4)\n"
    "ds_read_b64_tr_b16 a[104:105], v16 offset:18944\n"
    "ds_read_b64_tr_b16 a[106:107], v17 offset:18944\n"
    "v_mfma_f32_32x32x16_bf16 v[48:63], a[120:123], a[0:3], 0\n"
    "ds_read_b64_tr_b16 a[108:109], v16 offset:19200\n"
    "ds_read_b64_tr_b16 a[110:111], v17 offset:19200\n"
    "v_mfma_f32_32x32x16_bf16 v[64:79], a[124:127], a[0:3], 0\n"
    "s_cmp_eq_u32 s11, 2\n"
    "s_cselect_b32 s20, 4290248704, s81\n"
    "s_add_u32 s71, s71, s20\n"
    "s_add_u32 m0, s68, 13312\n"
    "ds_read_b64_tr_b16 a[112:113], v16 offset:19456\n"
    "ds_read_b64_tr_b16 a[114:115], v17 offset:19456\n"
    "v_exp_f32 v2, v112\n"
    "v_exp_f32 v3, v113\n"
    "v_mfma_f32_32x32x16_bf16 v[80:95], a[128:131], a[0:3], 0\n"
    "ds_read_b64_tr_b16 a[116:117], v16 offset:19712\n"
    "ds_read_b64_tr_b16 a[118:119], v17 offset:19712\n"
    "buffer_load_dwordx4 v19, s[48:51], s71 offen lds\n"
    "v_exp_f32 v4, v114\n"
    "v_exp_f32 v5, v115\n"
    "v_mfma_f32_32x32x16_bf16 v[96:111], a[132:135], a[0:3], 0\n"
    "s_waitcnt lgkmcnt(4)\n"
    "ds_read_b64_tr_b16 a[120:121], v16 offset:19968\n"
    "ds_read_b64_tr_b16 a[122:123], v17 offset:19968\n"
    "v_add_f32 v2, 1.0, v2\n"
    "v_add_f32 v3, 1.0, v3\n"
    "v_add_f32 v4, 1.0, v4\n"
    "v_add_f32 v5, 1.0, v5\n"
    "v_mfma_f32_32x32x16_bf16 v[48:63], a[136:139], a[4:7], v[48:63]\n"
    "ds_read_b64_tr_b16 a[124:125], v16 offset:20224\n"
    "ds_read_b64_tr_b16 a[126:127], v17 offset:20224\n"
    "v_rcp_f32 v2, v2\n"
    "v_rcp_f32 v3, v3\n"
    "v_rcp_f32 v4, v4\n"
    "v_mfma_f32_32x32x16_bf16 v[64:79], a[140:143], a[4:7], v[64:79]\n"
    "ds_read_b64_tr_b16 a[128:129], v16 offset:20480\n"
    "ds_read_b64_tr_b16 a[130:131], v17 offset:20480\n"
    "v_rcp_f32 v5, v5\n"
    "v_mul_f32 v2, v112, v2\n"
    "v_mul_f32 v3, v113, v3\n"
    "v_mul_f32 v4, v114, v4\n"
    "v_mfma_f32_32x32x16_bf16 v[80:95], a[104:107], a[4:7], v[80:95]\n"
    "ds_read_b64_tr_b16 a[132:133], v16 offset:20736\n"
    "ds_read_b64_tr_b16 a[134:135], v17 offset:20736\n"
    "v_mul_f32 v5, v115, v5\n"
    "v_cvt_pk_bf16_f32 v112, v2, v3\n"
    "v_cvt_pk_bf16_f32 v113, v4, v5\n"
    "v_exp_f32 v6, v116\n"
    "v_mfma_f32_32x32x16_bf16 v[96:111], a[108:111], a[4:7], v[96:111]\n"
    "s_waitcnt lgkmcnt(4)\n"
    "ds_read_b64_tr_b16 a[136:137], v16 offset:20992\n"
    "ds_read_b64_tr_b16 a[138:139], v17 offset:20992\n"
    "v_exp_f32 v7, v117\n"
    "v_exp_f32 v8, v118\n"
    "v_exp_f32 v9, v119\n"
    "v_mfma_f32_32x32x16_bf16 v[48:63], a[112:115], a[8:11], v[48:63]\n"
    "ds_read_b64_tr_b16 a[140:141], v16 offset:21248\n"
    "ds_read_b64_tr_b16 a[142:143], v17 offset:21248\n"
    "v_add_f32 v6, 1.0, v6\n"
    "v_add_f32 v7, 1.0, v7\n"
    "v_add_f32 v8, 1.0, v8\n"
    "v_add_f32 v9, 1.0, v9\n"
    "v_mfma_f32_32x32x16_bf16 v[64:79], a[116:119], a[8:11], v[64:79]\n"
    "ds_read_b64_tr_b16 a[104:105], v16 offset:21504\n"
    "ds_read_b64_tr_b16 a[106:107], v17 offset:21504\n"
    "v_rcp_f32 v6, v6\n"
    "v_rcp_f32 v7, v7\n"
    "v_rcp_f32 v8, v8\n"
    "v_mfma_f32_32x32x16_bf16 v[80:95], a[120:123], a[8:11], v[80:95]\n"
    "ds_read_b64_tr_b16 a[108:109], v16 offset:21760\n"
    "ds_read_b64_tr_b16 a[110:111], v17 offset:21760\n"
    "v_rcp_f32 v9, v9\n"
    "v_mul_f32 v6, v116, v6\n"
    "v_mul_f32 v7, v117, v7\n"
    "v_mul_f32 v8, v118, v8\n"
    "v_mul_f32 v9, v119, v9\n"
    "v_mfma_f32_32x32x16_bf16 v[96:111], a[124:127], a[8:11], v[96:111]\n"
    "s_waitcnt lgkmcnt(4)\n"
    "s_barrier\n"
    "s_add_u32 s20, s71, 146432\n"
    "v_mfma_f32_32x32x16_bf16 v[48:63], a[128:131], a[12:15], v[48:63]\n"
    "ds_read_b64_tr_b16 a[112:113], v16 offset:22016\n"
    "ds_read_b64_tr_b16 a[114:115], v17 offset:22016\n"
    "ds_read_b64_tr_b16 a[116:117], v16 offset:22272\n"
    "ds_read_b64_tr_b16 a[118:119], v17 offset:22272\n"
    "v_accvgpr_read_b32 v40, a160\n"
    "v_accvgpr_read_b32 v41, a161\n"
    "v_cvt_pk_bf16_f32 v114, v6, v7\n"
    "v_mfma_f32_32x32x16_bf16 v[64:79], a[132:135], a[12:15], v[64:79]\n"
    "ds_read_b64_tr_b16 a[120:121], v16 offset:22528\n"
    "ds_read_b64_tr_b16 a[122:123], v17 offset:22528\n"
    "buffer_load_dword v42, v28, s[60:63], s9 offen sc1\n"
    "buffer_load_dwordx4 v19, s[48:51], s20 offen offset:1024 lds\n"
    "v_cvt_pk_bf16_f32 v115, v8, v9\n"
    "v_accvgpr_write_b32 a164, v112\n"
    "v_mfma_f32_32x32x16_bf16 v[80:95], a[136:139], a[12:15], v[80:95]\n"
    "s_add_u32 s10, s10, s82\n"
    "s_and_b32 s10, s10, 7\n"
    "s_mul_i32 s20, s10, 82944\n"
    "s_add_u32 s87, s72, s20\n"
    "s_add_u32 m0, s68, 34816\n"
    "ds_read_b64_tr_b16 a[124:125], v16 offset:22784\n"
    "ds_read_b64_tr_b16 a[126:127], v17 offset:22784\n"
    "v_accvgpr_write_b32 a165, v113\n"
    "v_mfma_f32_32x32x16_bf16 v[96:111], a[140:143], a[12:15], v[96:111]\n"
    "s_waitcnt lgkmcnt(4)\n"
    "ds_read_b64_tr_b16 a[128:129], v16 offset:23040\n"
    "ds_read_b64_tr_b16 a[130:131], v17 offset:23040\n"
    "buffer_load_dwordx4 v14, s[56:59], s87 offen lds\n"
    "v_accvgpr_write_b32 a166, v114\n"
    "v_accvgpr_write_b32 a167, v115\n"
    "v_mfma_f32_32x32x16_bf16 v[48:63], a[104:107], a[16:19], v[48:63]\n"
    "s_add_u32 s21, s83, 0\n"
    "ds_read_b64_tr_b16 a[132:133], v16 offset:23296\n"
    "ds_read_b64_tr_b16 a[134:135], v17 offset:23296\n"
    "buffer_store_dwordx4 a[164:167], v25, s[60:63], s21 offen sc1\n"
    "v_exp_f32 v2, v120\n"
    "v_exp_f32 v3, v121\n"
    "v_mfma_f32_32x32x16_bf16 v[64:79], a[108:111], a[16:19], v[64:79]\n"
    "ds_read_b64_tr_b16 a[136:137], v16 offset:23552\n"
    "ds_read_b64_tr_b16 a[138:139], v17 offset:23552\n"
    "v_exp_f32 v4, v122\n"
    "v_exp_f32 v5, v123\n"
    "v_add_f32 v2, 1.0, v2\n"
    "v_mfma_f32_32x32x16_bf16 v[80:95], a[112:115], a[16:19], v[80:95]\n"
    "ds_read_b64_tr_b16 a[140:141], v16 offset:23808\n"
    "ds_read_b64_tr_b16 a[142:143], v17 offset:23808\n"
    "v_add_f32 v3, 1.0, v3\n"
    "v_add_f32 v4, 1.0, v4\n"
    "v_add_f32 v5, 1.0, v5\n"
    "v_rcp_f32 v2, v2\n"
    "v_mfma_f32_32x32x16_bf16 v[96:111], a[116:119], a[16:19], v[96:111]\n"
    "s_waitcnt lgkmcnt(4)\n"
    "ds_read_b64_tr_b16 a[104:105], v16 offset:24064\n"
    "ds_read_b64_tr_b16 a[106:107], v17 offset:24064\n"
    "v_rcp_f32 v3, v3\n"
    "v_rcp_f32 v4, v4\n"
    "v_mfma_f32_32x32x16_bf16 v[48:63], a[120:123], a[20:23], v[48:63]\n"
    "s_add_u32 m0, s68, 15360\n"
    "s_add_u32 s20, s71, 294912\n"
    "ds_read_b64_tr_b16 a[108:109], v16 offset:24320\n"
    "ds_read_b64_tr_b16 a[110:111], v17 offset:24320\n"
    "v_rcp_f32 v5, v5\n"
    "v_mul_f32 v2, v120, v2\n"
    "v_mul_f32 v3, v121, v3\n"
    "v_mul_f32 v4, v122, v4\n"
    "v_mul_f32 v5, v123, v5\n"
    "v_mfma_f32_32x32x16_bf16 v[64:79], a[124:127], a[20:23], v[64:79]\n"
    "ds_read_b64_tr_b16 a[112:113], v16 offset:24576\n"
    "ds_read_b64_tr_b16 a[114:115], v17 offset:24576\n"
    "buffer_load_dwordx4 v19, s[48:51], s20 offen lds\n"
    "v_cvt_pk_bf16_f32 v116, v2, v3\n"
    "v_cvt_pk_bf16_f32 v117, v4, v5\n"
    "v_mfma_f32_32x32x16_bf16 v[80:95], a[128:131], a[20:23], v[80:95]\n"
    "s_add_u32 s20, s71, 441344\n"
    "ds_read_b64_tr_b16 a[116:117], v16 offset:24832\n"
    "ds_read_b64_tr_b16 a[118:119], v17 offset:24832\n"
    "buffer_load_dwordx4 v19, s[48:51], s20 offen offset:1024 lds\n"
    "v_exp_f32 v6, v124\n"
    "v_exp_f32 v7, v125\n"
    "v_mfma_f32_32x32x16_bf16 v[96:111], a[132:135], a[20:23], v[96:111]\n"
    "s_waitcnt lgkmcnt(4)\n"
    "s_add_u32 s10, s10, s82\n"
    "s_and_b32 s10, s10, 7\n"
    "s_mul_i32 s20, s10, 82944\n"
    "s_add_u32 s87, s72, s20\n"
    "s_add_u32 m0, s68, 35840\n"
    "ds_read_b64_tr_b16 a[120:121], v16 offset:25088\n"
    "ds_read_b64_tr_b16 a[122:123], v17 offset:25088\n"
    "v_mfma_f32_32x32x16_bf16 v[48:63], a[136:139], a[24:27], v[48:63]\n"
    "ds_read_b64_tr_b16 a[124:125], v16 offset:25344\n"
    "ds_read_b64_tr_b16 a[126:127], v17 offset:25344\n"
    "buffer_load_dwordx4 v14, s[56:59], s87 offen lds\n"
    "v_exp_f32 v8, v126\n"
    "v_exp_f32 v9, v127\n"
    "v_mfma_f32_32x32x16_bf16 v[64:79], a[140:143], a[24:27], v[64:79]\n"
    "ds_read_b64_tr_b16 a[128:129], v16 offset:25600\n"
    "ds_read_b64_tr_b16 a[130:131], v17 offset:25600\n"
    "v_add_f32 v6, 1.0, v6\n"
    "v_add_f32 v7, 1.0, v7\n"
    "v_add_f32 v8, 1.0, v8\n"
    "v_add_f32 v9, 1.0, v9\n"
    "v_mfma_f32_32x32x16_bf16 v[80:95], a[104:107], a[24:27], v[80:95]\n"
    "s_cmp_eq_u32 s11, 1\n"
    "s_cselect_b32 s20, 4290248704, s81\n"
    "s_add_u32 s71, s71, s20\n"
    "s_add_u32 m0, s68, 17408\n"
    "ds_read_b64_tr_b16 a[132:133], v16 offset:25856\n"
    "ds_read_b64_tr_b16 a[134:135], v17 offset:25856\n"
    "v_rcp_f32 v6, v6\n"
    "v_mfma_f32_32x32x16_bf16 v[96:111], a[108:111], a[24:27], v[96:111]\n"
    "s_waitcnt lgkmcnt(4)\n"
    "ds_read_b64_tr_b16 a[136:137], v16 offset:26112\n"
    "ds_read_b64_tr_b16 a[138:139], v17 offset:26112\n"
    "buffer_load_dwordx4 v19, s[48:51], s71 offen lds\n"
    "v_rcp_f32 v7, v7\n"
    "v_rcp_f32 v8, v8\n"
    "v_mfma_f32_32x32x16_bf16 v[48:63], a[112:115], a[28:31], v[48:63]\n"
    "s_add_u32 s20, s71, 146432\n"
    "ds_read_b64_tr_b16 a[140:141], v16 offset:26368\n"
    "ds_read_b64_tr_b16 a[142:143], v17 offset:26368\n"
    "buffer_load_dwordx4 v19, s[48:51], s20 offen offset:1024 lds\n"
    "v_rcp_f32 v9, v9\n"
    "v_mul_f32 v6, v124, v6\n"
    "v_mul_f32 v7, v125, v7\n"
    "v_mfma_f32_32x32x16_bf16 v[64:79], a[116:119], a[28:31], v[64:79]\n"
    "s_waitcnt vmcnt(8)\n"
    "s_add_u32 s10, s10, s82\n"
    "s_and_b32 s10, s10, 7\n"
    "ds_read_b64_tr_b16 a[104:105], v16 offset:26624\n"
    "ds_read_b64_tr_b16 a[106:107], v17 offset:26624\n"
    "v_cmp_ne_u32 vcc, v29, v42\n"
    "s_cmp_lg_u64 vcc, 0\n"
    "s_cbranch_scc1 L_slow0_%=\n"
    "L_ret0_%=:\n"
    "v_mfma_f32_32x32x16_bf16 v[80:95], a[120:123], a[28:31], v[80:95]\n"
    "ds_read_b64_tr_b16 a[108:109], v16 offset:26880\n"
    "ds_read_b64_tr_b16 a[110:111], v17 offset:26880\n"
    "buffer_load_dwordx4 v[32:35], v40, s[60:63], s95 offen sc1\n"
    "buffer_load_dwordx4 v[36:39], v40, s[60:63], s95 offen offset:16 sc1\n"
    "v_mul_f32 v8, v126, v8\n"
    "v_mfma_f32_32x32x16_bf16 v[96:111], a[124:127], a[28:31], v[96:111]\n"
    "s_waitcnt lgkmcnt(4)\n"
    "ds_read_b64_tr_b16 a[112:113], v16 offset:27136\n"
    "ds_read_b64_tr_b16 a[114:115], v17 offset:27136\n"
    "v_mul_f32 v9, v127, v9\n"
    "v_cvt_pk_bf16_f32 v118, v6, v7\n"
    "v_cvt_pk_bf16_f32 v119, v8, v9\n"
    "v_accvgpr_write_b32 a168, v116\n"
    "v_accvgpr_write_b32 a169, v117\n"
    "v_accvgpr_write_b32 a170, v118\n"
    "v_mfma_f32_32x32x16_bf16 v[48:63], a[128:131], a[32:35], v[48:63]\n"
    "s_mul_i32 s20, s10, 82944\n"
    "s_add_u32 s87, s72, s20\n"
    "s_add_u32 m0, s68, 36864\n"
    "ds_read_b64_tr_b16 a[116:117], v16 offset:27392\n"
    "ds_read_b64_tr_b16 a[118:119], v17 offset:27392\n"
    "v_accvgpr_write_b32 a171, v119\n"
    "v_exp_f32 v2, v128\n"
    "v_mfma_f32_32x32x16_bf16 v[64:79], a[132:135], a[32:35], v[64:79]\n"
    "s_add_u32 s21, s83, 4096\n"
    "ds_read_b64_tr_b16 a[120:121], v16 offset:27648\n"
    "ds_read_b64_tr_b16 a[122:123], v17 offset:27648\n"
    "buffer_store_dwordx4 a[168:171], v25, s[60:63], s21 offen sc1\n"
    "v_exp_f32 v3, v129\n"
    "v_mfma_f32_32x32x16_bf16 v[80:95], a[136:139], a[32:35], v[80:95]\n"
    "ds_read_b64_tr_b16 a[124:125], v16 offset:27904\n"
    "ds_read_b64_tr_b16 a[126:127], v17 offset:27904\n"
    "v_exp_f32 v4, v130\n"
    "v_exp_f32 v5, v131\n"
    "v_add_f32 v2, 1.0, v2\n"
    "v_add_f32 v3, 1.0, v3\n"
    "v_mfma_f32_32x32x16_bf16 v[96:111], a[140:143], a[32:35], v[96:111]\n"
    "s_waitcnt lgkmcnt(4)\n"
    "s_barrier\n"
    "v_add_f32 v4, 1.0, v4\n"
    "v_mfma_f32_32x32x16_bf16 v[48:63], a[104:107], a[36:39], v[48:63]\n"
    "ds_read_b64_tr_b16 a[128:129], v16 offset:28160\n"
    "ds_read_b64_tr_b16 a[130:131], v17 offset:28160\n"
    "ds_read_b64_tr_b16 a[132:133], v16 offset:28416\n"
    "ds_read_b64_tr_b16 a[134:135], v17 offset:28416\n"
    "v_add_f32 v5, 1.0, v5\n"
    "v_rcp_f32 v2, v2\n"
    "v_mfma_f32_32x32x16_bf16 v[64:79], a[108:111], a[36:39], v[64:79]\n"
    "ds_read_b64_tr_b16 a[136:137], v16 offset:28672\n"
    "ds_read_b64_tr_b16 a[138:139], v17 offset:28672\n"
    "v_rcp_f32 v3, v3\n"
    "v_rcp_f32 v4, v4\n"
    "v_mfma_f32_32x32x16_bf16 v[80:95], a[112:115], a[36:39], v[80:95]\n"
    "ds_read_b64_tr_b16 a[140:141], v16 offset:28928\n"
    "ds_read_b64_tr_b16 a[142:143], v17 offset:28928\n"
    "buffer_load_dwordx4 v14, s[56:59], s87 offen lds\n"
    "v_rcp_f32 v5, v5\n"
    "v_mul_f32 v2, v128, v2\n"
    "v_mul_f32 v3, v129, v3\n"
    "v_mul_f32 v4, v130, v4\n"
    "v_mfma_f32_32x32x16_bf16 v[96:111], a[116:119], a[36:39], v[96:111]\n"
    "s_waitcnt lgkmcnt(4)\n"
    "ds_read_b64_tr_b16 a[104:105], v16 offset:29184\n"
    "ds_read_b64_tr_b16 a[106:107], v17 offset:29184\n"
    "v_mul_f32 v5, v131, v5\n"
    "v_cvt_pk_bf16_f32 v128, v2, v3\n"
    "v_cvt_pk_bf16_f32 v129, v4, v5\n"
    "v_exp_f32 v6, v132\n"
    "v_mfma_f32_32x32x16_bf16 v[48:63], a[120:123], a[40:43], v[48:63]\n"
    "s_add_u32 m0, s68, 19456\n"
    "s_add_u32 s20, s71, 294912\n"
    "ds_read_b64_tr_b16 a[108:109], v16 offset:29440\n"
    "ds_read_b64_tr_b16 a[110:111], v17 offset:29440\n"
    "v_exp_f32 v7, v133\n"
    "v_exp_f32 v8, v134\n"
    "v_mfma_f32_32x32x16_bf16 v[64:79], a[124:127], a[40:43], v[64:79]\n"
    "ds_read_b64_tr_b16 a[112:113], v16 offset:29696\n"
    "ds_read_b64_tr_b16 a[114:115], v17 offset:29696\n"
    "buffer_load_dwordx4 v19, s[48:51], s20 offen lds\n"
    "v_exp_f32 v9, v135\n"
    "v_add_f32 v6, 1.0, v6\n"
    "v_add_f32 v7, 1.0, v7\n"
    "v_mfma_f32_32x32x16_bf16 v[80:95], a[128:131], a[40:43], v[80:95]\n"
    "s_add_u32 s20, s71, 441344\n"
    "ds_read_b64_tr_b16 a[116:117], v16 offset:29952\n"
    "ds_read_b64_tr_b16 a[118:119], v17 offset:29952\n"
    "buffer_load_dwordx4 v19, s[48:51], s20 offen offset:1024 lds\n"
    "v_add_f32 v8, 1.0, v8\n"
    "v_add_f32 v9, 1.0, v9\n"
    "v_mfma_f32_32x32x16_bf16 v[96:111], a[132:135], a[40:43], v[96:111]\n"
    "s_waitcnt lgkmcnt(4)\n"
    "s_add_u32 s10, s10, s82\n"
    "s_and_b32 s10, s10, 7\n"
    "s_mul_i32 s20, s10, 82944\n"
    "s_add_u32 s87, s72, s20\n"
    "s_add_u32 m0, s68, 37888\n"
    "ds_read_b64_tr_b16 a[120:121], v16 offset:30208\n"
    "ds_read_b64_tr_b16 a[122:123], v17 offset:30208\n"
    "v_accvgpr_read_b32 v19, a162\n"
    "v_mfma_f32_32x32x16_bf16 v[48:63], a[136:139], a[44:47], v[48:63]\n"
    "ds_read_b64_tr_b16 a[124:125], v16 offset:30464\n"
    "ds_read_b64_tr_b16 a[126:127], v17 offset:30464\n"
    "buffer_load_dwordx4 v14, s[56:59], s87 offen lds\n"
    "v_rcp_f32 v6, v6\n"
    "v_mfma_f32_32x32x16_bf16 v[64:79], a[140:143], a[44:47], v[64:79]\n"
    "ds_read_b64_tr_b16 a[128:129], v16 offset:30720\n"
    "ds_read_b64_tr_b16 a[130:131], v17 offset:30720\n"
    "v_rcp_f32 v7, v7\n"
    "v_rcp_f32 v8, v8\n"
    "v_rcp_f32 v9, v9\n"
    "v_mul_f32 v6, v132, v6\n"
    "v_mfma_f32_32x32x16_bf16 v[80:95], a[104:107], a[44:47], v[80:95]\n"
    "s_add_u32 m0, s68, 21504\n"
    "ds_read_b64_tr_b16 a[132:133], v16 offset:30976\n"
    "ds_read_b64_tr_b16 a[134:135], v17 offset:30976\n"
    "v_mul_f32 v7, v133, v7\n"
    "v_mul_f32 v8, v134, v8\n"
    "v_mul_f32 v9, v135, v9\n"
    "v_cvt_pk_bf16_f32 v130, v6, v7\n"
    "v_cvt_pk_bf16_f32 v131, v8, v9\n"
    "v_mfma_f32_32x32x16_bf16 v[96:111], a[108:111], a[44:47], v[96:111]\n"
    "s_waitcnt lgkmcnt(4)\n"
    "ds_read_b64_tr_b16 a[136:137], v16 offset:31232\n"
    "ds_read_b64_tr_b16 a[138:139], v17 offset:31232\n"
    "buffer_load_dwordx4 v23, s[52:55], s78 offen lds\n"
    "v_accvgpr_write_b32 a172, v128\n"
    "v_accvgpr_write_b32 a173, v129\n"
    "v_accvgpr_write_b32 a174, v130\n"
    "v_mfma_f32_32x32x16_bf16 v[48:63], a[112:115], a[48:51], v[48:63]\n"
    "ds_read_b64_tr_b16 a[140:141], v16 offset:31488\n"
    "ds_read_b64_tr_b16 a[142:143], v17 offset:31488\n"
    "v_accvgpr_write_b32 a175, v131\n"
    "v_exp_f32 v2, v136\n"
    "v_exp_f32 v3, v137\n"
    "v_mfma_f32_32x32x16_bf16 v[64:79], a[116:119], a[48:51], v[64:79]\n"
    "s_add_u32 s21, s83, 8192\n"
    "ds_read_b64_tr_b16 a[104:105], v16 offset:31744\n"
    "ds_read_b64_tr_b16 a[106:107], v17 offset:31744\n"
    "buffer_store_dwordx4 a[172:175], v25, s[60:63], s21 offen sc1\n"
    "v_exp_f32 v4, v138\n"
    "v_mfma_f32_32x32x16_bf16 v[80:95], a[120:123], a[48:51], v[80:95]\n"
    "s_waitcnt vmcnt(7)\n"
    "ds_read_b64_tr_b16 a[108:109], v16 offset:32000\n"
    "ds_read_b64_tr_b16 a[110:111], v17 offset:32000\n"
    "buffer_load_dword v13, v27, s[60:63], s7 offen offset:2048 sc1\n"
    "v_permlane32_swap_b32 v32, v36\n"
    "v_permlane32_swap_b32 v33, v37\n"
    "v_mfma_f32_32x32x16_bf16 v[96:111], a[124:127], a[48:51], v[96:111]\n"
    "s_waitcnt lgkmcnt(4)\n"
    "ds_read_b64_tr_b16 a[112:113], v16 offset:32256\n"
    "ds_read_b64_tr_b16 a[114:115], v17 offset:32256\n"
    "v_permlane32_swap_b32 v34, v38\n"
    "v_permlane32_swap_b32 v35, v39\n"
    "v_add_f32 v32, v32, v36\n"
    "v_mfma_f32_32x32x16_bf16 v[48:63], a[128:131], a[52:55], v[48:63]\n"
    "ds_read_b64_tr_b16 a[116:117], v16 offset:32512\n"
    "ds_read_b64_tr_b16 a[118:119], v17 offset:32512\n"
    "v_add_f32 v33, v33, v37\n"
    "v_add_f32 v34, v34, v38\n"
    "v_add_f32 v35, v35, v39\n"
    "s_nop 0\n"
    "v_permlane16_swap_b32 v32, v34\n"
    "v_mfma_f32_32x32x16_bf16 v[64:79], a[132:135], a[52:55], v[64:79]\n"
    "ds_read_b64_tr_b16 a[120:121], v16\n"
    "ds_read_b64_tr_b16 a[122:123], v17\n"
    "v_permlane16_swap_b32 v33, v35\n"
    "v_add_f32 v32, v32, v34\n"
    "v_add_f32 v33, v33, v35\n"
    "v_exp_f32 v5, v139\n"
    "v_mfma_f32_32x32x16_bf16 v[80:95], a[136:139], a[52:55], v[80:95]\n"
    "ds_read_b64_tr_b16 a[124:125], v16 offset:256\n"
    "ds_read_b64_tr_b16 a[126:127], v17 offset:256\n"
    "v_add_f32_dpp v34, v32, v32 row_ror:4 row_mask:0xf bank_mask:0xf\n"
    "v_add_f32_dpp v35, v33, v33 row_ror:4 row_mask:0xf bank_mask:0xf\n"
    "v_add_f32 v2, 1.0, v2\n"
    "v_add_f32 v3, 1.0, v3\n"
    "v_add_f32 v4, 1.0, v4\n"
    "v_mfma_f32_32x32x16_bf16 v[96:111], a[140:143], a[52:55], v[96:111]\n"
    "s_waitcnt lgkmcnt(4)\n"
    "s_add_u32 m0, s68, 22528\n"
    "ds_read_b64_tr_b16 a[128:129], v16 offset:512\n"
    "ds_read_b64_tr_b16 a[130:131], v17 offset:512\n"
    "buffer_load_dwordx4 v21, s[52:55], s78 offen lds\n"
    "v_add_f32_dpp v32, v34, v34 row_ror:8 row_mask:0xf bank_mask:0xf\n"
    "v_add_f32_dpp v33, v35, v35 row_ror:8 row_mask:0xf bank_mask:0xf\n"
    "v_mfma_f32_32x32x16_bf16 v[48:63], a[104:107], a[56:59], v[48:63]\n"
    "ds_read_b64_tr_b16 a[132:133], v16 offset:768\n"
    "ds_read_b64_tr_b16 a[134:135], v17 offset:768\n"
    "buffer_store_dwordx2 v[32:33], v41, s[60:63], s15 offen sc1\n"
    "v_add_f32 v5, 1.0, v5\n"
    "v_rcp_f32 v2, v2\n"
    "v_mfma_f32_32x32x16_bf16 v[64:79], a[108:111], a[56:59], v[64:79]\n"
    "ds_read_b64_tr_b16 a[136:137], v16 offset:1024\n"
    "ds_read_b64_tr_b16 a[138:139], v17 offset:1024\n"
    "v_rcp_f32 v3, v3\n"
    "v_rcp_f32 v4, v4\n"
    "v_mfma_f32_32x32x16_bf16 v[80:95], a[112:115], a[56:59], v[80:95]\n"
    "ds_read_b64_tr_b16 a[140:141], v16 offset:1280\n"
    "ds_read_b64_tr_b16 a[142:143], v17 offset:1280\n"
    "v_rcp_f32 v5, v5\n"
    "v_mul_f32 v2, v136, v2\n"
    "v_mul_f32 v3, v137, v3\n"
    "v_mul_f32 v4, v138, v4\n"
    "v_mul_f32 v5, v139, v5\n"
    "v_mfma_f32_32x32x16_bf16 v[96:111], a[116:119], a[56:59], v[96:111]\n"
    "s_waitcnt lgkmcnt(4)\n"
    "s_barrier\n"
    "s_add_u32 s20, s78, 17408\n"
    "v_cvt_pk_bf16_f32 v132, v2, v3\n"
    "v_mfma_f32_32x32x16_bf16 v[48:63], a[120:123], a[60:63], v[48:63]\n"
    "ds_read_b64_tr_b16 a[104:105], v16 offset:1536\n"
    "ds_read_b64_tr_b16 a[106:107], v17 offset:1536\n"
    "ds_read_b64_tr_b16 a[108:109], v16 offset:1792\n"
    "ds_read_b64_tr_b16 a[110:111], v17 offset:1792\n"
    "buffer_load_dwordx4 v21, s[52:55], s20 offen offset:1024 lds\n"
    "v_cvt_pk_bf16_f32 v133, v4, v5\n"
    "v_mfma_f32_32x32x16_bf16 v[64:79], a[124:127], a[60:63], v[64:79]\n"
    "s_add_u32 m0, s68, 24576\n"
    "ds_read_b64_tr_b16 a[112:113], v16 offset:2048\n"
    "ds_read_b64_tr_b16 a[114:115], v17 offset:2048\n"
    "buffer_load_dwordx4 v21, s[52:55], s79 offen lds\n"
    "v_exp_f32 v6, v140\n"
    "v_mfma_f32_32x32x16_bf16 v[80:95], a[128:131], a[60:63], v[80:95]\n"
    "s_add_u32 s20, s79, 17408\n"
    "ds_read_b64_tr_b16 a[116:117], v16 offset:2304\n"
    "ds_read_b64_tr_b16 a[118:119], v17 offset:2304\n"
    "v_exp_f32 v7, v141\n"
    "v_exp_f32 v8, v142\n"
    "v_exp_f32 v9, v143\n"
    "v_mfma_f32_32x32x16_bf16 v[96:111], a[132:135], a[60:63], v[96:111]\n"
    "s_waitcnt lgkmcnt(4)\n"
    "ds_read_b64_tr_b16 a[120:121], v16 offset:2560\n"
    "ds_read_b64_tr_b16 a[122:123], v17 offset:2560\n"
    "buffer_load_dwordx4 v21, s[52:55], s20 offen offset:1024 lds\n"
    "v_add_f32 v6, 1.0, v6\n"
    "v_add_f32 v7, 1.0, v7\n"
    "v_add_f32 v8, 1.0, v8\n"
    "v_add_f32 v9, 1.0, v9\n"
    "v_mfma_f32_32x32x16_bf16 v[48:63], a[136:139], a[64:67], v[48:63]\n"
    "s_add_u32 m0, s68, 26624\n"
    "ds_read_b64_tr_b16 a[124:125], v16 offset:2816\n"
    "ds_read_b64_tr_b16 a[126:127], v17 offset:2816\n"
    "buffer_load_dwordx4 v21, s[52:55], s80 offen lds\n"
    "v_rcp_f32 v6, v6\n"
    "v_mfma_f32_32x32x16_bf16 v[64:79], a[140:143], a[64:67], v[64:79]\n"
    "s_add_u32 s20, s80, 17408\n"
    "ds_read_b64_tr_b16 a[128:129], v16 offset:3072\n"
    "ds_read_b64_tr_b16 a[130:131], v17 offset:3072\n"
    "v_rcp_f32 v7, v7\n"
    "v_rcp_f32 v8, v8\n"
    "v_rcp_f32 v9, v9\n"
    "v_mfma_f32_32x32x16_bf16 v[80:95], a[104:107], a[64:67], v[80:95]\n"
    "s_waitcnt vmcnt(6)\n"
    "ds_read_b64_tr_b16 a[132:133], v16 offset:3328\n"
    "ds_read_b64_tr_b16 a[134:135], v17 offset:3328\n"
    "buffer_load_dwordx4 v21, s[52:55], s20 offen offset:1024 lds\n"
    "v_cmp_ne_u32 vcc, v29, v13\n"
    "s_cmp_lg_u64 vcc, 0\n"
    "s_cbranch_scc1 L_slow2_%=\n"
    "L_ret2_%=:\n"
    "v_mul_f32 v6, v140, v6\n"
    "v_mfma_f32_32x32x16_bf16 v[96:111], a[108:111], a[64:67], v[96:111]\n"
    "s_waitcnt lgkmcnt(4)\n"
    "s_add_u32 m0, s68, 28672\n"
    "ds_read_b64_tr_b16 a[136:137], v16 offset:3584\n"
    "ds_read_b64_tr_b16 a[138:139], v17 offset:3584\n"
    "buffer_load_dwordx4 v23, s[52:55], s77 offen lds\n"
    "v_mul_f32 v7, v141, v7\n"
    "v_mul_f32 v8, v142, v8\n"
    "v_mfma_f32_32x32x16_bf16 v[48:63], a[112:115], a[68:71], v[48:63]\n"
    "ds_read_b64_tr_b16 a[140:141], v16 offset:3840\n"
    "ds_read_b64_tr_b16 a[142:143], v17 offset:3840\n"
    "v_mul_f32 v9, v143, v9\n"
    "v_cvt_pk_bf16_f32 v134, v6, v7\n"
    "v_cvt_pk_bf16_f32 v135, v8, v9\n"
    "v_accvgpr_write_b32 a176, v132\n"
    "v_accvgpr_write_b32 a177, v133\n"
    "v_mfma_f32_32x32x16_bf16 v[64:79], a[116:119], a[68:71], v[64:79]\n"
    "ds_read_b64_tr_b16 a[104:105], v16 offset:4096\n"
    "ds_read_b64_tr_b16 a[106:107], v17 offset:4096\n"
    "v_accvgpr_write_b32 a178, v134\n"
    "v_accvgpr_write_b32 a179, v135\n"
    "v_exp_f32 v2, v144\n"
    "v_mfma_f32_32x32x16_bf16 v[80:95], a[120:123], a[68:71], v[80:95]\n"
    "s_add_u32 s21, s83, 12288\n"
    "ds_read_b64_tr_b16 a[108:109], v16 offset:4352\n"
    "ds_read_b64_tr_b16 a[110:111], v17 offset:4352\n"
    "buffer_store_dwordx4 a[176:179], v25, s[60:63], s21 offen sc1\n"
    "v_exp_f32 v3, v145\n"
    "v_exp_f32 v4, v146\n"
    "v_mfma_f32_32x32x16_bf16 v[96:111], a[124:127], a[68:71], v[96:111]\n"
    "s_waitcnt lgkmcnt(4)\n"
    "ds_read_b64_tr_b16 a[112:113], v16 offset:4608\n"
    "ds_read_b64_tr_b16 a[114:115], v17 offset:4608\n"
    "v_exp_f32 v5, v147\n"
    "v_add_f32 v2, 1.0, v2\n"
    "v_add_f32 v3, 1.0, v3\n"
    "v_add_f32 v4, 1.0, v4\n"
    "v_mfma_f32_32x32x16_bf16 v[48:63], a[128:131], a[72:75], v[48:63]\n"
    "ds_read_b64_tr_b16 a[116:117], v16 offset:4864\n"
    "ds_read_b64_tr_b16 a[118:119], v17 offset:4864\n"
    "v_add_f32 v5, 1.0, v5\n"
    "v_rcp_f32 v2, v2\n"
    "v_rcp_f32 v3, v3\n"
    "v_mfma_f32_32x32x16_bf16 v[64:79], a[132:135], a[72:75], v[64:79]\n"
    "ds_read_b64_tr_b16 a[120:121], v16 offset:5120\n"
    "ds_read_b64_tr_b16 a[122:123], v17 offset:5120\n"
    "v_rcp_f32 v4, v4\n"
    "v_rcp_f32 v5, v5\n"
    "v_mul_f32 v2, v144, v2\n"
    "v_mfma_f32_32x32x16_bf16 v[80:95], a[136:139], a[72:75], v[80:95]\n"
    "s_add_u32 m0, s68, 29696\n"
    "s_add_u32 s22, s84, 32768\n"
    "ds_read_b64_tr_b16 a[124:125], v16 offset:5376\n"
    "ds_read_b64_tr_b16 a[126:127], v17 offset:5376\n"
    "v_mul_f32 v3, v145, v3\n"
    "v_mul_f32 v4, v146, v4\n"
    "v_mul_f32 v5, v147, v5\n"
    "v_mfma_f32_32x32x16_bf16 v[96:111], a[140:143], a[72:75], v[96:111]\n"
    "s_waitcnt vmcnt(7) lgkmcnt(4)\n"
    "ds_read_b64_tr_b16 a[128:129], v16 offset:5632\n"
    "ds_read_b64_tr_b16 a[130:131], v17 offset:5632\n"
    "buffer_store_dword v29, v26, s[60:63], s8 offen offset:2048 sc1\n"
    "v_cvt_pk_bf16_f32 v144, v2, v3\n"
    "v_mfma_f32_32x32x16_bf16 v[48:63], a[104:107], a[76:79], v[48:63]\n"
    "ds_read_b64_tr_b16 a[132:133], v16 offset:5888\n"
    "ds_read_b64_tr_b16 a[134:135], v17 offset:5888\n"
    "v_cvt_pk_bf16_f32 v145, v4, v5\n"
    "v_exp_f32 v6, v148\n"
    "v_exp_f32 v7, v149\n"
    "v_mfma_f32_32x32x16_bf16 v[64:79], a[108:111], a[76:79], v[64:79]\n"
    "ds_read_b64_tr_b16 a[136:137], v16 offset:6144\n"
    "ds_read_b64_tr_b16 a[138:139], v17 offset:6144\n"
    "buffer_load_dwordx4 v21, s[52:55], s77 offen lds\n"
    "v_exp_f32 v8, v150\n"
    "v_mfma_f32_32x32x16_bf16 v[80:95], a[112:115], a[76:79], v[80:95]\n"
    "ds_read_b64_tr_b16 a[140:141], v16 offset:6400\n"
    "ds_read_b64_tr_b16 a[142:143], v17 offset:6400\n"
    "buffer_load_dwordx4 a[220:223], v25, s[60:63], s22 offen sc1\n"
    "v_exp_f32 v9, v151\n"
    "v_mfma_f32_32x32x16_bf16 v[96:111], a[116:119], a[76:79], v[96:111]\n"
    "s_waitcnt lgkmcnt(4)\n"
    "s_add_u32 s20, s77, 17408\n"
    "s_add_u32 s22, s84, 36864\n"
    "ds_read_b64_tr_b16 a[104:105], v16 offset:6656\n"
    "ds_read_b64_tr_b16 a[106:107], v17 offset:6656\n"
    "v_add_f32 v6, 1.0, v6\n"
    "v_add_f32 v7, 1.0, v7\n"
    "v_mfma_f32_32x32x16_bf16 v[48:63], a[120:123], v[192:195], v[48:63]\n"
    "ds_read_b64_tr_b16 a[108:109], v16 offset:6912\n"
    "ds_read_b64_tr_b16 a[110:111], v17 offset:6912\n"
    "buffer_load_dwordx4 v21, s[52:55], s20 offen offset:1024 lds\n"
    "v_add_f32 v8, 1.0, v8\n"
    "v_add_f32 v9, 1.0, v9\n"
    "v_mfma_f32_32x32x16_bf16 v[64:79], a[124:127], v[192:195], v[64:79]\n"
    "s_lshr_b32 s29, s36, 1\n"
    "s_add_u32 m0, s68, 31744\n"
    "ds_read_b64_tr_b16 a[112:113], v16 offset:7168\n"
    "ds_read_b64_tr_b16 a[114:115], v17 offset:7168\n"
    "v_rcp_f32 v6, v6\n"
    "v_rcp_f32 v7, v7\n"
    "v_mfma_f32_32x32x16_bf16 v[80:95], a[128:131], v[192:195], v[80:95]\n"
    "s_lshl_b32 s29, s29, 8\n"
    "ds_read_b64_tr_b16 a[116:117], v16 offset:7424\n"
    "ds_read_b64_tr_b16 a[118:119], v17 offset:7424\n"
    "buffer_load_dwordx4 a[232:235], v25, s[60:63], s22 offen sc1\n"
    "v_rcp_f32 v8, v8\n"
    "v_mfma_f32_32x32x16_bf16 v[96:111], a[132:135], v[192:195], v[96:111]\n"
    "s_waitcnt lgkmcnt(4)\n"
    "s_barrier\n"
    "s_add_u32 s22, s85, 32768\n"
    "v_mfma_f32_32x32x16_bf16 v[48:63], a[136:139], v[196:199], v[48:63]\n"
    "s_and_b32 s19, s3, 1\n"
    "ds_read_b64_tr_b16 a[120:121], v16 offset:7680\n"
    "ds_read_b64_tr_b16 a[122:123], v17 offset:7680\n"
    "ds_read_b64_tr_b16 a[124:125], v16 offset:7936\n"
    "ds_read_b64_tr_b16 a[126:127], v17 offset:7936\n"
    "buffer_load_dwordx4 v23, s[52:55], s73 offen lds\n"
    "v_mfma_f32_32x32x16_bf16 v[64:79], a[140:143], v[196:199], v[64:79]\n"
    "s_lshl_b32 s19, s19, 1\n"
    "ds_read_b64_tr_b16 a[128:129], v16 offset:8192\n"
    "ds_read_b64_tr_b16 a[130:131], v17 offset:8192\n"
    "v_rcp_f32 v9, v9\n"
    "v_mul_f32 v6, v148, v6\n"
    "v_mul_f32 v7, v149, v7\n"
    "v_mfma_f32_32x32x16_bf16 v[80:95], a[104:107], v[196:199], v[80:95]\n"
    "s_lshl2_add_u32 s98, s35, s34\n"
    "ds_read_b64_tr_b16 a[132:133], v16 offset:8448\n"
    "ds_read_b64_tr_b16 a[134:135], v17 offset:8448\n"
    "v_mul_f32 v8, v150, v8\n"
    "v_mul_f32 v9, v151, v9\n"
    "v_cvt_pk_bf16_f32 v146, v6, v7\n"
    "v_cvt_pk_bf16_f32 v147, v8, v9\n"
    "v_accvgpr_write_b32 a180, v144\n"
    "v_mfma_f32_32x32x16_bf16 v[96:111], a[108:111], v[196:199], v[96:111]\n"
    "s_waitcnt lgkmcnt(4)\n"
    "s_add_u32 m0, s68, 0\n"
    "ds_read_b64_tr_b16 a[136:137], v16 offset:8704\n"
    "ds_read_b64_tr_b16 a[138:139], v17 offset:8704\n"
    "buffer_load_dwordx4 v21, s[52:55], s73 offen lds\n"
    "v_accvgpr_write_b32 a181, v145\n"
    "v_mfma_f32_32x32x16_bf16 v[48:63], a[112:115], v[200:203], v[48:63]\n"
    "s_lshl2_add_u32 s98, s98, s19\n"
    "s_add_u32 s20, s73, 17408\n"
    "ds_read_b64_tr_b16 a[140:141], v16 offset:8960\n"
    "ds_read_b64_tr_b16 a[142:143], v17 offset:8960\n"
    "v_accvgpr_write_b32 a182, v146\n"
    "v_accvgpr_write_b32 a183, v147\n"
    "v_mfma_f32_32x32x16_bf16 v[64:79], a[116:119], v[200:203], v[64:79]\n"
    "s_add_u32 s21, s83, 16384\n"
    "ds_read_b64_tr_b16 a[104:105], v16 offset:9216\n"
    "ds_read_b64_tr_b16 a[106:107], v17 offset:9216\n"
    "buffer_store_dwordx4 a[180:183], v25, s[60:63], s21 offen sc1\n"
    "v_exp_f32 v2, v152\n"
    "v_mfma_f32_32x32x16_bf16 v[80:95], a[120:123], v[200:203], v[80:95]\n"
    "s_lshl_b32 s98, s98, 14\n"
    "ds_read_b64_tr_b16 a[108:109], v16 offset:9472\n"
    "ds_read_b64_tr_b16 a[110:111], v17 offset:9472\n"
    "v_exp_f32 v3, v153\n"
    "v_exp_f32 v4, v154\n"
    "v_mfma_f32_32x32x16_bf16 v[96:111], a[124:127], v[200:203], v[96:111]\n"
    "s_waitcnt lgkmcnt(4)\n"
    "ds_read_b64_tr_b16 a[112:113], v16 offset:9728\n"
    "ds_read_b64_tr_b16 a[114:115], v17 offset:9728\n"
    "v_exp_f32 v5, v155\n"
    "v_add_f32 v2, 1.0, v2\n"
    "v_add_f32 v3, 1.0, v3\n"
    "v_mfma_f32_32x32x16_bf16 v[48:63], a[128:131], v[204:207], v[48:63]\n"
    "s_add_u32 s98, s98, s29\n"
    "ds_read_b64_tr_b16 a[116:117], v16 offset:9984\n"
    "ds_read_b64_tr_b16 a[118:119], v17 offset:9984\n"
    "v_add_f32 v4, 1.0, v4\n"
    "v_add_f32 v5, 1.0, v5\n"
    "v_rcp_f32 v2, v2\n"
    "v_mfma_f32_32x32x16_bf16 v[64:79], a[132:135], v[204:207], v[64:79]\n"
    "s_add_u32 s98, s98, 12587008\n"
    "ds_read_b64_tr_b16 a[120:121], v16 offset:10240\n"
    "ds_read_b64_tr_b16 a[122:123], v17 offset:10240\n"
    "v_rcp_f32 v3, v3\n"
    "v_rcp_f32 v4, v4\n"
    "v_mfma_f32_32x32x16_bf16 v[80:95], a[136:139], v[204:207], v[80:95]\n"
    "s_lshr_b32 s94, s36, 1\n"
    "ds_read_b64_tr_b16 a[124:125], v16 offset:10496\n"
    "ds_read_b64_tr_b16 a[126:127], v17 offset:10496\n"
    "buffer_load_dwordx4 a[224:227], v25, s[60:63], s22 offen sc1\n"
    "v_rcp_f32 v5, v5\n"
    "v_mul_f32 v2, v152, v2\n"
    "v_mfma_f32_32x32x16_bf16 v[96:111], a[140:143], v[204:207], v[96:111]\n"
    "s_waitcnt vmcnt(9) lgkmcnt(4)\n"
    "ds_read_b64_tr_b16 a[128:129], v16 offset:10752\n"
    "ds_read_b64_tr_b16 a[130:131], v17 offset:10752\n"
    "buffer_store_dword v29, v26, s[60:63], s6 offen sc1\n"
    "v_mul_f32 v3, v153, v3\n"
    "v_mul_f32 v4, v154, v4\n"
    "v_mfma_f32_32x32x16_bf16 v[48:63], a[104:107], v[208:211], v[48:63]\n"
    "s_mul_i32 s95, s94, 4608\n"
    "ds_read_b64_tr_b16 a[132:133], v16 offset:11008\n"
    "ds_read_b64_tr_b16 a[134:135], v17 offset:11008\n"
    "v_mul_f32 v5, v155, v5\n"
    "v_cvt_pk_bf16_f32 v148, v2, v3\n"
    "v_cvt_pk_bf16_f32 v149, v4, v5\n"
    "v_mfma_f32_32x32x16_bf16 v[64:79], a[108:111], v[208:211], v[64:79]\n"
    "s_add_u32 s95, s95, s97\n"
    "s_add_u32 s22, s85, 36864\n"
    "ds_read_b64_tr_b16 a[136:137], v16 offset:11264\n"
    "ds_read_b64_tr_b16 a[138:139], v17 offset:11264\n"
    "v_exp_f32 v6, v156\n"
    "v_mfma_f32_32x32x16_bf16 v[80:95], a[112:115], v[208:211], v[80:95]\n"
    "s_add_u32 s81, s98, 16384\n"
    "ds_read_b64_tr_b16 a[140:141], v16 offset:11520\n"
    "ds_read_b64_tr_b16 a[142:143], v17 offset:11520\n"
    "buffer_load_dwordx4 v21, s[52:55], s20 offen offset:1024 lds\n"
    "v_exp_f32 v7, v157\n"
    "v_mfma_f32_32x32x16_bf16 v[96:111], a[116:119], v[208:211], v[96:111]\n"
    "s_waitcnt lgkmcnt(4)\n"
    "s_add_u32 m0, s68, 2048\n"
    "ds_read_b64_tr_b16 a[104:105], v16 offset:11776\n"
    "ds_read_b64_tr_b16 a[106:107], v17 offset:11776\n"
    "buffer_load_dwordx4 v23, s[52:55], s74 offen lds\n"
    "v_exp_f32 v8, v158\n"
    "v_mfma_f32_32x32x16_bf16 v[48:63], a[120:123], v[212:215], v[48:63]\n"
    "s_lshl_b32 s77, s36, 7\n"
    "ds_read_b64_tr_b16 a[108:109], v16 offset:12032\n"
    "ds_read_b64_tr_b16 a[110:111], v17 offset:12032\n"
    "v_exp_f32 v9, v159\n"
    "v_add_f32 v6, 1.0, v6\n"
    "v_add_f32 v7, 1.0, v7\n"
    "v_mfma_f32_32x32x16_bf16 v[64:79], a[124:127], v[212:215], v[64:79]\n"
    "s_add_u32 s77, s77, s12\n"
    "ds_read_b64_tr_b16 a[112:113], v16 offset:12288\n"
    "ds_read_b64_tr_b16 a[114:115], v17 offset:12288\n"
    "v_add_f32 v8, 1.0, v8\n"
    "v_add_f32 v9, 1.0, v9\n"
    "v_rcp_f32 v6, v6\n"
    "v_mfma_f32_32x32x16_bf16 v[80:95], a[128:131], v[212:215], v[80:95]\n"
    "s_sub_u32 s77, s77, 2048\n"
    "s_add_u32 m0, s68, 3072\n"
    "ds_read_b64_tr_b16 a[116:117], v16 offset:12544\n"
    "ds_read_b64_tr_b16 a[118:119], v17 offset:12544\n"
    "v_rcp_f32 v7, v7\n"
    "v_rcp_f32 v8, v8\n"
    "v_mfma_f32_32x32x16_bf16 v[96:111], a[132:135], v[212:215], v[96:111]\n"
    "s_waitcnt lgkmcnt(4)\n"
    "ds_read_b64_tr_b16 a[120:121], v16 offset:12800\n"
    "ds_read_b64_tr_b16 a[122:123], v17 offset:12800\n"
    "buffer_load_dwordx4 a[236:239], v25, s[60:63], s22 offen sc1\n"
    "v_rcp_f32 v9, v9\n"
    "v_mfma_f32_32x32x16_bf16 v[48:63], a[136:139], v[216:219], v[48:63]\n"
    "s_lshr_b32 s29, s3, 1\n"
    "s_add_u32 s22, s86, 32768\n"
    "ds_read_b64_tr_b16 a[124:125], v16 offset:13056\n"
    "ds_read_b64_tr_b16 a[126:127], v17 offset:13056\n"
    "v_mul_f32 v6, v156, v6\n"
    "v_mul_f32 v7, v157, v7\n"
    "v_mul_f32 v8, v158, v8\n"
    "v_mul_f32 v9, v159, v9\n"
    "v_mfma_f32_32x32x16_bf16 v[64:79], a[140:143], v[216:219], v[64:79]\n"
    "s_lshl_b32 s29, s29, 7\n"
    "ds_read_b64_tr_b16 a[128:129], v16 offset:13312\n"
    "ds_read_b64_tr_b16 a[130:131], v17 offset:13312\n"
    "buffer_load_dwordx4 v21, s[52:55], s74 offen lds\n"
    "v_cvt_pk_bf16_f32 v150, v6, v7\n"
    "v_cvt_pk_bf16_f32 v151, v8, v9\n"
    "v_mfma_f32_32x32x16_bf16 v[80:95], a[104:107], v[216:219], v[80:95]\n"
    "s_lshl2_add_u32 s79, s35, s39\n"
    "ds_read_b64_tr_b16 a[132:133], v16 offset:13568\n"
    "ds_read_b64_tr_b16 a[134:135], v17 offset:13568\n"
    "buffer_load_dwordx4 a[228:231], v25, s[60:63], s22 offen sc1\n"
    "v_accvgpr_write_b32 a184, v148\n"
    "v_accvgpr_write_b32 a185, v149\n"
    "v_mfma_f32_32x32x16_bf16 v[96:111], a[108:111], v[216:219], v[96:111]\n"
    "s_waitcnt lgkmcnt(4)\n"
    "s_barrier\n"
    "v_mfma_f32_32x32x16_bf16 v[48:63], a[112:115], v[220:223], v[48:63]\n"
    "s_lshl_b32 s79, s79, 12\n"
    "s_add_u32 s22, s86, 36864\n"
    "s_add_u32 s20, s74, 17408\n"
    "ds_read_b64_tr_b16 a[136:137], v16 offset:13824\n"
    "ds_read_b64_tr_b16 a[138:139], v17 offset:13824\n"
    "ds_read_b64_tr_b16 a[140:141], v16 offset:14080\n"
    "ds_read_b64_tr_b16 a[142:143], v17 offset:14080\n"
    "v_accvgpr_write_b32 a186, v150\n"
    "v_mfma_f32_32x32x16_bf16 v[64:79], a[116:119], v[220:223], v[64:79]\n"
    "s_add_u32 s79, s79, s29\n"
    "ds_read_b64_tr_b16 a[104:105], v16 offset:14336\n"
    "ds_read_b64_tr_b16 a[106:107], v17 offset:14336\n"
    "buffer_load_dwordx4 a[240:243], v25, s[60:63], s22 offen sc1\n"
    "v_accvgpr_write_b32 a187, v151\n"
    "v_mfma_f32_32x32x16_bf16 v[80:95], a[120:123], v[220:223], v[80:95]\n"
    "s_add_u32 s21, s83, 20480\n"
    "ds_read_b64_tr_b16 a[108:109], v16 offset:14592\n"
    "ds_read_b64_tr_b16 a[110:111], v17 offset:14592\n"
    "buffer_store_dwordx4 a[184:187], v25, s[60:63], s21 offen sc1\n"
    "v_mfma_f32_32x32x16_bf16 v[96:111], a[124:127], v[220:223], v[96:111]\n"
    "s_waitcnt lgkmcnt(4)\n"
    "ds_read_b64_tr_b16 a[112:113], v16 offset:14848\n"
    "ds_read_b64_tr_b16 a[114:115], v17 offset:14848\n"
    "v_exp_f32 v2, v160\n"
    "v_exp_f32 v3, v161\n"
    "v_mfma_f32_32x32x16_bf16 v[48:63], a[128:131], v[224:227], v[48:63]\n"
    "s_add_u32 s79, s79, 1314816\n"
    "ds_read_b64_tr_b16 a[116:117], v16 offset:15104\n"
    "ds_read_b64_tr_b16 a[118:119], v17 offset:15104\n"
    "v_exp_f32 v4, v162\n"
    "v_exp_f32 v5, v163\n"
    "v_add_f32 v2, 1.0, v2\n"
    "v_mfma_f32_32x32x16_bf16 v[64:79], a[132:135], v[224:227], v[64:79]\n"
    "s_lshl2_add_u32 s80, s35, s3\n"
    "ds_read_b64_tr_b16 a[120:121], v16 offset:15360\n"
    "ds_read_b64_tr_b16 a[122:123], v17 offset:15360\n"
    "v_add_f32 v3, 1.0, v3\n"
    "v_add_f32 v4, 1.0, v4\n"
    "v_add_f32 v5, 1.0, v5\n"
    "v_mfma_f32_32x32x16_bf16 v[80:95], a[136:139], v[224:227], v[80:95]\n"
    "s_lshl2_add_u32 s80, s80, s34\n"
    "ds_read_b64_tr_b16 a[124:125], v16 offset:15616\n"
    "ds_read_b64_tr_b16 a[126:127], v17 offset:15616\n"
    "v_rcp_f32 v2, v2\n"
    "v_rcp_f32 v3, v3\n"
    "v_mfma_f32_32x32x16_bf16 v[96:111], a[140:143], v[224:227], v[96:111]\n"
    "s_waitcnt lgkmcnt(4)\n"
    "ds_read_b64_tr_b16 a[128:129], v16 offset:15872\n"
    "ds_read_b64_tr_b16 a[130:131], v17 offset:15872\n"
    "v_rcp_f32 v4, v4\n"
    "v_rcp_f32 v5, v5\n"
    "v_mul_f32 v2, v160, v2\n"
    "v_mfma_f32_32x32x16_bf16 v[48:63], a[104:107], v[228:231], v[48:63]\n"
    "s_lshl_b32 s80, s80, 9\n"
    "ds_read_b64_tr_b16 a[132:133], v16 offset:16128\n"
    "ds_read_b64_tr_b16 a[134:135], v17 offset:16128\n"
    "buffer_load_dwordx4 v21, s[52:55], s20 offen offset:1024 lds\n"
    "v_mul_f32 v3, v161, v3\n"
    "v_mul_f32 v4, v162, v4\n"
    "v_mfma_f32_32x32x16_bf16 v[64:79], a[108:111], v[228:231], v[64:79]\n"
    "s_add_u32 s80, s80, 1576960\n"
    "s_add_u32 s20, s74, 34816\n"
    "ds_read_b64_tr_b16 a[136:137], v16 offset:16384\n"
    "ds_read_b64_tr_b16 a[138:139], v17 offset:16384\n"
    "buffer_load_dwordx4 v21, s[52:55], s20 offen offset:2048 lds\n"
    "v_mul_f32 v5, v163, v5\n"
    "v_cvt_pk_bf16_f32 v160, v2, v3\n"
    "v_mfma_f32_32x32x16_bf16 v[80:95], a[112:115], v[228:231], v[80:95]\n"
    "s_lshl_b32 s78, s39, 7\n"
    "s_add_u32 s20, s73, 33792\n"
    "ds_read_b64_tr_b16 a[140:141], v16 offset:16640\n"
    "ds_read_b64_tr_b16 a[142:143], v17 offset:16640\n"
    "v_cvt_pk_bf16_f32 v161, v4, v5\n"
    "v_exp_f32 v6, v164\n"
    "v_mfma_f32_32x32x16_bf16 v[96:111], a[116:119], v[228:231], v[96:111]\n"
    "s_waitcnt lgkmcnt(4)\n"
    "ds_read_b64_tr_b16 a[104:105], v16 offset:16896\n"
    "ds_read_b64_tr_b16 a[106:107], v17 offset:16896\n"
    "buffer_load_dwordx4 v21, s[52:55], s20 offen offset:3072 lds\n"
    "v_exp_f32 v7, v165\n"
    "v_mfma_f32_32x32x16_bf16 v[48:63], a[120:123], v[232:235], v[48:63]\n"
    "s_add_u32 s78, s78, s80\n"
    "s_add_u32 m0, s68, 7168\n"
    "s_add_u32 s20, s74, 55296\n"
    "ds_read_b64_tr_b16 a[108:109], v16 offset:17152\n"
    "ds_read_b64_tr_b16 a[110:111], v17 offset:17152\n"
    "v_exp_f32 v8, v166\n"
    "v_mfma_f32_32x32x16_bf16 v[64:79], a[124:127], v[232:235], v[64:79]\n"
    "ds_read_b64_tr_b16 a[112:113], v16 offset:17408\n"
    "ds_read_b64_tr_b16 a[114:115], v17 offset:17408\n"
    "buffer_load_dwordx4 v21, s[52:55], s20 offen lds\n"
    "v_exp_f32 v9, v167\n"
    "v_add_f32 v6, 1.0, v6\n"
    "v_mfma_f32_32x32x16_bf16 v[80:95], a[128:131], v[232:235], v[80:95]\n"
    "s_add_u32 m0, s68, 8192\n"
    "ds_read_b64_tr_b16 a[116:117], v16 offset:17664\n"
    "ds_read_b64_tr_b16 a[118:119], v17 offset:17664\n"
    "buffer_load_dwordx4 v21, s[52:55], s75 offen lds\n"
    "v_add_f32 v7, 1.0, v7\n"
    "v_add_f32 v8, 1.0, v8\n"
    "v_mfma_f32_32x32x16_bf16 v[96:111], a[132:135], v[232:235], v[96:111]\n"
    "s_waitcnt lgkmcnt(4)\n"
    "s_add_u32 s20, s75, 17408\n"
    "ds_read_b64_tr_b16 a[120:121], v16 offset:17920\n"
    "ds_read_b64_tr_b16 a[122:123], v17 offset:17920\n"
    "v_add_f32 v9, 1.0, v9\n"
    "v_rcp_f32 v6, v6\n"
    "v_mfma_f32_32x32x16_bf16 v[48:63], a[136:139], v[236:239], v[48:63]\n"
    "ds_read_b64_tr_b16 a[124:125], v16 offset:18176\n"
    "ds_read_b64_tr_b16 a[126:127], v17 offset:18176\n"
    "buffer_load_dwordx4 v21, s[52:55], s20 offen offset:1024 lds\n"
    "v_rcp_f32 v7, v7\n"
    "v_mfma_f32_32x32x16_bf16 v[64:79], a[140:143], v[236:239], v[64:79]\n"
    "s_add_u32 s20, s73, 53248\n"
    "ds_read_b64_tr_b16 a[128:129], v16 offset:18432\n"
    "ds_read_b64_tr_b16 a[130:131], v17 offset:18432\n"
    "v_rcp_f32 v8, v8\n"
    "v_rcp_f32 v9, v9\n"
    "v_mul_f32 v6, v164, v6\n"
    "v_mfma_f32_32x32x16_bf16 v[80:95], a[104:107], v[236:239], v[80:95]\n"
    "ds_read_b64_tr_b16 a[132:133], v16 offset:18688\n"
    "ds_read_b64_tr_b16 a[134:135], v17 offset:18688\n"
    "buffer_load_dwordx4 v21, s[52:55], s20 offen offset:2048 lds\n"
    "v_accvgpr_read_b32 v43, a151\n"
    "v_accvgpr_read_b32 v31, a152\n"
    "v_mul_f32 v7, v165, v7\n"
    "v_mfma_f32_32x32x16_bf16 v[96:111], a[108:111], v[236:239], v[96:111]\n"
    "s_waitcnt lgkmcnt(4)\n"
    "s_barrier\n"
    "v_mfma_f32_32x32x16_bf16 v[48:63], a[112:115], v[240:243], v[48:63]\n"
    "s_add_u32 s20, s75, 33792\n"
    "ds_read_b64_tr_b16 a[136:137], v16 offset:18944\n"
    "ds_read_b64_tr_b16 a[138:139], v17 offset:18944\n"
    "ds_read_b64_tr_b16 a[140:141], v16 offset:19200\n"
    "ds_read_b64_tr_b16 a[142:143], v17 offset:19200\n"
    "buffer_load_dword v42, v28, s[60:63], s17 offen sc1\n"
    "v_mfma_f32_32x32x16_bf16 v[64:79], a[116:119], v[240:243], v[64:79]\n"
    "ds_read_b64_tr_b16 a[104:105], v16 offset:19456\n"
    "ds_read_b64_tr_b16 a[106:107], v17 offset:19456\n"
    "buffer_load_dwordx4 v21, s[52:55], s20 offen offset:3072 lds\n"
    "v_mul_f32 v8, v166, v8\n"
    "v_mul_f32 v9, v167, v9\n"
    "v_cvt_pk_bf16_f32 v162, v6, v7\n"
    "v_mfma_f32_32x32x16_bf16 v[80:95], a[120:123], v[240:243], v[80:95]\n"
    "s_add_u32 m0, s68, 12288\n"
    "s_add_u32 s20, s75, 55296\n"
    "ds_read_b64_tr_b16 a[108:109], v16 offset:19712\n"
    "ds_read_b64_tr_b16 a[110:111], v17 offset:19712\n"
    "buffer_load_dwordx4 v21, s[52:55], s20 offen lds\n"
    "v_cvt_pk_bf16_f32 v163, v8, v9\n"
    "v_mfma_f32_32x32x16_bf16 v[96:111], a[124:127], v[240:243], v[96:111]\n"
    "s_waitcnt lgkmcnt(4)\n"
    "s_add_u32 m0, s68, 13312\n"
    "ds_read_b64_tr_b16 a[112:113], v16 offset:19968\n"
    "ds_read_b64_tr_b16 a[114:115], v17 offset:19968\n"
    "buffer_load_dwordx4 v21, s[52:55], s76 offen lds\n"
    "v_accvgpr_write_b32 a188, v160\n"
    "v_mfma_f32_32x32x16_bf16 v[48:63], a[128:131], v[244:247], v[48:63]\n"
    "s_add_u32 s73, s73, s13\n"
    "s_add_u32 m0, s68, 14336\n"
    "ds_read_b64_tr_b16 a[116:117], v16 offset:20224\n"
    "ds_read_b64_tr_b16 a[118:119], v17 offset:20224\n"
    "v_accvgpr_write_b32 a189, v161\n"
    "v_accvgpr_write_b32 a190, v162\n"
    "v_accvgpr_write_b32 a191, v163\n"
    "v_mfma_f32_32x32x16_bf16 v[64:79], a[132:135], v[244:247], v[64:79]\n"
    "s_add_u32 s21, s83, 24576\n"
    "ds_read_b64_tr_b16 a[120:121], v16 offset:20480\n"
    "ds_read_b64_tr_b16 a[122:123], v17 offset:20480\n"
    "buffer_store_dwordx4 a[188:191], v25, s[60:63], s21 offen sc1\n"
    "v_exp_f32 v2, v168\n"
    "v_mfma_f32_32x32x16_bf16 v[80:95], a[136:139], v[244:247], v[80:95]\n"
    "ds_read_b64_tr_b16 a[124:125], v16 offset:20736\n"
    "ds_read_b64_tr_b16 a[126:127], v17 offset:20736\n"
    "v_exp_f32 v3, v169\n"
    "v_exp_f32 v4, v170\n"
    "v_mfma_f32_32x32x16_bf16 v[96:111], a[140:143], v[244:247], v[96:111]\n"
    "s_waitcnt lgkmcnt(4)\n"
    "ds_read_b64_tr_b16 a[128:129], v16 offset:20992\n"
    "ds_read_b64_tr_b16 a[130:131], v17 offset:20992\n"
    "v_exp_f32 v5, v171\n"
    "v_add_f32 v2, 1.0, v2\n"
    "v_add_f32 v3, 1.0, v3\n"
    "v_mfma_f32_32x32x16_bf16 v[48:63], a[104:107], v[248:251], v[48:63]\n"
    "ds_read_b64_tr_b16 a[132:133], v16 offset:21248\n"
    "ds_read_b64_tr_b16 a[134:135], v17 offset:21248\n"
    "buffer_load_dword v20, v27, s[60:63], s7 offen sc1\n"
    "v_add_f32 v4, 1.0, v4\n"
    "v_add_f32 v5, 1.0, v5\n"
    "v_mfma_f32_32x32x16_bf16 v[64:79], a[108:111], v[248:251], v[64:79]\n"
    "ds_read_b64_tr_b16 a[136:137], v16 offset:22528\n"
    "ds_read_b64_tr_b16 a[138:139], v17 offset:22528\n"
    "v_rcp_f32 v2, v2\n"
    "v_rcp_f32 v3, v3\n"
    "v_mfma_f32_32x32x16_bf16 v[80:95], a[112:115], v[248:251], v[80:95]\n"
    "ds_read_b64_tr_b16 a[140:141], v16 offset:22784\n"
    "ds_read_b64_tr_b16 a[142:143], v17 offset:22784\n"
    "buffer_load_dwordx4 v23, s[52:55], s73 offen lds\n"
    "v_rcp_f32 v4, v4\n"
    "v_mfma_f32_32x32x16_bf16 v[96:111], a[116:119], v[248:251], v[96:111]\n"
    "s_waitcnt lgkmcnt(4)\n"
    "ds_read_b64_tr_b16 a[104:105], v16 offset:23040\n"
    "ds_read_b64_tr_b16 a[106:107], v17 offset:23040\n"
    "v_rcp_f32 v5, v5\n"
    "v_mul_f32 v2, v168, v2\n"
    "v_mul_f32 v3, v169, v3\n"
    "v_mul_f32 v4, v170, v4\n"
    "v_mul_f32 v5, v171, v5\n"
    "v_mfma_f32_32x32x16_bf16 v[48:63], a[120:123], v[252:255], v[48:63]\n"
    "ds_read_b64_tr_b16 a[108:109], v16 offset:23296\n"
    "ds_read_b64_tr_b16 a[110:111], v17 offset:23296\n"
    "v_cvt_pk_bf16_f32 v164, v2, v3\n"
    "v_cvt_pk_bf16_f32 v165, v4, v5\n"
    "v_exp_f32 v6, v172\n"
    "v_mfma_f32_32x32x16_bf16 v[64:79], a[124:127], v[252:255], v[64:79]\n"
    "s_add_u32 m0, s68, 15360\n"
    "ds_read_b64_tr_b16 a[112:113], v16 offset:21504\n"
    "ds_read_b64_tr_b16 a[114:115], v17 offset:21504\n"
    "v_exp_f32 v7, v173\n"
    "v_exp_f32 v8, v174\n"
    "v_mfma_f32_32x32x16_bf16 v[80:95], a[128:131], v[252:255], v[80:95]\n"
    "ds_read_b64_tr_b16 a[116:117], v16 offset:23552\n"
    "ds_read_b64_tr_b16 a[118:119], v17 offset:23552\n"
    "buffer_load_dwordx4 v21, s[52:55], s73 offen lds\n"
    "v_exp_f32 v9, v175\n"
    "v_add_f32 v6, 1.0, v6\n"
    "v_mfma_f32_32x32x16_bf16 v[96:111], a[132:135], v[252:255], v[96:111]\n"
    "s_waitcnt vmcnt(29) lgkmcnt(6)\n"
    "s_add_u32 s20, s73, 17408\n"
    "ds_read_b64_tr_b16 a[120:121], v16 offset:23808\n"
    "ds_read_b64_tr_b16 a[122:123], v17 offset:23808\n"
    "buffer_load_dwordx4 v21, s[52:55], s20 offen offset:1024 lds\n"
    "v_add_f32 v7, 1.0, v7\n"
    "v_mfma_f32_32x32x16_bf16 v[192:207], a[136:139], a[220:223], 0\n"
    "s_waitcnt vmcnt(8)\n"
    "ds_read_b64_tr_b16 a[124:125], v16 offset:24064\n"
    "ds_read_b64_tr_b16 a[126:127], v17 offset:24064\n"
    "v_cmp_ne_u32 vcc, v29, v42\n"
    "s_cmp_lg_u64 vcc, 0\n"
    "s_cbranch_scc1 L_slow1_%=\n"
    "L_ret1_%=:\n"
    "v_add_f32 v8, 1.0, v8\n"
    "v_add_f32 v9, 1.0, v9\n"
    "v_mfma_f32_32x32x16_bf16 v[208:223], a[140:143], a[220:223], 0\n"
    "ds_read_b64_tr_b16 a[128:129], v16 offset:24320\n"
    "ds_read_b64_tr_b16 a[130:131], v17 offset:24320\n"
    "buffer_load_dwordx4 v[32:35], v10, s[60:63], s16 offen sc1\n"
    "buffer_load_dwordx4 v[36:39], v10, s[60:63], s16 offen offset:768 sc1\n"
    "buffer_load_dwordx2 v[40:41], v11, s[60:63], s16 offen offset:1536 sc1\n"
    "v_mfma_f32_32x32x16_bf16 v[224:239], a[104:107], a[220:223], 0\n"
    "s_waitcnt lgkmcnt(8)\n"
    "ds_read_b64_tr_b16 a[132:133], v16 offset:21760\n"
    "ds_read_b64_tr_b16 a[134:135], v17 offset:21760\n"
    "v_rcp_f32 v6, v6\n"
    "v_rcp_f32 v7, v7\n"
    "v_mfma_f32_32x32x16_bf16 v[240:255], a[108:111], a[220:223], 0\n"
    "s_barrier\n"
    "v_rcp_f32 v8, v8\n"
    "v_rcp_f32 v9, v9\n"
    "v_mfma_f32_32x32x16_bf16 v[112:127], a[112:115], a[220:223], 0\n"
    "s_waitcnt lgkmcnt(4)\n"
    "ds_read_b64_tr_b16 a[136:137], v16 offset:24576\n"
    "ds_read_b64_tr_b16 a[138:139], v17 offset:24576\n"
    "ds_read_b64_tr_b16 a[140:141], v16 offset:24832\n"
    "ds_read_b64_tr_b16 a[142:143], v17 offset:24832\n"
    "v_mul_f32 v6, v172, v6\n"
    "v_mul_f32 v7, v173, v7\n"
    "v_mul_f32 v8, v174, v8\n"
    "v_mfma_f32_32x32x16_bf16 v[192:207], a[116:119], a[232:235], v[192:207]\n"
    "s_add_u32 s20, s76, 16384\n"
    "ds_read_b64_tr_b16 a[104:105], v16 offset:25088\n"
    "ds_read_b64_tr_b16 a[106:107], v17 offset:25088\n"
    "buffer_load_dwordx4 v21, s[52:55], s20 offen offset:2048 lds\n"
    "v_mul_f32 v9, v175, v9\n"
    "v_cvt_pk_bf16_f32 v166, v6, v7\n"
    "v_cvt_pk_bf16_f32 v167, v8, v9\n"
    "v_accvgpr_write_b32 a192, v164\n"
    "v_mfma_f32_32x32x16_bf16 v[208:223], a[120:123], a[232:235], v[208:223]\n"
    "s_add_u32 s20, s76, 33792\n"
    "ds_read_b64_tr_b16 a[108:109], v16 offset:25344\n"
    "ds_read_b64_tr_b16 a[110:111], v17 offset:25344\n"
    "v_accvgpr_write_b32 a193, v165\n"
    "v_accvgpr_write_b32 a194, v166\n"
    "v_accvgpr_write_b32 a195, v167\n"
    "v_mfma_f32_32x32x16_bf16 v[224:239], a[124:127], a[232:235], v[224:239]\n"
    "s_waitcnt lgkmcnt(8)\n"
    "s_add_u32 s21, s83, 28672\n"
    "ds_read_b64_tr_b16 a[112:113], v16 offset:25600\n"
    "ds_read_b64_tr_b16 a[114:115], v17 offset:25600\n"
    "buffer_store_dwordx4 a[192:195], v25, s[60:63], s21 offen sc1\n"
    "v_exp_f32 v2, v48\n"
    "v_mfma_f32_32x32x16_bf16 v[240:255], a[128:131], a[232:235], v[240:255]\n"
    "s_waitcnt vmcnt(8)\n"
    "ds_read_b64_tr_b16 a[116:117], v16 offset:25856\n"
    "ds_read_b64_tr_b16 a[118:119], v17 offset:25856\n"
    "v_cmp_ne_u32 vcc, v29, v20\n"
    "s_cmp_lg_u64 vcc, 0\n"
    "s_cbranch_scc1 L_slow3_%=\n"
    "L_ret3_%=:\n"
    "v_exp_f32 v3, v49\n"
    "v_mfma_f32_32x32x16_bf16 v[112:127], a[132:135], a[232:235], v[112:127]\n"
    "s_waitcnt lgkmcnt(4)\n"
    "ds_read_b64_tr_b16 a[120:121], v16 offset:26112\n"
    "ds_read_b64_tr_b16 a[122:123], v17 offset:26112\n"
    "v_exp_f32 v4, v50\n"
    "v_exp_f32 v5, v51\n"
    "v_add_f32 v2, 1.0, v2\n"
    "v_add_f32 v3, 1.0, v3\n"
    "v_mfma_f32_32x32x16_bf16 v[192:207], a[136:139], a[224:227], v[192:207]\n"
    "ds_read_b64_tr_b16 a[124:125], v16 offset:26368\n"
    "ds_read_b64_tr_b16 a[126:127], v17 offset:26368\n"
    "v_add_f32 v4, 1.0, v4\n"
    "v_add_f32 v5, 1.0, v5\n"
    "v_rcp_f32 v2, v2\n"
    "v_rcp_f32 v3, v3\n"
    "v_mfma_f32_32x32x16_bf16 v[208:223], a[140:143], a[224:227], v[208:223]\n"
    "s_add_u32 s22, s84, 0\n"
    "ds_read_b64_tr_b16 a[128:129], v16 offset:26624\n"
    "ds_read_b64_tr_b16 a[130:131], v17 offset:26624\n"
    "buffer_load_dwordx4 a[0:3], v25, s[60:63], s22 offen sc1\n"
    "v_rcp_f32 v4, v4\n"
    "v_mfma_f32_32x32x16_bf16 v[224:239], a[104:107], a[224:227], v[224:239]\n"
    "ds_read_b64_tr_b16 a[132:133], v16 offset:26880\n"
    "ds_read_b64_tr_b16 a[134:135], v17 offset:26880\n"
    "v_rcp_f32 v5, v5\n"
    "v_mul_f32 v2, v48, v2\n"
    "v_mul_f32 v3, v49, v3\n"
    "v_mul_f32 v4, v50, v4\n"
    "v_mul_f32 v5, v51, v5\n"
    "v_mfma_f32_32x32x16_bf16 v[240:255], a[108:111], a[224:227], v[240:255]\n"
    "s_waitcnt lgkmcnt(4)\n"
    "s_add_u32 s22, s84, 4096\n"
    "ds_read_b64_tr_b16 a[136:137], v16 offset:27136\n"
    "ds_read_b64_tr_b16 a[138:139], v17 offset:27136\n"
    "buffer_load_dwordx4 a[4:7], v25, s[60:63], s22 offen sc1\n"
    "v_cvt_pk_bf16_f32 v48, v2, v3\n"
    "v_cvt_pk_bf16_f32 v49, v4, v5\n"
    "v_mfma_f32_32x32x16_bf16 v[192:207], a[112:115], a[236:239], v[192:207]\n"
    "s_add_u32 s22, s84, 8192\n"
    "ds_read_b64_tr_b16 a[140:141], v16 offset:27392\n"
    "ds_read_b64_tr_b16 a[142:143], v17 offset:27392\n"
    "v_exp_f32 v6, v52\n"
    "v_exp_f32 v7, v53\n"
    "v_mfma_f32_32x32x16_bf16 v[208:223], a[116:119], a[236:239], v[208:223]\n"
    "ds_read_b64_tr_b16 a[104:105], v16 offset:27648\n"
    "ds_read_b64_tr_b16 a[106:107], v17 offset:27648\n"
    "buffer_load_dwordx4 a[8:11], v25, s[60:63], s22 offen sc1\n"
    "v_exp_f32 v8, v54\n"
    "v_exp_f32 v9, v55\n"
    "v_mfma_f32_32x32x16_bf16 v[224:239], a[120:123], a[236:239], v[224:239]\n"
    "s_add_u32 s22, s84, 12288\n"
    "ds_read_b64_tr_b16 a[108:109], v16 offset:27904\n"
    "ds_read_b64_tr_b16 a[110:111], v17 offset:27904\n"
    "v_add_f32 v6, 1.0, v6\n"
    "v_add_f32 v7, 1.0, v7\n"
    "v_add_f32 v8, 1.0, v8\n"
    "v_add_f32 v9, 1.0, v9\n"
    "v_rcp_f32 v6, v6\n"
    "v_mfma_f32_32x32x16_bf16 v[240:255], a[124:127], a[236:239], v[240:255]\n"
    "s_waitcnt lgkmcnt(4)\n"
    "ds_read_b64_tr_b16 a[112:113], v16 offset:28160\n"
    "ds_read_b64_tr_b16 a[114:115], v17 offset:28160\n"
    "buffer_load_dwordx4 a[12:15], v25, s[60:63], s22 offen sc1\n"
    "v_rcp_f32 v7, v7\n"
    "v_mfma_f32_32x32x16_bf16 v[192:207], a[128:131], a[228:231], v[192:207]\n"
    "s_add_u32 s22, s85, 0\n"
    "ds_read_b64_tr_b16 a[116:117], v16 offset:28416\n"
    "ds_read_b64_tr_b16 a[118:119], v17 offset:28416\n"
    "v_rcp_f32 v8, v8\n"
    "v_rcp_f32 v9, v9\n"
    "v_mul_f32 v6, v52, v6\n"
    "v_mul_f32 v7, v53, v7\n"
    "v_mfma_f32_32x32x16_bf16 v[208:223], a[132:135], a[228:231], v[208:223]\n"
    "ds_read_b64_tr_b16 a[120:121], v16 offset:29696\n"
    "ds_read_b64_tr_b16 a[122:123], v17 offset:29696\n"
    "buffer_load_dwordx4 a[16:19], v25, s[60:63], s22 offen sc1\n"
    "v_mul_f32 v8, v54, v8\n"
    "v_mul_f32 v9, v55, v9\n"
    "v_cvt_pk_bf16_f32 v50, v6, v7\n"
    "v_mfma_f32_32x32x16_bf16 v[224:239], a[136:139], a[228:231], v[224:239]\n"
    "s_add_u32 s22, s85, 4096\n"
    "ds_read_b64_tr_b16 a[124:125], v16 offset:29952\n"
    "ds_read_b64_tr_b16 a[126:127], v17 offset:29952\n"
    "v_cvt_pk_bf16_f32 v51, v8, v9\n"
    "v_accvgpr_write_b32 a204, v48\n"
    "v_accvgpr_write_b32 a205, v49\n"
    "v_accvgpr_write_b32 a206, v50\n"
    "v_accvgpr_write_b32 a207, v51\n"
    "v_mfma_f32_32x32x16_bf16 v[240:255], a[140:143], a[228:231], v[240:255]\n"
    "s_waitcnt vmcnt(7) lgkmcnt(4)\n"
    "ds_read_b64_tr_b16 a[128:129], v16 offset:30208\n"
    "ds_read_b64_tr_b16 a[130:131], v17 offset:30208\n"
    "v_cvt_pk_bf16_f32 v32, v32, v33\n"
    "v_cvt_pk_bf16_f32 v33, v34, v35\n"
    "v_cvt_pk_bf16_f32 v34, v36, v37\n"
    "v_cvt_pk_bf16_f32 v35, v38, v39\n"
    "v_cvt_pk_bf16_f32 v36, v40, v41\n"
    "v_mfma_f32_32x32x16_bf16 v[192:207], a[104:107], a[240:243], v[192:207]\n"
    "s_add_u32 s21, s83, 40960\n"
    "ds_read_b64_tr_b16 a[132:133], v16 offset:30464\n"
    "ds_read_b64_tr_b16 a[134:135], v17 offset:30464\n"
    "buffer_store_dwordx4 a[204:207], v25, s[60:63], s21 offen sc1\n"
    "v_mov_b32 v37, 0\n"
    "v_mfma_f32_32x32x16_bf16 v[208:223], a[108:111], a[240:243], v[208:223]\n"
    "s_barrier\n"
    "ds_write_b64 v43, v[32:33]\n"
    "v_mfma_f32_32x32x16_bf16 v[224:239], a[112:115], a[240:243], v[224:239]\n"
    "ds_read_b64_tr_b16 a[136:137], v16 offset:28672\n"
    "ds_read_b64_tr_b16 a[138:139], v17 offset:28672\n"
    "ds_read_b64_tr_b16 a[140:141], v16 offset:30720\n"
    "ds_read_b64_tr_b16 a[142:143], v17 offset:30720\n"
    "ds_write_b64 v43, v[34:35] offset:16\n"
    "v_mfma_f32_32x32x16_bf16 v[240:255], a[116:119], a[240:243], v[240:255]\n"
    "s_waitcnt lgkmcnt(8)\n"
    "ds_read_b64_tr_b16 a[104:105], v16 offset:30976\n"
    "ds_read_b64_tr_b16 a[106:107], v17 offset:30976\n"
    "ds_write_b64 v31, v[36:37]\n"
    "v_exp_f32 v2, v56\n"
    "v_mfma_f32_32x32x16_bf16 v[192:207], a[120:123], a[196:199], v[192:207]\n"
    "ds_read_b64_tr_b16 a[108:109], v16 offset:31232\n"
    "ds_read_b64_tr_b16 a[110:111], v17 offset:31232\n"
    "buffer_load_dwordx4 a[20:23], v25, s[60:63], s22 offen sc1\n"
    "v_accvgpr_read_b32 v43, a153\n"
    "v_accvgpr_read_b32 v31, a154\n"
    "v_lshrrev_b32 v15, 2, v25\n"
    "v_mfma_f32_32x32x16_bf16 v[208:223], a[124:127], a[196:199], v[208:223]\n"
    "s_add_u32 s22, s85, 8192\n"
    "ds_read_b64_tr_b16 a[112:113], v16 offset:31488\n"
    "ds_read_b64_tr_b16 a[114:115], v17 offset:31488\n"
    "buffer_load_dwordx4 a[24:27], v25, s[60:63], s22 offen sc1\n"
    "v_exp_f32 v3, v57\n"
    "v_exp_f32 v4, v58\n"
    "v_mfma_f32_32x32x16_bf16 v[224:239], a[128:131], a[196:199], v[224:239]\n"
    "s_waitcnt lgkmcnt(4)\n"
    "s_barrier\n"
    "ds_read_b64_tr_b16 a[116:117], v16 offset:28928\n"
    "ds_read_b64_tr_b16 a[118:119], v17 offset:28928\n"
    "ds_read_b128 a[80:83], v43\n"
    "v_mfma_f32_32x32x16_bf16 v[240:255], a[132:135], a[196:199], v[240:255]\n"
    "s_add_u32 s22, s85, 12288\n"
    "ds_read_b64_tr_b16 a[120:121], v16\n"
    "ds_read_b64_tr_b16 a[122:123], v17\n"
    "ds_read_b128 a[84:87], v43 offset:16\n"
    "ds_read_b128 a[88:91], v43 offset:32\n"
    "ds_read_b128 a[92:95], v43 offset:48\n"
    "ds_read_b128 a[96:99], v43 offset:64\n"
    "v_mfma_f32_32x32x16_bf16 v[112:127], a[136:139], a[196:199], v[112:127]\n"
    "s_waitcnt lgkmcnt(11)\n"
    "ds_read_b64_tr_b16 a[124:125], v16 offset:256\n"
    "ds_read_b64_tr_b16 a[126:127], v17 offset:256\n"
    "buffer_load_dwordx4 v21, s[52:55], s20 offen offset:3072 lds\n"
    "v_exp_f32 v5, v59\n"
    "v_add_f32 v2, 1.0, v2\n"
    "v_add_f32 v3, 1.0, v3\n"
    "v_mfma_f32_32x32x16_bf16 v[192:207], a[140:143], a[200:203], v[192:207]\n"
    "s_add_u32 m0, s68, 19456\n"
    "s_add_u32 s20, s76, 55296\n"
    "ds_read_b64_tr_b16 a[128:129], v16 offset:512\n"
    "ds_read_b64_tr_b16 a[130:131], v17 offset:512\n"
    "buffer_load_dwordx4 v21, s[52:55], s20 offen lds\n"
    "v_add_f32 v4, 1.0, v4\n"
    "v_add_f32 v5, 1.0, v5\n"
    "v_mfma_f32_32x32x16_bf16 v[208:223], a[104:107], a[200:203], v[208:223]\n"
    "s_waitcnt vmcnt(10) lgkmcnt(4)\n"
    "ds_read_b64_tr_b16 a[132:133], v16 offset:768\n"
    "ds_read_b64_tr_b16 a[134:135], v17 offset:768\n"
    "buffer_store_dword v29, v26, s[60:63], s6 offen offset:1024 sc1\n"
    "v_rcp_f32 v2, v2\n"
    "v_mfma_f32_32x32x16_bf16 v[128:143], a[80:83], v[176:179], 0\n"
    "s_add_u32 s20, s73, 35840\n"
    "ds_read_b128 a[100:103], v31\n"
    "v_rcp_f32 v3, v3\n"
    "v_rcp_f32 v4, v4\n"
    "v_mfma_f32_32x32x16_bf16 v[128:143], a[84:87], v[180:183], v[128:143]\n"
    "ds_read_b128 a[80:83], v31 offset:16\n"
    "buffer_load_dwordx4 a[28:31], v25, s[60:63], s22 offen sc1\n"
    "v_rcp_f32 v5, v5\n"
    "v_mul_f32 v2, v56, v2\n"
    "v_mfma_f32_32x32x16_bf16 v[224:239], a[108:111], a[200:203], v[224:239]\n"
    "s_add_u32 s22, s86, 0\n"
    "ds_read_b64_tr_b16 a[136:137], v16 offset:31744\n"
    "ds_read_b64_tr_b16 a[138:139], v17 offset:31744\n"
    "v_mul_f32 v3, v57, v3\n"
    "v_mul_f32 v4, v58, v4\n"
    "v_mul_f32 v5, v59, v5\n"
    "v_cvt_pk_bf16_f32 v52, v2, v3\n"
    "v_cvt_pk_bf16_f32 v53, v4, v5\n"
    "v_mfma_f32_32x32x16_bf16 v[128:143], a[88:91], v[184:187], v[128:143]\n"
    "ds_read_b128 a[84:87], v31 offset:32\n"
    "buffer_load_dwordx4 v21, s[52:55], s20 offen offset:1024 lds\n"
    "v_exp_f32 v6, v60\n"
    "v_mfma_f32_32x32x16_bf16 v[128:143], a[92:95], v[188:191], v[128:143]\n"
    "ds_read_b128 a[88:91], v31 offset:48\n"
    "v_exp_f32 v7, v61\n"
    "v_exp_f32 v8, v62\n"
    "v_mfma_f32_32x32x16_bf16 v[240:255], a[112:115], a[200:203], v[240:255]\n"
    "s_add_u32 s20, s73, 53248\n"
    "ds_read_b64_tr_b16 a[140:141], v16 offset:1024\n"
    "ds_read_b64_tr_b16 a[142:143], v17 offset:1024\n"
    "buffer_load_dwordx4 v21, s[52:55], s20 offen offset:2048 lds\n"
    "v_exp_f32 v9, v63\n"
    "v_add_f32 v6, 1.0, v6\n"
    "v_mfma_f32_32x32x16_bf16 v[128:143], a[96:99], v[44:47], v[128:143]\n"
    "ds_read_b128 a[92:95], v31 offset:64\n"
    "buffer_load_dwordx4 a[32:35], v25, s[60:63], s22 offen sc1\n"
    "v_add_f32 v7, 1.0, v7\n"
    "v_add_f32 v8, 1.0, v8\n"
    "v_add_f32 v9, 1.0, v9\n"
    "v_mfma_f32_32x32x16_bf16 v[112:127], a[116:119], a[200:203], v[112:127]\n"
    "s_waitcnt lgkmcnt(11)\n"
    "s_add_u32 s74, s74, s13\n"
    "s_add_u32 m0, s68, 22528\n"
    "s_add_u32 s22, s86, 4096\n"
    "ds_read_b64_tr_b16 a[104:105], v16 offset:1280\n"
    "ds_read_b64_tr_b16 a[106:107], v17 offset:1280\n"
    "v_rcp_f32 v6, v6\n"
    "v_mfma_f32_32x32x16_bf16 v[192:207], a[120:123], a[164:167], v[192:207]\n"
    "s_waitcnt lgkmcnt(2)\n"
    "ds_read_b64_tr_b16 a[108:109], v16 offset:1536\n"
    "ds_read_b64_tr_b16 a[110:111], v17 offset:1536\n"
    "buffer_load_dwordx4 v23, s[52:55], s74 offen lds\n"
    "v_rcp_f32 v7, v7\n"
    "v_mfma_f32_32x32x16_bf16 v[144:159], a[100:103], v[176:179], 0\n"
    "v_rcp_f32 v8, v8\n"
    "v_rcp_f32 v9, v9\n"
    "v_mul_f32 v6, v60, v6\n"
    "v_mul_f32 v7, v61, v7\n"
    "v_mul_f32 v8, v62, v8\n"
    "v_mfma_f32_32x32x16_bf16 v[144:159], a[80:83], v[180:183], v[144:159]\n"
    "v_mul_f32 v9, v63, v9\n"
    "v_cvt_pk_bf16_f32 v54, v6, v7\n"
    "v_cvt_pk_bf16_f32 v55, v8, v9\n"
    "v_accvgpr_write_b32 a208, v52\n"
    "v_accvgpr_write_b32 a209, v53\n"
    "v_accvgpr_write_b32 a210, v54\n"
    "v_mfma_f32_32x32x16_bf16 v[208:223], a[124:127], a[164:167], v[208:223]\n"
    "s_add_u32 m0, s68, 23552\n"
    "ds_read_b64_tr_b16 a[112:113], v16 offset:1792\n"
    "ds_read_b64_tr_b16 a[114:115], v17 offset:1792\n"
    "v_accvgpr_write_b32 a211, v55\n"
    "v_exp_f32 v2, v64\n"
    "v_exp_f32 v3, v65\n"
    "v_mfma_f32_32x32x16_bf16 v[144:159], a[84:87], v[184:187], v[144:159]\n"
    "buffer_load_dwordx4 v21, s[52:55], s74 offen lds\n"
    "v_exp_f32 v4, v66\n"
    "v_exp_f32 v5, v67\n"
    "v_mfma_f32_32x32x16_bf16 v[144:159], a[88:91], v[188:191], v[144:159]\n"
    "v_add_f32 v2, 1.0, v2\n"
    "v_add_f32 v3, 1.0, v3\n"
    "v_add_f32 v4, 1.0, v4\n"
    "v_add_f32 v5, 1.0, v5\n"
    "v_rcp_f32 v2, v2\n"
    "v_mfma_f32_32x32x16_bf16 v[224:239], a[128:131], a[164:167], v[224:239]\n"
    "s_add_u32 s21, s83, 45056\n"
    "ds_read_b64_tr_b16 a[116:117], v16 offset:32000\n"
    "ds_read_b64_tr_b16 a[118:119], v17 offset:32000\n"
    "buffer_store_dwordx4 a[208:211], v25, s[60:63], s21 offen sc1\n"
    "v_mfma_f32_32x32x16_bf16 v[144:159], a[92:95], v[44:47], v[144:159]\n"
    "v_rcp_f32 v3, v3\n"
    "v_rcp_f32 v4, v4\n"
    "v_rcp_f32 v5, v5\n"
    "v_mul_f32 v2, v64, v2\n"
    "v_mfma_f32_32x32x16_bf16 v[240:255], a[132:135], a[164:167], v[240:255]\n"
    "ds_read_b64_tr_b16 a[120:121], v16 offset:3072\n"
    "ds_read_b64_tr_b16 a[122:123], v17 offset:3072\n"
    "v_mul_f32 v3, v65, v3\n"
    "v_mul_f32 v4, v66, v4\n"
    "v_mul_f32 v5, v67, v5\n"
    "v_cvt_pk_bf16_f32 v64, v2, v3\n"
    "v_cvt_pk_bf16_f32 v65, v4, v5\n"
    "v_mfma_f32_32x32x16_bf16 v[112:127], a[136:139], a[164:167], v[112:127]\n"
    "s_waitcnt lgkmcnt(6)\n"
    "ds_read_b64_tr_b16 a[124:125], v16 offset:3328\n"
    "ds_read_b64_tr_b16 a[126:127], v17 offset:3328\n"
    "v_add_f32 v146, v146, v30\n"
    "v_rcp_f32 v146, v146\n"
    "s_nop 0\n"
    "v_mul_f32 v128, v128, v146\n"
    "v_mul_f32 v129, v129, v146\n"
    "v_mfma_f32_32x32x16_bf16 v[192:207], a[140:143], a[168:171], v[192:207]\n"
    "s_add_u32 s20, s74, 17408\n"
    "ds_read_b64_tr_b16 a[128:129], v16 offset:3584\n"
    "ds_read_b64_tr_b16 a[130:131], v17 offset:3584\n"
    "v_mul_f32 v130, v130, v146\n"
    "v_mul_f32 v131, v131, v146\n"
    "v_mul_f32 v132, v132, v146\n"
    "v_mul_f32 v133, v133, v146\n"
    "v_mul_f32 v134, v134, v146\n"
    "v_mul_f32 v135, v135, v146\n"
    "v_mfma_f32_32x32x16_bf16 v[208:223], a[104:107], a[168:171], v[208:223]\n"
    "ds_read_b64_tr_b16 a[132:133], v16 offset:3840\n"
    "ds_read_b64_tr_b16 a[134:135], v17 offset:3840\n"
    "v_mul_f32 v136, v136, v146\n"
    "v_mul_f32 v137, v137, v146\n"
    "v_mul_f32 v138, v138, v146\n"
    "v_mul_f32 v139, v139, v146\n"
    "v_mul_f32 v140, v140, v146\n"
    "v_mfma_f32_32x32x16_bf16 v[224:239], a[108:111], a[168:171], v[224:239]\n"
    "s_waitcnt lgkmcnt(8)\n"
    "s_barrier\n"
    "v_mul_f32 v141, v141, v146\n"
    "v_mfma_f32_32x32x16_bf16 v[240:255], a[112:115], a[168:171], v[240:255]\n"
    "ds_read_b64_tr_b16 a[136:137], v16 offset:2048\n"
    "ds_read_b64_tr_b16 a[138:139], v17 offset:2048\n"
    "ds_read_b64_tr_b16 a[140:141], v16 offset:4096\n"
    "ds_read_b64_tr_b16 a[142:143], v17 offset:4096\n"
    "v_mul_f32 v142, v142, v146\n"
    "v_mul_f32 v143, v143, v146\n"
    "v_mul_f32 v144, v144, v146\n"
    "v_mfma_f32_32x32x16_bf16 v[112:127], a[116:119], a[168:171], v[112:127]\n"
    "s_waitcnt vmcnt(17) lgkmcnt(6)\n"
    "ds_read_b64_tr_b16 a[104:105], v16 offset:4352\n"
    "ds_read_b64_tr_b16 a[106:107], v17 offset:4352\n"
    "v_mul_f32 v145, v145, v146\n"
    "v_cvt_pk_bf16_f32 v176, v128, v129\n"
    "v_cvt_pk_bf16_f32 v177, v130, v131\n"
    "v_cvt_pk_bf16_f32 v178, v132, v133\n"
    "v_cvt_pk_bf16_f32 v179, v134, v135\n"
    "v_mfma_f32_32x32x16_bf16 v[192:207], a[120:123], a[0:3], v[192:207]\n"
    "ds_read_b64_tr_b16 a[108:109], v16 offset:4608\n"
    "ds_read_b64_tr_b16 a[110:111], v17 offset:4608\n"
    "v_cvt_pk_bf16_f32 v180, v136, v137\n"
    "v_cvt_pk_bf16_f32 v181, v138, v139\n"
    "v_cvt_pk_bf16_f32 v182, v140, v141\n"
    "v_cvt_pk_bf16_f32 v183, v142, v143\n"
    "v_cvt_pk_bf16_f32 v184, v144, v145\n"
    "v_mfma_f32_32x32x16_bf16 v[208:223], a[124:127], a[0:3], v[208:223]\n"
    "ds_read_b64_tr_b16 a[112:113], v16 offset:4864\n"
    "ds_read_b64_tr_b16 a[114:115], v17 offset:4864\n"
    "buffer_store_dwordx4 v[176:179], v25, s[60:63], s96 offen sc1\n"
    "v_exp_f32 v6, v68\n"
    "v_mfma_f32_32x32x16_bf16 v[224:239], a[128:131], a[0:3], v[224:239]\n"
    "s_waitcnt lgkmcnt(8)\n"
    "ds_read_b64_tr_b16 a[116:117], v16 offset:2304\n"
    "ds_read_b64_tr_b16 a[118:119], v17 offset:2304\n"
    "buffer_store_dwordx4 v[180:183], v25, s[60:63], s96 offen offset:1024 sc1\n"
    "v_exp_f32 v7, v69\n"
    "v_mfma_f32_32x32x16_bf16 v[240:255], a[132:135], a[0:3], v[240:255]\n"
    "ds_read_b64_tr_b16 a[120:121], v16 offset:5120\n"
    "ds_read_b64_tr_b16 a[122:123], v17 offset:5120\n"
    "buffer_store_dword v184, v15, s[60:63], s96 offen offset:2048 sc1\n"
    "v_exp_f32 v8, v70\n"
    "v_mfma_f32_32x32x16_bf16 v[112:127], a[136:139], a[0:3], v[112:127]\n"
    "s_waitcnt vmcnt(19) lgkmcnt(6)\n"
    "ds_read_b64_tr_b16 a[124:125], v16 offset:5376\n"
    "ds_read_b64_tr_b16 a[126:127], v17 offset:5376\n"
    "v_exp_f32 v9, v71\n"
    "v_add_f32 v6, 1.0, v6\n"
    "v_add_f32 v7, 1.0, v7\n"
    "v_add_f32 v8, 1.0, v8\n"
    "v_add_f32 v9, 1.0, v9\n"
    "v_mfma_f32_32x32x16_bf16 v[192:207], a[140:143], a[4:7], v[192:207]\n"
    "ds_read_b64_tr_b16 a[128:129], v16 offset:5632\n"
    "ds_read_b64_tr_b16 a[130:131], v17 offset:5632\n"
    "v_rcp_f32 v6, v6\n"
    "v_rcp_f32 v7, v7\n"
    "v_accvgpr_read_b32 v40, a156\n"
    "v_mfma_f32_32x32x16_bf16 v[208:223], a[104:107], a[4:7], v[208:223]\n"
    "ds_read_b64_tr_b16 a[132:133], v16 offset:5888\n"
    "ds_read_b64_tr_b16 a[134:135], v17 offset:5888\n"
    "v_rcp_f32 v8, v8\n"
    "v_rcp_f32 v9, v9\n"
    "v_mul_f32 v6, v68, v6\n"
    "v_mfma_f32_32x32x16_bf16 v[224:239], a[108:111], a[4:7], v[224:239]\n"
    "s_waitcnt lgkmcnt(8)\n"
    "ds_read_b64_tr_b16 a[136:137], v16 offset:2560\n"
    "ds_read_b64_tr_b16 a[138:139], v17 offset:2560\n"
    "v_mul_f32 v7, v69, v7\n"
    "v_mul_f32 v8, v70, v8\n"
    "v_mul_f32 v9, v71, v9\n"
    "v_cvt_pk_bf16_f32 v66, v6, v7\n"
    "v_cvt_pk_bf16_f32 v67, v8, v9\n"
    "v_mfma_f32_32x32x16_bf16 v[240:255], a[112:115], a[4:7], v[240:255]\n"
    "ds_read_b64_tr_b16 a[140:141], v16 offset:6144\n"
    "ds_read_b64_tr_b16 a[142:143], v17 offset:6144\n"
    "v_accvgpr_write_b32 a212, v64\n"
    "v_accvgpr_write_b32 a213, v65\n"
    "v_accvgpr_write_b32 a214, v66\n"
    "v_accvgpr_write_b32 a215, v67\n"
    "v_accvgpr_read_b32 v41, a157\n"
    "v_mfma_f32_32x32x16_bf16 v[112:127], a[116:119], a[4:7], v[112:127]\n"
    "s_waitcnt vmcnt(18) lgkmcnt(6)\n"
    "s_add_u32 s21, s83, 49152\n"
    "ds_read_b64_tr_b16 a[104:105], v16 offset:6400\n"
    "ds_read_b64_tr_b16 a[106:107], v17 offset:6400\n"
    "buffer_store_dwordx4 a[212:215], v25, s[60:63], s21 offen sc1\n"
    "v_mfma_f32_32x32x16_bf16 v[192:207], a[120:123], a[8:11], v[192:207]\n"
    "ds_read_b64_tr_b16 a[108:109], v16 offset:6656\n"
    "ds_read_b64_tr_b16 a[110:111], v17 offset:6656\n"
    "buffer_load_dword a100, v41, s[40:43], s5 offen\n"
    "v_exp_f32 v2, v72\n"
    "v_exp_f32 v3, v73\n"
    "v_mfma_f32_32x32x16_bf16 v[208:223], a[124:127], a[8:11], v[208:223]\n"
    "ds_read_b64_tr_b16 a[112:113], v16 offset:6912\n"
    "ds_read_b64_tr_b16 a[114:115], v17 offset:6912\n"
    "v_exp_f32 v4, v74\n"
    "v_exp_f32 v5, v75\n"
    "v_add_f32 v2, 1.0, v2\n"
    "v_mfma_f32_32x32x16_bf16 v[224:239], a[128:131], a[8:11], v[224:239]\n"
    "s_waitcnt lgkmcnt(8)\n"
    "ds_read_b64_tr_b16 a[116:117], v16 offset:32256\n"
    "ds_read_b64_tr_b16 a[118:119], v17 offset:32256\n"
    "v_add_f32 v3, 1.0, v3\n"
    "v_add_f32 v4, 1.0, v4\n"
    "v_add_f32 v5, 1.0, v5\n"
    "v_rcp_f32 v2, v2\n"
    "v_mfma_f32_32x32x16_bf16 v[240:255], a[132:135], a[8:11], v[240:255]\n"
    "ds_read_b64_tr_b16 a[120:121], v16 offset:7168\n"
    "ds_read_b64_tr_b16 a[122:123], v17 offset:7168\n"
    "v_rcp_f32 v3, v3\n"
    "v_rcp_f32 v4, v4\n"
    "v_rcp_f32 v5, v5\n"
    "v_mfma_f32_32x32x16_bf16 v[112:127], a[136:139], a[8:11], v[112:127]\n"
    "s_waitcnt lgkmcnt(6)\n"
    "ds_read_b64_tr_b16 a[124:125], v16 offset:7424\n"
    "ds_read_b64_tr_b16 a[126:127], v17 offset:7424\n"
    "buffer_load_dwordx4 a[36:39], v25, s[60:63], s22 offen sc1\n"
    "v_mul_f32 v2, v72, v2\n"
    "v_mul_f32 v3, v73, v3\n"
    "v_mul_f32 v4, v74, v4\n"
    "v_mfma_f32_32x32x16_bf16 v[192:207], a[140:143], a[172:175], v[192:207]\n"
    "s_add_u32 s22, s86, 8192\n"
    "ds_read_b64_tr_b16 a[128:129], v16 offset:7680\n"
    "ds_read_b64_tr_b16 a[130:131], v17 offset:7680\n"
    "v_mul_f32 v5, v75, v5\n"
    "v_cvt_pk_bf16_f32 v68, v2, v3\n"
    "v_cvt_pk_bf16_f32 v69, v4, v5\n"
    "v_exp_f32 v6, v76\n"
    "v_mfma_f32_32x32x16_bf16 v[208:223], a[104:107], a[172:175], v[208:223]\n"
    "ds_read_b64_tr_b16 a[132:133], v16 offset:7936\n"
    "ds_read_b64_tr_b16 a[134:135], v17 offset:7936\n"
    "buffer_load_dwordx4 v21, s[52:55], s20 offen offset:1024 lds\n"
    "v_exp_f32 v7, v77\n"
    "v_mfma_f32_32x32x16_bf16 v[224:239], a[108:111], a[172:175], v[224:239]\n"
    "s_waitcnt lgkmcnt(8)\n"
    "s_barrier\n"
    "buffer_load_dwordx4 a[40:43], v25, s[60:63], s22 offen sc1\n"
    "v_mfma_f32_32x32x16_bf16 v[240:255], a[112:115], a[172:175], v[240:255]\n"
    "s_add_u32 s20, s74, 34816\n"
    "s_add_u32 s22, s86, 12288\n"
    "ds_read_b64_tr_b16 a[136:137], v16 offset:2816\n"
    "ds_read_b64_tr_b16 a[138:139], v17 offset:2816\n"
    "ds_read_b64_tr_b16 a[140:141], v16 offset:8192\n"
    "ds_read_b64_tr_b16 a[142:143], v17 offset:8192\n"
    "v_exp_f32 v8, v78\n"
    "v_mfma_f32_32x32x16_bf16 v[112:127], a[116:119], a[172:175], v[112:127]\n"
    "s_waitcnt vmcnt(22) lgkmcnt(6)\n"
    "ds_read_b64_tr_b16 a[104:105], v16 offset:8448\n"
    "ds_read_b64_tr_b16 a[106:107], v17 offset:8448\n"
    "buffer_load_dwordx4 v21, s[52:55], s20 offen offset:2048 lds\n"
    "v_exp_f32 v9, v79\n"
    "v_add_f32 v6, 1.0, v6\n"
    "v_mfma_f32_32x32x16_bf16 v[192:207], a[120:123], a[12:15], v[192:207]\n"
    "s_add_u32 s20, s74, 52224\n"
    "ds_read_b64_tr_b16 a[108:109], v16 offset:8704\n"
    "ds_read_b64_tr_b16 a[110:111], v17 offset:8704\n"
    "v_add_f32 v7, 1.0, v7\n"
    "v_add_f32 v8, 1.0, v8\n"
    "v_add_f32 v9, 1.0, v9\n"
    "v_rcp_f32 v6, v6\n"
    "v_mfma_f32_32x32x16_bf16 v[208:223], a[124:127], a[12:15], v[208:223]\n"
    "ds_read_b64_tr_b16 a[112:113], v16 offset:8960\n"
    "ds_read_b64_tr_b16 a[114:115], v17 offset:8960\n"
    "buffer_load_dwordx4 v21, s[52:55], s20 offen offset:3072 lds\n"
    "v_rcp_f32 v7, v7\n"
    "v_rcp_f32 v8, v8\n"
    "v_mfma_f32_32x32x16_bf16 v[224:239], a[128:131], a[12:15], v[224:239]\n"
    "s_waitcnt vmcnt(7) lgkmcnt(8)\n"
    "ds_read_b64_tr_b16 a[116:117], v16 offset:9216\n"
    "ds_read_b64_tr_b16 a[118:119], v17 offset:9216\n"
    "buffer_store_dword v29, v26, s[60:63], s18 offen sc1\n"
    "v_rcp_f32 v9, v9\n"
    "v_mfma_f32_32x32x16_bf16 v[240:255], a[132:135], a[12:15], v[240:255]\n"
    "ds_read_b64_tr_b16 a[120:121], v16 offset:9472\n"
    "ds_read_b64_tr_b16 a[122:123], v17 offset:9472\n"
    "buffer_load_dwordx4 a[44:47], v25, s[60:63], s22 offen sc1\n"
    "v_mul_f32 v6, v76, v6\n"
    "v_mul_f32 v7, v77, v7\n"
    "v_mul_f32 v8, v78, v8\n"
    "v_mfma_f32_32x32x16_bf16 v[112:127], a[136:139], a[12:15], v[112:127]\n"
    "s_waitcnt lgkmcnt(4)\n"
    "s_add_u32 s75, s75, s13\n"
    "s_add_u32 m0, s68, 27648\n"
    "ds_read_b64_tr_b16 a[124:125], v16 offset:9728\n"
    "ds_read_b64_tr_b16 a[126:127], v17 offset:9728\n"
    "v_mul_f32 v9, v79, v9\n"
    "v_cvt_pk_bf16_f32 v70, v6, v7\n"
    "v_cvt_pk_bf16_f32 v71, v8, v9\n"
    "v_mfma_f32_32x32x16_bf16 v[192:207], a[140:143], a[16:19], v[192:207]\n"
    "ds_read_b64_tr_b16 a[128:129], v16 offset:9984\n"
    "ds_read_b64_tr_b16 a[130:131], v17 offset:9984\n"
    "buffer_load_dwordx4 v21, s[52:55], s75 offen lds\n"
    "v_accvgpr_write_b32 a216, v68\n"
    "v_accvgpr_write_b32 a217, v69\n"
    "v_accvgpr_write_b32 a218, v70\n"
    "v_mfma_f32_32x32x16_bf16 v[208:223], a[104:107], a[16:19], v[208:223]\n"
    "s_add_u32 s20, s75, 17408\n"
    "ds_read_b64_tr_b16 a[132:133], v16 offset:10240\n"
    "ds_read_b64_tr_b16 a[134:135], v17 offset:10240\n"
    "buffer_load_dwordx4 v21, s[52:55], s20 offen offset:1024 lds\n"
    "v_accvgpr_write_b32 a219, v71\n"
    "v_exp_f32 v2, v80\n"
    "v_mfma_f32_32x32x16_bf16 v[224:239], a[108:111], a[16:19], v[224:239]\n"
    "s_add_u32 s21, s83, 53248\n"
    "ds_read_b64_tr_b16 a[136:137], v16 offset:10496\n"
    "ds_read_b64_tr_b16 a[138:139], v17 offset:10496\n"
    "buffer_load_dword v13, v27, s[60:63], s7 offen offset:1024 sc1\n"
    "buffer_store_dwordx4 a[216:219], v25, s[60:63], s21 offen sc1\n"
    "v_mfma_f32_32x32x16_bf16 v[240:255], a[112:115], a[16:19], v[240:255]\n"
    "s_waitcnt lgkmcnt(4)\n"
    "ds_read_b64_tr_b16 a[140:141], v16 offset:10752\n"
    "ds_read_b64_tr_b16 a[142:143], v17 offset:10752\n"
    "v_exp_f32 v3, v81\n"
    "v_exp_f32 v4, v82\n"
    "v_mfma_f32_32x32x16_bf16 v[192:207], a[116:119], a[20:23], v[192:207]\n"
    "ds_read_b64_tr_b16 a[104:105], v16 offset:11008\n"
    "ds_read_b64_tr_b16 a[106:107], v17 offset:11008\n"
    "v_exp_f32 v5, v83\n"
    "v_add_f32 v2, 1.0, v2\n"
    "v_add_f32 v3, 1.0, v3\n"
    "v_add_f32 v4, 1.0, v4\n"
    "v_add_f32 v5, 1.0, v5\n"
    "v_mfma_f32_32x32x16_bf16 v[208:223], a[120:123], a[20:23], v[208:223]\n"
    "ds_read_b64_tr_b16 a[108:109], v16 offset:32512\n"
    "ds_read_b64_tr_b16 a[110:111], v17 offset:32512\n"
    "v_rcp_f32 v2, v2\n"
    "v_rcp_f32 v3, v3\n"
    "v_rcp_f32 v4, v4\n"
    "v_mfma_f32_32x32x16_bf16 v[224:239], a[124:127], a[20:23], v[224:239]\n"
    "ds_read_b64_tr_b16 a[112:113], v16 offset:11264\n"
    "ds_read_b64_tr_b16 a[114:115], v17 offset:11264\n"
    "v_rcp_f32 v5, v5\n"
    "v_mul_f32 v2, v80, v2\n"
    "v_mul_f32 v3, v81, v3\n"
    "v_mul_f32 v4, v82, v4\n"
    "v_mfma_f32_32x32x16_bf16 v[240:255], a[128:131], a[20:23], v[240:255]\n"
    "s_waitcnt lgkmcnt(6)\n"
    "s_add_u32 s20, s75, 34816\n"
    "ds_read_b64_tr_b16 a[116:117], v16 offset:11520\n"
    "ds_read_b64_tr_b16 a[118:119], v17 offset:11520\n"
    "v_mul_f32 v5, v83, v5\n"
    "v_cvt_pk_bf16_f32 v80, v2, v3\n"
    "v_cvt_pk_bf16_f32 v81, v4, v5\n"
    "v_mfma_f32_32x32x16_bf16 v[192:207], a[132:135], a[176:179], v[192:207]\n"
    "ds_read_b64_tr_b16 a[120:121], v16 offset:11776\n"
    "ds_read_b64_tr_b16 a[122:123], v17 offset:11776\n"
    "buffer_load_dwordx4 v21, s[52:55], s20 offen offset:2048 lds\n"
    "v_exp_f32 v6, v84\n"
    "v_exp_f32 v7, v85\n"
    "v_mfma_f32_32x32x16_bf16 v[208:223], a[136:139], a[176:179], v[208:223]\n"
    "s_add_u32 s20, s75, 52224\n"
    "ds_read_b64_tr_b16 a[124:125], v16 offset:12032\n"
    "ds_read_b64_tr_b16 a[126:127], v17 offset:12032\n"
    "buffer_load_dwordx4 v21, s[52:55], s20 offen offset:3072 lds\n"
    "v_exp_f32 v8, v86\n"
    "v_mfma_f32_32x32x16_bf16 v[224:239], a[140:143], a[176:179], v[224:239]\n"
    "s_waitcnt lgkmcnt(8)\n"
    "ds_read_b64_tr_b16 a[128:129], v16 offset:12288\n"
    "ds_read_b64_tr_b16 a[130:131], v17 offset:12288\n"
    "v_exp_f32 v9, v87\n"
    "v_add_f32 v6, 1.0, v6\n"
    "v_add_f32 v7, 1.0, v7\n"
    "v_add_f32 v8, 1.0, v8\n"
    "v_add_f32 v9, 1.0, v9\n"
    "v_mfma_f32_32x32x16_bf16 v[240:255], a[104:107], a[176:179], v[240:255]\n"
    "ds_read_b64_tr_b16 a[132:133], v16 offset:12544\n"
    "ds_read_b64_tr_b16 a[134:135], v17 offset:12544\n"
    "v_rcp_f32 v6, v6\n"
    "v_rcp_f32 v7, v7\n"
    "v_mfma_f32_32x32x16_bf16 v[112:127], a[108:111], a[176:179], v[112:127]\n"
    "s_waitcnt lgkmcnt(4)\n"
    "s_barrier\n"
    "s_add_u32 s76, s76, s13\n"
    "s_add_u32 m0, s68, 31744\n"
    "v_mfma_f32_32x32x16_bf16 v[192:207], a[112:115], a[24:27], v[192:207]\n"
    "ds_read_b64_tr_b16 a[136:137], v16 offset:12800\n"
    "ds_read_b64_tr_b16 a[138:139], v17 offset:12800\n"
    "ds_read_b64_tr_b16 a[140:141], v16 offset:13056\n"
    "ds_read_b64_tr_b16 a[142:143], v17 offset:13056\n"
    "buffer_load_dwordx4 v21, s[52:55], s76 offen lds\n"
    "v_mfma_f32_32x32x16_bf16 v[208:223], a[116:119], a[24:27], v[208:223]\n"
    "s_add_u32 m0, s68, 0\n"
    "s_add_u32 s20, s76, 18432\n"
    "ds_read_b64_tr_b16 a[104:105], v16 offset:13312\n"
    "ds_read_b64_tr_b16 a[106:107], v17 offset:13312\n"
    "buffer_load_dwordx4 v21, s[52:55], s20 offen lds\n"
    "v_rcp_f32 v8, v8\n"
    "v_mfma_f32_32x32x16_bf16 v[224:239], a[120:123], a[24:27], v[224:239]\n"
    "s_add_u32 s20, s76, 35840\n"
    "ds_read_b64_tr_b16 a[108:109], v16 offset:13568\n"
    "ds_read_b64_tr_b16 a[110:111], v17 offset:13568\n"
    "buffer_load_dwordx4 v21, s[52:55], s20 offen offset:1024 lds\n"
    "v_rcp_f32 v9, v9\n"
    "v_mfma_f32_32x32x16_bf16 v[240:255], a[124:127], a[24:27], v[240:255]\n"
    "s_waitcnt lgkmcnt(4)\n"
    "s_add_u32 s20, s76, 53248\n"
    "ds_read_b64_tr_b16 a[112:113], v16 offset:13824\n"
    "ds_read_b64_tr_b16 a[114:115], v17 offset:13824\n"
    "buffer_load_dwordx4 v21, s[52:55], s20 offen offset:2048 lds\n"
    "v_mul_f32 v6, v84, v6\n"
    "v_mfma_f32_32x32x16_bf16 v[192:207], a[128:131], a[28:31], v[192:207]\n"
    "s_add_u32 s73, s73, s69\n"
    "s_add_u32 m0, s68, 3072\n"
    "ds_read_b64_tr_b16 a[116:117], v16 offset:14080\n"
    "ds_read_b64_tr_b16 a[118:119], v17 offset:14080\n"
    "buffer_load_dwordx4 v23, s[52:55], s73 offen lds\n"
    "v_mul_f32 v7, v85, v7\n"
    "v_mul_f32 v8, v86, v8\n"
    "v_mfma_f32_32x32x16_bf16 v[208:223], a[132:135], a[28:31], v[208:223]\n"
    "s_waitcnt vmcnt(8)\n"
    "ds_read_b64_tr_b16 a[120:121], v16 offset:15360\n"
    "ds_read_b64_tr_b16 a[122:123], v17 offset:15360\n"
    "v_cmp_ne_u32 vcc, v29, v13\n"
    "s_cmp_lg_u64 vcc, 0\n"
    "s_cbranch_scc1 L_slow4_%=\n"
    "L_ret4_%=:\n"
    "v_mul_f32 v9, v87, v9\n"
    "v_mfma_f32_32x32x16_bf16 v[224:239], a[136:139], a[28:31], v[224:239]\n"
    "s_add_u32 s22, s84, 16384\n"
    "s_add_u32 m0, s68, 4096\n"
    "ds_read_b64_tr_b16 a[124:125], v16 offset:15616\n"
    "ds_read_b64_tr_b16 a[126:127], v17 offset:15616\n"
    "v_cvt_pk_bf16_f32 v82, v6, v7\n"
    "v_cvt_pk_bf16_f32 v83, v8, v9\n"
    "v_exp_f32 v2, v88\n"
    "v_mfma_f32_32x32x16_bf16 v[240:255], a[140:143], a[28:31], v[240:255]\n"
    "s_waitcnt lgkmcnt(4)\n"
    "s_add_u32 s21, s83, 57344\n"
    "ds_read_b64_tr_b16 a[128:129], v16 offset:15872\n"
    "ds_read_b64_tr_b16 a[130:131], v17 offset:15872\n"
    "buffer_store_dwordx4 v[80:83], v25, s[60:63], s21 offen sc1\n"
    "v_mfma_f32_32x32x16_bf16 v[192:207], a[104:107], a[32:35], v[192:207]\n"
    "ds_read_b64_tr_b16 a[132:133], v16 offset:16128\n"
    "ds_read_b64_tr_b16 a[134:135], v17 offset:16128\n"
    "v_exp_f32 v3, v89\n"
    "v_exp_f32 v4, v90\n"
    "v_mfma_f32_32x32x16_bf16 v[208:223], a[108:111], a[32:35], v[208:223]\n"
    "ds_read_b64_tr_b16 a[136:137], v16 offset:14336\n"
    "ds_read_b64_tr_b16 a[138:139], v17 offset:14336\n"
    "v_exp_f32 v5, v91\n"
    "v_add_f32 v2, 1.0, v2\n"
    "v_add_f32 v3, 1.0, v3\n"
    "v_add_f32 v4, 1.0, v4\n"
    "v_mfma_f32_32x32x16_bf16 v[224:239], a[112:115], a[32:35], v[224:239]\n"
    "ds_read_b64_tr_b16 a[140:141], v16 offset:16384\n"
    "ds_read_b64_tr_b16 a[142:143], v17 offset:16384\n"
    "v_add_f32 v5, 1.0, v5\n"
    "v_rcp_f32 v2, v2\n"
    "v_mfma_f32_32x32x16_bf16 v[240:255], a[116:119], a[32:35], v[240:255]\n"
    "s_waitcnt lgkmcnt(6)\n"
    "ds_read_b64_tr_b16 a[104:105], v16 offset:16640\n"
    "ds_read_b64_tr_b16 a[106:107], v17 offset:16640\n"
    "v_rcp_f32 v3, v3\n"
    "v_rcp_f32 v4, v4\n"
    "v_mfma_f32_32x32x16_bf16 v[192:207], a[120:123], a[180:183], v[192:207]\n"
    "ds_read_b64_tr_b16 a[108:109], v16 offset:16896\n"
    "ds_read_b64_tr_b16 a[110:111], v17 offset:16896\n"
    "buffer_load_dwordx4 a[48:51], v25, s[60:63], s22 offen sc1\n"
    "v_rcp_f32 v5, v5\n"
    "v_mul_f32 v2, v88, v2\n"
    "v_mfma_f32_32x32x16_bf16 v[208:223], a[124:127], a[180:183], v[208:223]\n"
    "s_add_u32 s22, s84, 20480\n"
    "ds_read_b64_tr_b16 a[112:113], v16 offset:17152\n"
    "ds_read_b64_tr_b16 a[114:115], v17 offset:17152\n"
    "buffer_load_dwordx4 a[52:55], v25, s[60:63], s22 offen sc1\n"
    "v_mul_f32 v3, v89, v3\n"
    "v_mul_f32 v4, v90, v4\n"
    "v_mfma_f32_32x32x16_bf16 v[224:239], a[128:131], a[180:183], v[224:239]\n"
    "s_waitcnt vmcnt(10) lgkmcnt(8)\n"
    "ds_read_b64_tr_b16 a[116:117], v16 offset:14592\n"
    "ds_read_b64_tr_b16 a[118:119], v17 offset:14592\n"
    "buffer_store_dword v29, v26, s[60:63], s6 offen offset:512 sc1\n"
    "v_mul_f32 v5, v91, v5\n"
    "v_cvt_pk_bf16_f32 v84, v2, v3\n"
    "v_mfma_f32_32x32x16_bf16 v[240:255], a[132:135], a[180:183], v[240:255]\n"
    "ds_read_b64_tr_b16 a[120:121], v16 offset:17408\n"
    "ds_read_b64_tr_b16 a[122:123], v17 offset:17408\n"
    "v_cvt_pk_bf16_f32 v85, v4, v5\n"
    "v_exp_f32 v6, v92\n"
    "v_mfma_f32_32x32x16_bf16 v[112:127], a[136:139], a[180:183], v[112:127]\n"
    "s_waitcnt lgkmcnt(6)\n"
    "s_add_u32 s22, s84, 24576\n"
    "ds_read_b64_tr_b16 a[124:125], v16 offset:17664\n"
    "ds_read_b64_tr_b16 a[126:127], v17 offset:17664\n"
    "v_exp_f32 v7, v93\n"
    "v_exp_f32 v8, v94\n"
    "v_mfma_f32_32x32x16_bf16 v[192:207], a[140:143], a[184:187], v[192:207]\n"
    "ds_read_b64_tr_b16 a[128:129], v16 offset:17920\n"
    "ds_read_b64_tr_b16 a[130:131], v17 offset:17920\n"
    "buffer_load_dwordx4 a[56:59], v25, s[60:63], s22 offen sc1\n"
    "v_exp_f32 v9, v95\n"
    "v_mfma_f32_32x32x16_bf16 v[208:223], a[104:107], a[184:187], v[208:223]\n"
    "s_add_u32 s22, s84, 28672\n"
    "ds_read_b64_tr_b16 a[132:133], v16 offset:18176\n"
    "ds_read_b64_tr_b16 a[134:135], v17 offset:18176\n"
    "v_add_f32 v6, 1.0, v6\n"
    "v_add_f32 v7, 1.0, v7\n"
    "v_add_f32 v8, 1.0, v8\n"
    "v_add_f32 v9, 1.0, v9\n"
    "v_mfma_f32_32x32x16_bf16 v[224:239], a[108:111], a[184:187], v[224:239]\n"
    "s_waitcnt lgkmcnt(8)\n"
    "s_barrier\n"
    "v_mfma_f32_32x32x16_bf16 v[240:255], a[112:115], a[184:187], v[240:255]\n"
    "ds_read_b64_tr_b16 a[136:137], v16 offset:18432\n"
    "ds_read_b64_tr_b16 a[138:139], v17 offset:18432\n"
    "ds_read_b64_tr_b16 a[140:141], v16 offset:18688\n"
    "ds_read_b64_tr_b16 a[142:143], v17 offset:18688\n"
    "buffer_load_dwordx4 a[60:63], v25, s[60:63], s22 offen sc1\n"
    "v_mfma_f32_32x32x16_bf16 v[112:127], a[116:119], a[184:187], v[112:127]\n"
    "s_waitcnt lgkmcnt(4)\n"
    "s_add_u32 s22, s85, 16384\n"
    "ds_read_b64_tr_b16 a[104:105], v16 offset:18944\n"
    "ds_read_b64_tr_b16 a[106:107], v17 offset:18944\n"
    "buffer_load_dwordx4 a[64:67], v25, s[60:63], s22 offen sc1\n"
    "v_rcp_f32 v6, v6\n"
    "v_mfma_f32_32x32x16_bf16 v[192:207], a[120:123], a[36:39], v[192:207]\n"
    "s_add_u32 s22, s85, 20480\n"
    "ds_read_b64_tr_b16 a[108:109], v16 offset:19200\n"
    "ds_read_b64_tr_b16 a[110:111], v17 offset:19200\n"
    "buffer_load_dwordx4 a[68:71], v25, s[60:63], s22 offen sc1\n"
    "v_rcp_f32 v7, v7\n"
    "v_mfma_f32_32x32x16_bf16 v[208:223], a[124:127], a[36:39], v[208:223]\n"
    "ds_read_b64_tr_b16 a[112:113], v16 offset:19456\n"
    "ds_read_b64_tr_b16 a[114:115], v17 offset:19456\n"
    "v_rcp_f32 v8, v8\n"
    "v_rcp_f32 v9, v9\n"
    "v_mul_f32 v6, v92, v6\n"
    "v_mfma_f32_32x32x16_bf16 v[224:239], a[128:131], a[36:39], v[224:239]\n"
    "ds_read_b64_tr_b16 a[116:117], v16 offset:19712\n"
    "ds_read_b64_tr_b16 a[118:119], v17 offset:19712\n"
    "buffer_load_dwordx4 v21, s[52:55], s73 offen lds\n"
    "v_mul_f32 v7, v93, v7\n"
    "v_mul_f32 v8, v94, v8\n"
    "v_mul_f32 v9, v95, v9\n"
    "v_mfma_f32_32x32x16_bf16 v[240:255], a[132:135], a[36:39], v[240:255]\n"
    "s_waitcnt lgkmcnt(4)\n"
    "s_add_u32 s20, s73, 17408\n"
    "s_add_u32 s22, s85, 24576\n"
    "ds_read_b64_tr_b16 a[120:121], v16 offset:19968\n"
    "ds_read_b64_tr_b16 a[122:123], v17 offset:19968\n"
    "v_cvt_pk_bf16_f32 v86, v6, v7\n"
    "v_cvt_pk_bf16_f32 v87, v8, v9\n"
    "v_mfma_f32_32x32x16_bf16 v[192:207], a[136:139], a[40:43], v[192:207]\n"
    "s_add_u32 s21, s83, 61440\n"
    "ds_read_b64_tr_b16 a[124:125], v16 offset:20224\n"
    "ds_read_b64_tr_b16 a[126:127], v17 offset:20224\n"
    "buffer_store_dwordx4 v[84:87], v25, s[60:63], s21 offen sc1\n"
    "v_exp_f32 v2, v96\n"
    "v_mfma_f32_32x32x16_bf16 v[208:223], a[140:143], a[40:43], v[208:223]\n"
    "ds_read_b64_tr_b16 a[128:129], v16 offset:20480\n"
    "ds_read_b64_tr_b16 a[130:131], v17 offset:20480\n"
    "v_exp_f32 v3, v97\n"
    "v_exp_f32 v4, v98\n"
    "v_mfma_f32_32x32x16_bf16 v[224:239], a[104:107], a[40:43], v[224:239]\n"
    "ds_read_b64_tr_b16 a[132:133], v16 offset:20736\n"
    "ds_read_b64_tr_b16 a[134:135], v17 offset:20736\n"
    "v_exp_f32 v5, v99\n"
    "v_add_f32 v2, 1.0, v2\n"
    "v_add_f32 v3, 1.0, v3\n"
    "v_mfma_f32_32x32x16_bf16 v[240:255], a[108:111], a[40:43], v[240:255]\n"
    "s_waitcnt lgkmcnt(4)\n"
    "ds_read_b64_tr_b16 a[136:137], v16 offset:20992\n"
    "ds_read_b64_tr_b16 a[138:139], v17 offset:20992\n"
    "v_add_f32 v4, 1.0, v4\n"
    "v_add_f32 v5, 1.0, v5\n"
    "v_rcp_f32 v2, v2\n"
    "v_mfma_f32_32x32x16_bf16 v[192:207], a[112:115], a[44:47], v[192:207]\n"
    "ds_read_b64_tr_b16 a[140:141], v16 offset:21248\n"
    "ds_read_b64_tr_b16 a[142:143], v17 offset:21248\n"
    "v_rcp_f32 v3, v3\n"
    "v_rcp_f32 v4, v4\n"
    "v_mfma_f32_32x32x16_bf16 v[208:223], a[116:119], a[44:47], v[208:223]\n"
    "ds_read_b64_tr_b16 a[104:105], v16 offset:14848\n"
    "ds_read_b64_tr_b16 a[106:107], v17 offset:14848\n"
    "buffer_load_dwordx4 v21, s[52:55], s20 offen offset:1024 lds\n"
    "v_rcp_f32 v5, v5\n"
    "v_mul_f32 v2, v96, v2\n"
    "v_mul_f32 v3, v97, v3\n"
    "v_mfma_f32_32x32x16_bf16 v[224:239], a[120:123], a[44:47], v[224:239]\n"
    "ds_read_b64_tr_b16 a[108:109], v16 offset:21504\n"
    "ds_read_b64_tr_b16 a[110:111], v17 offset:21504\n"
    "buffer_load_dwordx4 a[72:75], v25, s[60:63], s22 offen sc1\n"
    "v_mul_f32 v4, v98, v4\n"
    "v_mul_f32 v5, v99, v5\n"
    "v_mfma_f32_32x32x16_bf16 v[240:255], a[124:127], a[44:47], v[240:255]\n"
    "s_waitcnt lgkmcnt(6)\n"
    "s_add_u32 s22, s85, 28672\n"
    "s_add_u32 s20, s73, 34816\n"
    "ds_read_b64_tr_b16 a[112:113], v16 offset:21760\n"
    "ds_read_b64_tr_b16 a[114:115], v17 offset:21760\n"
    "v_cvt_pk_bf16_f32 v88, v2, v3\n"
    "v_cvt_pk_bf16_f32 v89, v4, v5\n"
    "v_mfma_f32_32x32x16_bf16 v[192:207], a[128:131], a[188:191], v[192:207]\n"
    "ds_read_b64_tr_b16 a[116:117], v16 offset:22016\n"
    "ds_read_b64_tr_b16 a[118:119], v17 offset:22016\n"
    "buffer_load_dwordx4 a[76:79], v25, s[60:63], s22 offen sc1\n"
    "v_exp_f32 v6, v100\n"
    "v_mfma_f32_32x32x16_bf16 v[208:223], a[132:135], a[188:191], v[208:223]\n"
    "s_add_u32 s22, s86, 16384\n"
    "ds_read_b64_tr_b16 a[120:121], v16 offset:22272\n"
    "ds_read_b64_tr_b16 a[122:123], v17 offset:22272\n"
    "v_exp_f32 v7, v101\n"
    "v_exp_f32 v8, v102\n"
    "v_mfma_f32_32x32x16_bf16 v[224:239], a[136:139], a[188:191], v[224:239]\n"
    "s_waitcnt lgkmcnt(8)\n"
    "ds_read_b64_tr_b16 a[124:125], v16 offset:15104\n"
    "ds_read_b64_tr_b16 a[126:127], v17 offset:15104\n"
    "buffer_load_dwordx4 a[0:3], v25, s[60:63], s22 offen sc1\n"
    "v_exp_f32 v9, v103\n"
    "v_add_f32 v6, 1.0, v6\n"
    "v_add_f32 v7, 1.0, v7\n"
    "v_mfma_f32_32x32x16_bf16 v[240:255], a[140:143], a[188:191], v[240:255]\n"
    "s_add_u32 s22, s86, 20480\n"
    "ds_read_b64_tr_b16 a[128:129], v16 offset:23552\n"
    "ds_read_b64_tr_b16 a[130:131], v17 offset:23552\n"
    "v_add_f32 v8, 1.0, v8\n"
    "v_add_f32 v9, 1.0, v9\n"
    "v_rcp_f32 v6, v6\n"
    "v_mfma_f32_32x32x16_bf16 v[112:127], a[104:107], a[188:191], v[112:127]\n"
    "s_waitcnt lgkmcnt(6)\n"
    "ds_read_b64_tr_b16 a[132:133], v16 offset:23808\n"
    "ds_read_b64_tr_b16 a[134:135], v17 offset:23808\n"
    "buffer_load_dwordx4 v21, s[52:55], s20 offen offset:2048 lds\n"
    "v_rcp_f32 v7, v7\n"
    "v_mfma_f32_32x32x16_bf16 v[192:207], a[108:111], a[192:195], v[192:207]\n"
    "s_barrier\n"
    "buffer_load_dwordx4 a[4:7], v25, s[60:63], s22 offen sc1\n"
    "v_mfma_f32_32x32x16_bf16 v[208:223], a[112:115], a[192:195], v[208:223]\n"
    "s_add_u32 s20, s73, 52224\n"
    "s_add_u32 s22, s86, 24576\n"
    "ds_read_b64_tr_b16 a[136:137], v16 offset:24064\n"
    "ds_read_b64_tr_b16 a[138:139], v17 offset:24064\n"
    "ds_read_b64_tr_b16 a[140:141], v16 offset:24320\n"
    "ds_read_b64_tr_b16 a[142:143], v17 offset:24320\n"
    "v_mfma_f32_32x32x16_bf16 v[224:239], a[116:119], a[192:195], v[224:239]\n"
    "s_waitcnt lgkmcnt(8)\n"
    "ds_read_b64_tr_b16 a[104:105], v16 offset:22528\n"
    "ds_read_b64_tr_b16 a[106:107], v17 offset:22528\n"
    "buffer_load_dwordx4 v21, s[52:55], s20 offen offset:3072 lds\n"
    "v_rcp_f32 v8, v8\n"
    "v_mfma_f32_32x32x16_bf16 v[240:255], a[120:123], a[192:195], v[240:255]\n"
    "s_add_u32 s74, s74, s69\n"
    "s_add_u32 m0, s68, 8192\n"
    "ds_read_b64_tr_b16 a[108:109], v16 offset:24576\n"
    "ds_read_b64_tr_b16 a[110:111], v17 offset:24576\n"
    "buffer_load_dwordx4 v23, s[52:55], s74 offen lds\n"
    "v_rcp_f32 v9, v9\n"
    "v_mfma_f32_32x32x16_bf16 v[112:127], a[124:127], a[192:195], v[112:127]\n"
    "s_waitcnt vmcnt(16) lgkmcnt(6)\n"
    "ds_read_b64_tr_b16 a[112:113], v16 offset:24832\n"
    "ds_read_b64_tr_b16 a[114:115], v17 offset:24832\n"
    "v_mul_f32 v6, v100, v6\n"
    "v_mul_f32 v7, v101, v7\n"
    "v_mul_f32 v8, v102, v8\n"
    "v_mul_f32 v9, v103, v9\n"
    "v_mfma_f32_32x32x16_bf16 v[192:207], a[128:131], a[48:51], v[192:207]\n"
    "s_add_u32 m0, s68, 9216\n"
    "ds_read_b64_tr_b16 a[116:117], v16 offset:25088\n"
    "ds_read_b64_tr_b16 a[118:119], v17 offset:25088\n"
    "v_cvt_pk_bf16_f32 v90, v6, v7\n"
    "v_cvt_pk_bf16_f32 v91, v8, v9\n"
    "v_exp_f32 v2, v104\n"
    "v_mfma_f32_32x32x16_bf16 v[208:223], a[132:135], a[48:51], v[208:223]\n"
    "s_add_u32 s21, s83, 65536\n"
    "ds_read_b64_tr_b16 a[120:121], v16 offset:25344\n"
    "ds_read_b64_tr_b16 a[122:123], v17 offset:25344\n"
    "buffer_store_dwordx4 v[88:91], v25, s[60:63], s21 offen sc1\n"
    "buffer_load_dword v42, v28, s[60:63], s12 offen sc1\n"
    "v_mfma_f32_32x32x16_bf16 v[224:239], a[136:139], a[48:51], v[224:239]\n"
    "s_waitcnt lgkmcnt(8)\n"
    "ds_read_b64_tr_b16 a[124:125], v16 offset:22784\n"
    "ds_read_b64_tr_b16 a[126:127], v17 offset:22784\n"
    "v_exp_f32 v3, v105\n"
    "v_exp_f32 v4, v106\n"
    "v_mfma_f32_32x32x16_bf16 v[240:255], a[140:143], a[48:51], v[240:255]\n"
    "ds_read_b64_tr_b16 a[128:129], v16 offset:25600\n"
    "ds_read_b64_tr_b16 a[130:131], v17 offset:25600\n"
    "v_exp_f32 v5, v107\n"
    "v_add_f32 v2, 1.0, v2\n"
    "v_add_f32 v3, 1.0, v3\n"
    "v_add_f32 v4, 1.0, v4\n"
    "v_mfma_f32_32x32x16_bf16 v[112:127], a[104:107], a[48:51], v[112:127]\n"
    "s_waitcnt vmcnt(17) lgkmcnt(6)\n"
    "ds_read_b64_tr_b16 a[132:133], v16 offset:25856\n"
    "ds_read_b64_tr_b16 a[134:135], v17 offset:25856\n"
    "v_add_f32 v5, 1.0, v5\n"
    "v_rcp_f32 v2, v2\n"
    "v_mfma_f32_32x32x16_bf16 v[192:207], a[108:111], a[52:55], v[192:207]\n"
    "ds_read_b64_tr_b16 a[136:137], v16 offset:26112\n"
    "ds_read_b64_tr_b16 a[138:139], v17 offset:26112\n"
    "buffer_load_dwordx4 a[8:11], v25, s[60:63], s22 offen sc1\n"
    "v_rcp_f32 v3, v3\n"
    "v_mfma_f32_32x32x16_bf16 v[208:223], a[112:115], a[52:55], v[208:223]\n"
    "s_add_u32 s22, s86, 28672\n"
    "ds_read_b64_tr_b16 a[140:141], v16 offset:26368\n"
    "ds_read_b64_tr_b16 a[142:143], v17 offset:26368\n"
    "v_rcp_f32 v4, v4\n"
    "v_rcp_f32 v5, v5\n"
    "v_mul_f32 v2, v104, v2\n"
    "v_mfma_f32_32x32x16_bf16 v[224:239], a[116:119], a[52:55], v[224:239]\n"
    "s_waitcnt lgkmcnt(8)\n"
    "ds_read_b64_tr_b16 a[104:105], v16 offset:23040\n"
    "ds_read_b64_tr_b16 a[106:107], v17 offset:23040\n"
    "buffer_load_dwordx4 v21, s[52:55], s74 offen lds\n"
    "v_mul_f32 v3, v105, v3\n"
    "v_mul_f32 v4, v106, v4\n"
    "v_mul_f32 v5, v107, v5\n"
    "v_mfma_f32_32x32x16_bf16 v[240:255], a[120:123], a[52:55], v[240:255]\n"
    "s_add_u32 s20, s74, 17408\n"
    "ds_read_b64_tr_b16 a[108:109], v16 offset:26624\n"
    "ds_read_b64_tr_b16 a[110:111], v17 offset:26624\n"
    "buffer_load_dwordx4 v21, s[52:55], s20 offen offset:1024 lds\n"
    "v_cvt_pk_bf16_f32 v92, v2, v3\n"
    "v_cvt_pk_bf16_f32 v93, v4, v5\n"
    "v_mfma_f32_32x32x16_bf16 v[112:127], a[124:127], a[52:55], v[112:127]\n"
    "s_waitcnt vmcnt(18) lgkmcnt(6)\n"
    "ds_read_b64_tr_b16 a[112:113], v16 offset:26880\n"
    "ds_read_b64_tr_b16 a[114:115], v17 offset:26880\n"
    "buffer_load_dwordx4 a[12:15], v25, s[60:63], s22 offen sc1\n"
    "v_exp_f32 v6, v108\n"
    "v_mfma_f32_32x32x16_bf16 v[192:207], a[128:131], a[56:59], v[192:207]\n"
    "s_add_u32 s20, s74, 34816\n"
    "ds_read_b64_tr_b16 a[116:117], v16 offset:27136\n"
    "ds_read_b64_tr_b16 a[118:119], v17 offset:27136\n"
    "v_exp_f32 v7, v109\n"
    "v_exp_f32 v8, v110\n"
    "v_mfma_f32_32x32x16_bf16 v[208:223], a[132:135], a[56:59], v[208:223]\n"
    "ds_read_b64_tr_b16 a[120:121], v16 offset:27392\n"
    "ds_read_b64_tr_b16 a[122:123], v17 offset:27392\n"
    "buffer_load_dword v20, v27, s[60:63], s7 offen offset:512 sc1\n"
    "buffer_load_dwordx4 v21, s[52:55], s20 offen offset:2048 lds\n"
    "v_mfma_f32_32x32x16_bf16 v[224:239], a[136:139], a[56:59], v[224:239]\n"
    "s_waitcnt lgkmcnt(8)\n"
    "s_add_u32 s73, s73, s13\n"
    "s_add_u32 m0, s68, 12288\n"
    "ds_read_b64_tr_b16 a[124:125], v16 offset:23296\n"
    "ds_read_b64_tr_b16 a[126:127], v17 offset:23296\n"
    "buffer_load_dwordx4 v23, s[52:55], s73 offen lds\n"
    "v_exp_f32 v9, v111\n"
    "v_mfma_f32_32x32x16_bf16 v[240:255], a[140:143], a[56:59], v[240:255]\n"
    "ds_read_b64_tr_b16 a[128:129], v16 offset:27648\n"
    "ds_read_b64_tr_b16 a[130:131], v17 offset:27648\n"
    "v_add_f32 v6, 1.0, v6\n"
    "v_add_f32 v7, 1.0, v7\n"
    "v_add_f32 v8, 1.0, v8\n"
    "v_add_f32 v9, 1.0, v9\n"
    "v_mfma_f32_32x32x16_bf16 v[112:127], a[104:107], a[56:59], v[112:127]\n"
    "s_waitcnt vmcnt(21) lgkmcnt(6)\n"
    "s_add_u32 m0, s68, 13312\n"
    "ds_read_b64_tr_b16 a[132:133], v16 offset:27904\n"
    "ds_read_b64_tr_b16 a[134:135], v17 offset:27904\n"
    "v_rcp_f32 v6, v6\n"
    "v_mfma_f32_32x32x16_bf16 v[192:207], a[108:111], a[60:63], v[192:207]\n"
    "s_barrier\n"
    "buffer_load_dwordx4 v21, s[52:55], s73 offen lds\n"
    "v_mfma_f32_32x32x16_bf16 v[208:223], a[112:115], a[60:63], v[208:223]\n"
    "s_add_u32 s20, s74, 54272\n"
    "ds_read_b64_tr_b16 a[136:137], v16 offset:28160\n"
    "ds_read_b64_tr_b16 a[138:139], v17 offset:28160\n"
    "ds_read_b64_tr_b16 a[140:141], v16 offset:28416\n"
    "ds_read_b64_tr_b16 a[142:143], v17 offset:28416\n"
    "buffer_load_dwordx4 v21, s[52:55], s20 offen offset:1024 lds\n"
    "v_mfma_f32_32x32x16_bf16 v[224:239], a[116:119], a[60:63], v[224:239]\n"
    "s_waitcnt vmcnt(9) lgkmcnt(8)\n"
    "s_add_u32 s75, s75, s69\n"
    "s_add_u32 m0, s68, 15360\n"
    "ds_read_b64_tr_b16 a[104:105], v16 offset:28672\n"
    "ds_read_b64_tr_b16 a[106:107], v17 offset:28672\n"
    "v_cmp_ne_u32 vcc, v29, v42\n"
    "s_cmp_lg_u64 vcc, 0\n"
    "s_cbranch_scc1 L_slow7_%=\n"
    "L_ret7_%=:\n"
    "v_mfma_f32_32x32x16_bf16 v[240:255], a[120:123], a[60:63], v[240:255]\n"
    "ds_read_b64_tr_b16 a[108:109], v16 offset:28928\n"
    "ds_read_b64_tr_b16 a[110:111], v17 offset:28928\n"
    "buffer_load_dwordx4 v21, s[52:55], s75 offen lds\n"
    "buffer_load_dwordx4 a[220:223], v25, s[60:63], s95 offen sc1\n"
    "v_mfma_f32_32x32x16_bf16 v[112:127], a[124:127], a[60:63], v[112:127]\n"
    "s_waitcnt lgkmcnt(4)\n"
    "s_add_u32 s20, s75, 17408\n"
    "ds_read_b64_tr_b16 a[112:113], v16 offset:29184\n"
    "ds_read_b64_tr_b16 a[114:115], v17 offset:29184\n"
    "v_rcp_f32 v7, v7\n"
    "v_mfma_f32_32x32x16_bf16 v[192:207], a[128:131], a[64:67], v[192:207]\n"
    "ds_read_b64_tr_b16 a[116:117], v16 offset:29440\n"
    "ds_read_b64_tr_b16 a[118:119], v17 offset:29440\n"
    "buffer_load_dwordx4 v21, s[52:55], s20 offen offset:1024 lds\n"
    "v_rcp_f32 v8, v8\n"
    "v_mfma_f32_32x32x16_bf16 v[208:223], a[132:135], a[64:67], v[208:223]\n"
    "s_add_u32 s20, s73, 16384\n"
    "ds_read_b64_tr_b16 a[120:121], v16 offset:29696\n"
    "ds_read_b64_tr_b16 a[122:123], v17 offset:29696\n"
    "buffer_load_dwordx4 v21, s[52:55], s20 offen offset:2048 lds\n"
    "v_rcp_f32 v9, v9\n"
    "v_mul_f32 v6, v108, v6\n"
    "v_mfma_f32_32x32x16_bf16 v[224:239], a[136:139], a[64:67], v[224:239]\n"
    "s_add_u32 m0, s68, 18432\n"
    "ds_read_b64_tr_b16 a[124:125], v16 offset:29952\n"
    "ds_read_b64_tr_b16 a[126:127], v17 offset:29952\n"
    "buffer_load_dwordx4 v14, s[56:59], s89 offen lds\n"
    "v_mul_f32 v7, v109, v7\n"
    "v_mul_f32 v8, v110, v8\n"
    "v_mul_f32 v9, v111, v9\n"
    "v_mfma_f32_32x32x16_bf16 v[240:255], a[140:143], a[64:67], v[240:255]\n"
    "s_waitcnt lgkmcnt(4)\n"
    "ds_read_b64_tr_b16 a[128:129], v16 offset:30208\n"
    "ds_read_b64_tr_b16 a[130:131], v17 offset:30208\n"
    "v_cvt_pk_bf16_f32 v94, v6, v7\n"
    "v_cvt_pk_bf16_f32 v95, v8, v9\n"
    "v_mfma_f32_32x32x16_bf16 v[192:207], a[104:107], a[68:71], v[192:207]\n"
    "s_add_u32 s21, s83, 69632\n"
    "ds_read_b64_tr_b16 a[132:133], v16 offset:30464\n"
    "ds_read_b64_tr_b16 a[134:135], v17 offset:30464\n"
    "buffer_store_dwordx4 v[92:95], v25, s[60:63], s21 offen sc1\n"
    "v_mfma_f32_32x32x16_bf16 v[208:223], a[108:111], a[68:71], v[208:223]\n"
    "ds_read_b64_tr_b16 a[136:137], v16 offset:30720\n"
    "ds_read_b64_tr_b16 a[138:139], v17 offset:30720\n"
    "v_mfma_f32_32x32x16_bf16 v[224:239], a[112:115], a[68:71], v[224:239]\n"
    "ds_read_b64_tr_b16 a[140:141], v16 offset:30976\n"
    "ds_read_b64_tr_b16 a[142:143], v17 offset:30976\n"
    "v_mfma_f32_32x32x16_bf16 v[240:255], a[116:119], a[68:71], v[240:255]\n"
    "s_waitcnt vmcnt(10) lgkmcnt(4)\n"
    "ds_read_b64_tr_b16 a[104:105], v16 offset:31232\n"
    "ds_read_b64_tr_b16 a[106:107], v17 offset:31232\n"
    "v_cmp_ne_u32 vcc, v29, v20\n"
    "s_cmp_lg_u64 vcc, 0\n"
    "s_cbranch_scc1 L_slow5_%=\n"
    "L_ret5_%=:\n"
    "v_mfma_f32_32x32x16_bf16 v[192:207], a[120:123], a[72:75], v[192:207]\n"
    "s_add_u32 s22, s84, 40960\n"
    "s_add_u32 m0, s68, 19456\n"
    "ds_read_b64_tr_b16 a[108:109], v16 offset:31488\n"
    "ds_read_b64_tr_b16 a[110:111], v17 offset:31488\n"
    "buffer_load_dwordx4 v24, s[56:59], s89 offen lds\n"
    "v_mfma_f32_32x32x16_bf16 v[208:223], a[124:127], a[72:75], v[208:223]\n"
    "ds_read_b64_tr_b16 a[112:113], v16 offset:31744\n"
    "ds_read_b64_tr_b16 a[114:115], v17 offset:31744\n"
    "buffer_load_dwordx4 a[16:19], v25, s[60:63], s22 offen sc1\n"
    "v_mfma_f32_32x32x16_bf16 v[224:239], a[128:131], a[72:75], v[224:239]\n"
    "s_add_u32 s22, s84, 45056\n"
    "s_add_u32 s20, s75, 35840\n"
    "ds_read_b64_tr_b16 a[116:117], v16 offset:32000\n"
    "ds_read_b64_tr_b16 a[118:119], v17 offset:32000\n"
    "v_mfma_f32_32x32x16_bf16 v[240:255], a[132:135], a[72:75], v[240:255]\n"
    "s_waitcnt lgkmcnt(4)\n"
    "ds_read_b64_tr_b16 a[120:121], v16 offset:32256\n"
    "ds_read_b64_tr_b16 a[122:123], v17 offset:32256\n"
    "buffer_load_dwordx4 a[20:23], v25, s[60:63], s22 offen sc1\n"
    "v_mfma_f32_32x32x16_bf16 v[192:207], a[136:139], a[76:79], v[192:207]\n"
    "s_add_u32 s14, s90, 4096\n"
    "s_add_u32 s22, s84, 49152\n"
    "ds_read_b64_tr_b16 a[124:125], v16 offset:32512\n"
    "ds_read_b64_tr_b16 a[126:127], v17 offset:32512\n"
    "v_lshrrev_b32 v39, 4, v25\n"
    "s_add_u32 s19, s39, 1\n"
    "v_mfma_f32_32x32x16_bf16 v[208:223], a[140:143], a[76:79], v[208:223]\n"
    "ds_read_b64_tr_b16 a[128:129], v16\n"
    "ds_read_b64_tr_b16 a[130:131], v17\n"
    "buffer_load_dwordx4 a[24:27], v25, s[60:63], s22 offen sc1\n"
    "v_mfma_f32_32x32x16_bf16 v[224:239], a[104:107], a[76:79], v[224:239]\n"
    "ds_read_b64_tr_b16 a[132:133], v16 offset:256\n"
    "ds_read_b64_tr_b16 a[134:135], v17 offset:256\n"
    "buffer_load_dwordx4 v21, s[52:55], s20 offen offset:1024 lds\n"
    "v_mfma_f32_32x32x16_bf16 v[240:255], a[108:111], a[76:79], v[240:255]\n"
    "s_waitcnt lgkmcnt(4)\n"
    "s_barrier\n"
    "v_mfma_f32_32x32x16_bf16 v[192:207], a[112:115], a[0:3], v[192:207]\n"
    "s_add_u32 s20, s75, 53248\n"
    "ds_read_b64_tr_b16 a[136:137], v16 offset:512\n"
    "ds_read_b64_tr_b16 a[138:139], v17 offset:512\n"
    "ds_read_b64_tr_b16 a[140:141], v16 offset:768\n"
    "ds_read_b64_tr_b16 a[142:143], v17 offset:768\n"
    "buffer_load_dwordx4 v21, s[52:55], s20 offen offset:2048 lds\n"
    "v_mfma_f32_32x32x16_bf16 v[208:223], a[116:119], a[0:3], v[208:223]\n"
    "s_add_u32 s76, s76, s69\n"
    "s_add_u32 m0, s68, 22528\n"
    "s_add_u32 s22, s84, 53248\n"
    "ds_read_b64_tr_b16 a[104:105], v16 offset:1024\n"
    "ds_read_b64_tr_b16 a[106:107], v17 offset:1024\n"
    "buffer_load_dwordx4 v21, s[52:55], s76 offen lds\n"
    "v_mfma_f32_32x32x16_bf16 v[224:239], a[120:123], a[0:3], v[224:239]\n"
    "ds_read_b64_tr_b16 a[108:109], v16 offset:1280\n"
    "ds_read_b64_tr_b16 a[110:111], v17 offset:1280\n"
    "buffer_load_dwordx4 a[28:31], v25, s[60:63], s22 offen sc1\n"
    "v_mfma_f32_32x32x16_bf16 v[240:255], a[124:127], a[0:3], v[240:255]\n"
    "s_waitcnt lgkmcnt(4)\n"
    "s_add_u32 s20, s73, 35840\n"
    "ds_read_b64_tr_b16 a[112:113], v16 offset:1536\n"
    "ds_read_b64_tr_b16 a[114:115], v17 offset:1536\n"
    "buffer_load_dwordx4 v21, s[52:55], s20 offen offset:1024 lds\n"
    "v_mfma_f32_32x32x16_bf16 v[192:207], a[128:131], a[4:7], v[192:207]\n"
    "s_add_u32 s22, s85, 40960\n"
    "s_add_u32 s20, s76, 16384\n"
    "ds_read_b64_tr_b16 a[116:117], v16 offset:1792\n"
    "ds_read_b64_tr_b16 a[118:119], v17 offset:1792\n"
    "v_mfma_f32_32x32x16_bf16 v[208:223], a[132:135], a[4:7], v[208:223]\n"
    "ds_read_b64_tr_b16 a[120:121], v16 offset:2048\n"
    "ds_read_b64_tr_b16 a[122:123], v17 offset:2048\n"
    "buffer_load_dwordx4 a[32:35], v25, s[60:63], s22 offen sc1\n"
    "v_mfma_f32_32x32x16_bf16 v[224:239], a[136:139], a[4:7], v[224:239]\n"
    "ds_read_b64_tr_b16 a[124:125], v16 offset:2304\n"
    "ds_read_b64_tr_b16 a[126:127], v17 offset:2304\n"
    "buffer_load_dwordx4 v21, s[52:55], s20 offen offset:2048 lds\n"
    "v_mfma_f32_32x32x16_bf16 v[240:255], a[140:143], a[4:7], v[240:255]\n"
    "s_waitcnt lgkmcnt(4)\n"
    "s_add_u32 s20, s76, 33792\n"
    "ds_read_b64_tr_b16 a[128:129], v16 offset:2560\n"
    "ds_read_b64_tr_b16 a[130:131], v17 offset:2560\n"
    "buffer_load_dwordx4 v21, s[52:55], s20 offen offset:3072 lds\n"
    "v_mfma_f32_32x32x16_bf16 v[192:207], a[104:107], a[8:11], v[192:207]\n"
    "s_add_u32 s22, s85, 45056\n"
    "s_add_u32 m0, s68, 26624\n"
    "s_add_u32 s20, s76, 55296\n"
    "ds_read_b64_tr_b16 a[132:133], v16 offset:2816\n"
    "ds_read_b64_tr_b16 a[134:135], v17 offset:2816\n"
    "v_mfma_f32_32x32x16_bf16 v[208:223], a[108:111], a[8:11], v[208:223]\n"
    "ds_read_b64_tr_b16 a[136:137], v16 offset:4096\n"
    "ds_read_b64_tr_b16 a[138:139], v17 offset:4096\n"
    "buffer_load_dwordx4 a[36:39], v25, s[60:63], s22 offen sc1\n"
    "v_mfma_f32_32x32x16_bf16 v[224:239], a[112:115], a[8:11], v[224:239]\n"
    "ds_read_b64_tr_b16 a[140:141], v16 offset:4352\n"
    "ds_read_b64_tr_b16 a[142:143], v17 offset:4352\n"
    "buffer_load_dwordx4 v21, s[52:55], s20 offen lds\n"
    "v_mfma_f32_32x32x16_bf16 v[240:255], a[116:119], a[8:11], v[240:255]\n"
    "s_waitcnt lgkmcnt(4)\n"
    "ds_read_b64_tr_b16 a[104:105], v16 offset:4608\n"
    "ds_read_b64_tr_b16 a[106:107], v17 offset:4608\n"
    "buffer_load_dwordx4 a[224:227], v25, s[60:63], s95 offen offset:1024 sc1\n"
    "buffer_load_dword a80, v15, s[60:63], s95 offen offset:2048 sc1\n"
    "v_mfma_f32_32x32x16_bf16 v[192:207], a[120:123], a[12:15], v[192:207]\n"
    "ds_read_b64_tr_b16 a[108:109], v16 offset:4864\n"
    "ds_read_b64_tr_b16 a[110:111], v17 offset:4864\n"
    "v_cmp_gt_u32 vcc, 3, v39\n"
    "s_nop 1\n"
    "v_cndmask_b32 v35, 0, v39, vcc\n"
    "v_mfma_f32_32x32x16_bf16 v[208:223], a[124:127], a[12:15], v[208:223]\n"
    "s_waitcnt vmcnt(16)\n"
    "ds_read_b64_tr_b16 a[112:113], v16 offset:3072\n"
    "ds_read_b64_tr_b16 a[114:115], v17 offset:3072\n"
    "buffer_store_dword v29, v26, s[60:63], s6 offen offset:1536 sc1\n"
    "v_add_u32 v36, s19, v35\n"
    "v_mfma_f32_32x32x16_bf16 v[224:239], a[128:131], a[12:15], v[224:239]\n"
    "s_add_u32 s20, s73, 54272\n"
    "ds_read_b64_tr_b16 a[116:117], v16 offset:5120\n"
    "ds_read_b64_tr_b16 a[118:119], v17 offset:5120\n"
    "buffer_load_dwordx4 v21, s[52:55], s20 offen offset:1024 lds\n"
    "v_and_b32 v37, 3, v36\n"
    "v_lshlrev_b32 v31, 7, v37\n"
    "v_mfma_f32_32x32x16_bf16 v[240:255], a[132:135], a[12:15], v[240:255]\n"
    "s_waitcnt lgkmcnt(6)\n"
    "s_add_u32 s28, s95, 2304\n"
    "s_add_u32 s74, s74, s13\n"
    "s_add_u32 m0, s68, 28672\n"
    "s_add_u32 s22, s85, 49152\n"
    "ds_read_b64_tr_b16 a[120:121], v16 offset:5376\n"
    "ds_read_b64_tr_b16 a[122:123], v17 offset:5376\n"
    "v_mfma_f32_32x32x16_bf16 v[192:207], a[136:139], a[204:207], v[192:207]\n"
    "ds_read_b64_tr_b16 a[124:125], v16 offset:5632\n"
    "ds_read_b64_tr_b16 a[126:127], v17 offset:5632\n"
    "buffer_load_dwordx4 a[228:231], v25, s[60:63], s28 offen sc1\n"
    "v_and_b32 v37, 7, v39\n"
    "v_lshlrev_b32 v43, 8, v37\n"
    "v_mfma_f32_32x32x16_bf16 v[208:223], a[140:143], a[204:207], v[208:223]\n"
    "ds_read_b64_tr_b16 a[128:129], v16 offset:5888\n"
    "ds_read_b64_tr_b16 a[130:131], v17 offset:5888\n"
    "buffer_load_dwordx4 a[232:235], v25, s[60:63], s28 offen offset:1024 sc1\n"
    "buffer_load_dword a81, v15, s[60:63], s28 offen offset:2048 sc1\n"
    "v_mfma_f32_32x32x16_bf16 v[224:239], a[104:107], a[204:207], v[224:239]\n"
    "s_waitcnt lgkmcnt(8)\n"
    "ds_read_b64_tr_b16 a[132:133], v16 offset:3328\n"
    "ds_read_b64_tr_b16 a[134:135], v17 offset:3328\n"
    "v_mfma_f32_32x32x16_bf16 v[240:255], a[108:111], a[204:207], v[240:255]\n"
    "s_barrier\n"
    "s_add_u32 s94, s94, s82\n"
    "v_mfma_f32_32x32x16_bf16 v[112:127], a[112:115], a[204:207], v[112:127]\n"
    "s_waitcnt lgkmcnt(4)\n"
    "ds_read_b64_tr_b16 a[136:137], v16 offset:6144\n"
    "ds_read_b64_tr_b16 a[138:139], v17 offset:6144\n"
    "ds_read_b64_tr_b16 a[140:141], v16 offset:6400\n"
    "ds_read_b64_tr_b16 a[142:143], v17 offset:6400\n"
    "buffer_load_dwordx4 v23, s[52:55], s74 offen lds\n"
    "v_mfma_f32_32x32x16_bf16 v[192:207], a[116:119], a[208:211], v[192:207]\n"
    "s_and_b32 s94, s94, 7\n"
    "s_mul_i32 s28, s94, 4608\n"
    "s_add_u32 s95, s97, s28\n"
    "s_add_u32 m0, s68, 29696\n"
    "ds_read_b64_tr_b16 a[104:105], v16 offset:6656\n"
    "ds_read_b64_tr_b16 a[106:107], v17 offset:6656\n"
    "v_mfma_f32_32x32x16_bf16 v[208:223], a[120:123], a[208:211], v[208:223]\n"
    "ds_read_b64_tr_b16 a[108:109], v16 offset:6912\n"
    "ds_read_b64_tr_b16 a[110:111], v17 offset:6912\n"
    "buffer_load_dwordx4 a[40:43], v25, s[60:63], s22 offen sc1\n"
    "v_mfma_f32_32x32x16_bf16 v[224:239], a[124:127], a[208:211], v[224:239]\n"
    "s_waitcnt lgkmcnt(8)\n"
    "ds_read_b64_tr_b16 a[112:113], v16 offset:3584\n"
    "ds_read_b64_tr_b16 a[114:115], v17 offset:3584\n"
    "buffer_load_dwordx4 a[236:239], v25, s[60:63], s95 offen sc1\n"
    "v_mfma_f32_32x32x16_bf16 v[240:255], a[128:131], a[208:211], v[240:255]\n"
    "s_add_u32 s22, s85, 53248\n"
    "ds_read_b64_tr_b16 a[116:117], v16 offset:7168\n"
    "ds_read_b64_tr_b16 a[118:119], v17 offset:7168\n"
    "v_mfma_f32_32x32x16_bf16 v[112:127], a[132:135], a[208:211], v[112:127]\n"
    "s_waitcnt lgkmcnt(6)\n"
    "ds_read_b64_tr_b16 a[120:121], v16 offset:7424\n"
    "ds_read_b64_tr_b16 a[122:123], v17 offset:7424\n"
    "buffer_load_dwordx4 a[240:243], v25, s[60:63], s95 offen offset:1024 sc1\n"
    "buffer_load_dword a82, v15, s[60:63], s95 offen offset:2048 sc1\n"
    "v_mfma_f32_32x32x16_bf16 v[192:207], a[136:139], a[212:215], v[192:207]\n"
    "s_add_u32 s28, s95, 2304\n"
    "ds_read_b64_tr_b16 a[124:125], v16 offset:7680\n"
    "ds_read_b64_tr_b16 a[126:127], v17 offset:7680\n"
    "v_mfma_f32_32x32x16_bf16 v[208:223], a[140:143], a[212:215], v[208:223]\n"
    "ds_read_b64_tr_b16 a[128:129], v16 offset:7936\n"
    "ds_read_b64_tr_b16 a[130:131], v17 offset:7936\n"
    "buffer_load_dwordx4 v21, s[52:55], s74 offen lds\n"
    "v_mfma_f32_32x32x16_bf16 v[224:239], a[104:107], a[212:215], v[224:239]\n"
    "s_waitcnt lgkmcnt(8)\n"
    "ds_read_b64_tr_b16 a[132:133], v16 offset:3840\n"
    "ds_read_b64_tr_b16 a[134:135], v17 offset:3840\n"
    "buffer_load_dwordx4 a[244:247], v25, s[60:63], s28 offen sc1\n"
    "v_mfma_f32_32x32x16_bf16 v[240:255], a[108:111], a[212:215], v[240:255]\n"
    "s_add_u32 s20, s74, 17408\n"
    "ds_read_b64_tr_b16 a[136:137], v16 offset:9216\n"
    "ds_read_b64_tr_b16 a[138:139], v17 offset:9216\n"
    "v_mfma_f32_32x32x16_bf16 v[112:127], a[112:115], a[212:215], v[112:127]\n"
    "s_waitcnt lgkmcnt(6)\n"
    "ds_read_b64_tr_b16 a[140:141], v16 offset:9472\n"
    "ds_read_b64_tr_b16 a[142:143], v17 offset:9472\n"
    "buffer_load_dwordx4 a[248:251], v25, s[60:63], s28 offen offset:1024 sc1\n"
    "buffer_load_dword a83, v15, s[60:63], s28 offen offset:2048 sc1\n"
    "v_mfma_f32_32x32x16_bf16 v[192:207], a[116:119], a[216:219], v[192:207]\n"
    "ds_read_b64_tr_b16 a[104:105], v16 offset:9728\n"
    "ds_read_b64_tr_b16 a[106:107], v17 offset:9728\n"
    "buffer_load_dwordx4 v21, s[52:55], s20 offen offset:1024 lds\n"
    "v_mfma_f32_32x32x16_bf16 v[208:223], a[120:123], a[216:219], v[208:223]\n"
    "s_add_u32 s20, s74, 34816\n"
    "s_add_u32 s94, s94, s82\n"
    "s_and_b32 s94, s94, 7\n"
    "s_mul_i32 s28, s94, 4608\n"
    "s_add_u32 s95, s97, s28\n"
    "ds_read_b64_tr_b16 a[108:109], v16 offset:9984\n"
    "ds_read_b64_tr_b16 a[110:111], v17 offset:9984\n"
    "v_mfma_f32_32x32x16_bf16 v[224:239], a[124:127], a[216:219], v[224:239]\n"
    "s_waitcnt lgkmcnt(8)\n"
    "ds_read_b64_tr_b16 a[112:113], v16 offset:8192\n"
    "ds_read_b64_tr_b16 a[114:115], v17 offset:8192\n"
    "buffer_load_dwordx4 v21, s[52:55], s20 offen offset:2048 lds\n"
    "v_mfma_f32_32x32x16_bf16 v[240:255], a[128:131], a[216:219], v[240:255]\n"
    "s_add_u32 m0, s68, 0\n"
    "s_add_u32 s20, s74, 55296\n"
    "ds_read_b64_tr_b16 a[116:117], v16 offset:10240\n"
    "ds_read_b64_tr_b16 a[118:119], v17 offset:10240\n"
    "buffer_load_dwordx4 v21, s[52:55], s20 offen lds\n"
    "v_mfma_f32_32x32x16_bf16 v[112:127], a[132:135], a[216:219], v[112:127]\n"
    "s_waitcnt vmcnt(31) lgkmcnt(6)\n"
    "ds_read_b64_tr_b16 a[120:121], v16 offset:10496\n"
    "ds_read_b64_tr_b16 a[122:123], v17 offset:10496\n"
    "buffer_load_dwordx4 a[44:47], v25, s[60:63], s22 offen sc1\n"
    "v_mfma_f32_32x32x16_bf16 v[192:207], a[136:139], a[16:19], v[192:207]\n"
    "s_add_u32 s75, s75, s13\n"
    "s_add_u32 m0, s68, 1024\n"
    "s_add_u32 s22, s86, 40960\n"
    "ds_read_b64_tr_b16 a[124:125], v16 offset:10752\n"
    "ds_read_b64_tr_b16 a[126:127], v17 offset:10752\n"
    "v_mfma_f32_32x32x16_bf16 v[208:223], a[140:143], a[16:19], v[208:223]\n"
    "ds_read_b64_tr_b16 a[128:129], v16 offset:11008\n"
    "ds_read_b64_tr_b16 a[130:131], v17 offset:11008\n"
    "buffer_load_dwordx4 v21, s[52:55], s75 offen lds\n"
    "v_mfma_f32_32x32x16_bf16 v[224:239], a[104:107], a[16:19], v[224:239]\n"
    "s_waitcnt lgkmcnt(8)\n"
    "s_add_u32 s20, s75, 17408\n"
    "ds_read_b64_tr_b16 a[132:133], v16 offset:8448\n"
    "ds_read_b64_tr_b16 a[134:135], v17 offset:8448\n"
    "buffer_load_dwordx4 v21, s[52:55], s20 offen offset:1024 lds\n"
    "v_mfma_f32_32x32x16_bf16 v[240:255], a[108:111], a[16:19], v[240:255]\n"
    "s_barrier\n"
    "s_add_u32 s20, s75, 34816\n"
    "v_mfma_f32_32x32x16_bf16 v[112:127], a[112:115], a[16:19], v[112:127]\n"
    "s_waitcnt vmcnt(33) lgkmcnt(4)\n"
    "ds_read_b64_tr_b16 a[136:137], v16 offset:11264\n"
    "ds_read_b64_tr_b16 a[138:139], v17 offset:11264\n"
    "ds_read_b64_tr_b16 a[140:141], v16 offset:11520\n"
    "ds_read_b64_tr_b16 a[142:143], v17 offset:11520\n"
    "buffer_load_dwordx4 a[48:51], v25, s[60:63], s22 offen sc1\n"
    "v_mfma_f32_32x32x16_bf16 v[192:207], a[116:119], a[20:23], v[192:207]\n"
    "s_add_u32 s22, s86, 45056\n"
    "ds_read_b64_tr_b16 a[104:105], v16 offset:11776\n"
    "ds_read_b64_tr_b16 a[106:107], v17 offset:11776\n"
    "buffer_load_dwordx4 a[52:55], v25, s[60:63], s22 offen sc1\n"
    "v_mfma_f32_32x32x16_bf16 v[208:223], a[120:123], a[20:23], v[208:223]\n"
    "s_add_u32 s22, s86, 49152\n"
    "ds_read_b64_tr_b16 a[108:109], v16 offset:12032\n"
    "ds_read_b64_tr_b16 a[110:111], v17 offset:12032\n"
    "buffer_load_dwordx4 a[56:59], v25, s[60:63], s22 offen sc1\n"
    "v_mfma_f32_32x32x16_bf16 v[224:239], a[124:127], a[20:23], v[224:239]\n"
    "s_waitcnt lgkmcnt(8)\n"
    "s_add_u32 s22, s86, 53248\n"
    "ds_read_b64_tr_b16 a[112:113], v16 offset:8704\n"
    "ds_read_b64_tr_b16 a[114:115], v17 offset:8704\n"
    "v_mfma_f32_32x32x16_bf16 v[240:255], a[128:131], a[20:23], v[240:255]\n"
    "ds_read_b64_tr_b16 a[116:117], v16 offset:13312\n"
    "ds_read_b64_tr_b16 a[118:119], v17 offset:13312\n"
    "buffer_load_dwordx4 a[60:63], v25, s[60:63], s22 offen sc1\n"
    "v_mfma_f32_32x32x16_bf16 v[112:127], a[132:135], a[20:23], v[112:127]\n"
    "s_waitcnt vmcnt(36) lgkmcnt(6)\n"
    "ds_read_b64_tr_b16 a[120:121], v16 offset:13568\n"
    "ds_read_b64_tr_b16 a[122:123], v17 offset:13568\n"
    "buffer_load_dwordx4 v21, s[52:55], s20 offen offset:2048 lds\n"
    "v_mfma_f32_32x32x16_bf16 v[192:207], a[136:139], a[24:27], v[192:207]\n"
    "s_add_u32 s20, s75, 52224\n"
    "ds_read_b64_tr_b16 a[124:125], v16 offset:13824\n"
    "ds_read_b64_tr_b16 a[126:127], v17 offset:13824\n"
    "v_mfma_f32_32x32x16_bf16 v[208:223], a[140:143], a[24:27], v[208:223]\n"
    "ds_read_b64_tr_b16 a[128:129], v16 offset:14080\n"
    "ds_read_b64_tr_b16 a[130:131], v17 offset:14080\n"
    "buffer_load_dwordx4 v21, s[52:55], s20 offen offset:3072 lds\n"
    "v_mfma_f32_32x32x16_bf16 v[224:239], a[104:107], a[24:27], v[224:239]\n"
    "s_waitcnt lgkmcnt(8)\n"
    "s_add_u32 s76, s76, s13\n"
    "s_add_u32 m0, s68, 5120\n"
    "ds_read_b64_tr_b16 a[132:133], v16 offset:12288\n"
    "ds_read_b64_tr_b16 a[134:135], v17 offset:12288\n"
    "buffer_load_dwordx4 v21, s[52:55], s76 offen lds\n"
    "v_mfma_f32_32x32x16_bf16 v[240:255], a[108:111], a[24:27], v[240:255]\n"
    "s_add_u32 s20, s76, 17408\n"
    "ds_read_b64_tr_b16 a[136:137], v16 offset:14336\n"
    "ds_read_b64_tr_b16 a[138:139], v17 offset:14336\n"
    "buffer_load_dwordx4 v21, s[52:55], s20 offen offset:1024 lds\n"
    "v_mfma_f32_32x32x16_bf16 v[112:127], a[112:115], a[24:27], v[112:127]\n"
    "s_waitcnt lgkmcnt(6)\n"
    "s_add_u32 s20, s76, 34816\n"
    "ds_read_b64_tr_b16 a[140:141], v16 offset:14592\n"
    "ds_read_b64_tr_b16 a[142:143], v17 offset:14592\n"
    "buffer_load_dwordx4 v21, s[52:55], s20 offen offset:2048 lds\n"
    "v_mfma_f32_32x32x16_bf16 v[192:207], a[116:119], v[80:83], v[192:207]\n"
    "ds_read_b64_tr_b16 a[104:105], v16 offset:14848\n"
    "ds_read_b64_tr_b16 a[106:107], v17 offset:14848\n"
    "v_mfma_f32_32x32x16_bf16 v[208:223], a[120:123], v[80:83], v[208:223]\n"
    "ds_read_b64_tr_b16 a[108:109], v16 offset:15104\n"
    "ds_read_b64_tr_b16 a[110:111], v17 offset:15104\n"
    "buffer_load_dwordx4 a[196:199], v25, s[60:63], s95 offen sc1\n"
    "v_mfma_f32_32x32x16_bf16 v[224:239], a[124:127], v[80:83], v[224:239]\n"
    "s_waitcnt lgkmcnt(8)\n"
    "ds_read_b64_tr_b16 a[112:113], v16 offset:8960\n"
    "ds_read_b64_tr_b16 a[114:115], v17 offset:8960\n"
    "buffer_load_dwordx4 a[200:203], v25, s[60:63], s95 offen offset:1024 sc1\n"
    "buffer_load_dword a84, v15, s[60:63], s95 offen offset:2048 sc1\n"
    "v_mfma_f32_32x32x16_bf16 v[240:255], a[128:131], v[80:83], v[240:255]\n"
    "ds_read_b64_tr_b16 a[116:117], v16 offset:15616\n"
    "ds_read_b64_tr_b16 a[118:119], v17 offset:15616\n"
    "buffer_load_dword v13, v27, s[60:63], s7 offen offset:1536 sc1\n"
    "v_mfma_f32_32x32x16_bf16 v[112:127], a[132:135], v[80:83], v[112:127]\n"
    "s_waitcnt vmcnt(41) lgkmcnt(6)\n"
    "s_add_u32 s28, s95, 2304\n"
    "ds_read_b64_tr_b16 a[120:121], v16 offset:15872\n"
    "ds_read_b64_tr_b16 a[122:123], v17 offset:15872\n"
    "buffer_load_dwordx4 a[164:167], v25, s[60:63], s28 offen sc1\n"
    "v_mfma_f32_32x32x16_bf16 v[192:207], a[136:139], a[28:31], v[192:207]\n"
    "ds_read_b64_tr_b16 a[124:125], v16 offset:16128\n"
    "ds_read_b64_tr_b16 a[126:127], v17 offset:16128\n"
    "v_mfma_f32_32x32x16_bf16 v[208:223], a[140:143], a[28:31], v[208:223]\n"
    "ds_read_b64_tr_b16 a[128:129], v16 offset:16640\n"
    "ds_read_b64_tr_b16 a[130:131], v17 offset:16640\n"
    "buffer_load_dwordx4 a[168:171], v25, s[60:63], s28 offen offset:1024 sc1\n"
    "buffer_load_dword a85, v15, s[60:63], s28 offen offset:2048 sc1\n"
    "v_mfma_f32_32x32x16_bf16 v[224:239], a[104:107], a[28:31], v[224:239]\n"
    "s_waitcnt lgkmcnt(8)\n"
    "s_add_u32 s94, s94, s82\n"
    "s_and_b32 s94, s94, 7\n"
    "s_mul_i32 s28, s94, 4608\n"
    "s_add_u32 s95, s97, s28\n"
    "ds_read_b64_tr_b16 a[132:133], v16 offset:16896\n"
    "ds_read_b64_tr_b16 a[134:135], v17 offset:16896\n"
    "v_mfma_f32_32x32x16_bf16 v[240:255], a[108:111], a[28:31], v[240:255]\n"
    "s_barrier\n"
    "v_mfma_f32_32x32x16_bf16 v[112:127], a[112:115], a[28:31], v[112:127]\n"
    "s_waitcnt vmcnt(42) lgkmcnt(4)\n"
    "ds_read_b64_tr_b16 a[136:137], v16 offset:17152\n"
    "ds_read_b64_tr_b16 a[138:139], v17 offset:17152\n"
    "ds_read_b64_tr_b16 a[140:141], v16 offset:17408\n"
    "ds_read_b64_tr_b16 a[142:143], v17 offset:17408\n"
    "buffer_load_dwordx4 a[172:175], v25, s[60:63], s95 offen sc1\n"
    "v_mfma_f32_32x32x16_bf16 v[208:223], a[116:119], a[32:35], v[208:223]\n"
    "ds_read_b64_tr_b16 a[104:105], v16 offset:17664\n"
    "ds_read_b64_tr_b16 a[106:107], v17 offset:17664\n"
    "buffer_load_dwordx4 a[176:179], v25, s[60:63], s95 offen offset:1024 sc1\n"
    "buffer_load_dword a86, v15, s[60:63], s95 offen offset:2048 sc1\n"
    "v_mfma_f32_32x32x16_bf16 v[224:239], a[120:123], a[32:35], v[224:239]\n"
    "s_add_u32 s28, s95, 2304\n"
    "ds_read_b64_tr_b16 a[108:109], v16 offset:17920\n"
    "ds_read_b64_tr_b16 a[110:111], v17 offset:17920\n"
    "buffer_load_dwordx4 v[48:51], v25, s[60:63], s28 offen sc1\n"
    "v_mfma_f32_32x32x16_bf16 v[240:255], a[124:127], a[32:35], v[240:255]\n"
    "s_waitcnt vmcnt(43) lgkmcnt(6)\n"
    "ds_read_b64_tr_b16 a[112:113], v16 offset:18176\n"
    "ds_read_b64_tr_b16 a[114:115], v17 offset:18176\n"
    "v_mfma_f32_32x32x16_bf16 v[208:223], a[128:131], a[36:39], v[208:223]\n"
    "ds_read_b64_tr_b16 a[116:117], v16 offset:12544\n"
    "ds_read_b64_tr_b16 a[118:119], v17 offset:12544\n"
    "buffer_load_dwordx4 v[52:55], v25, s[60:63], s28 offen offset:1024 sc1\n"
    "buffer_load_dword a87, v15, s[60:63], s28 offen offset:2048 sc1\n"
    "v_mfma_f32_32x32x16_bf16 v[224:239], a[132:135], a[36:39], v[224:239]\n"
    "s_add_u32 s94, s94, s82\n"
    "s_and_b32 s94, s94, 7\n"
    "s_mul_i32 s28, s94, 4608\n"
    "s_add_u32 s95, s97, s28\n"
    "ds_read_b64_tr_b16 a[120:121], v16 offset:18432\n"
    "ds_read_b64_tr_b16 a[122:123], v17 offset:18432\n"
    "v_mfma_f32_32x32x16_bf16 v[240:255], a[136:139], a[36:39], v[240:255]\n"
    "s_waitcnt lgkmcnt(6)\n"
    "ds_read_b64_tr_b16 a[124:125], v16 offset:18688\n"
    "ds_read_b64_tr_b16 a[126:127], v17 offset:18688\n"
    "buffer_load_dwordx4 v[56:59], v25, s[60:63], s95 offen sc1\n"
    "v_mfma_f32_32x32x16_bf16 v[192:207], a[140:143], v[84:87], v[192:207]\n"
    "ds_read_b64_tr_b16 a[128:129], v16 offset:18944\n"
    "ds_read_b64_tr_b16 a[130:131], v17 offset:18944\n"
    "v_mfma_f32_32x32x16_bf16 v[208:223], a[104:107], v[84:87], v[208:223]\n"
    "ds_read_b64_tr_b16 a[132:133], v16 offset:19200\n"
    "ds_read_b64_tr_b16 a[134:135], v17 offset:19200\n"
    "buffer_load_dwordx4 v[60:63], v25, s[60:63], s95 offen offset:1024 sc1\n"
    "buffer_load_dword a88, v15, s[60:63], s95 offen offset:2048 sc1\n"
    "v_mfma_f32_32x32x16_bf16 v[224:239], a[108:111], v[84:87], v[224:239]\n"
    "s_waitcnt lgkmcnt(8)\n"
    "s_add_u32 s28, s95, 2304\n"
    "ds_read_b64_tr_b16 a[136:137], v16 offset:19456\n"
    "ds_read_b64_tr_b16 a[138:139], v17 offset:19456\n"
    "v_mfma_f32_32x32x16_bf16 v[240:255], a[112:115], v[84:87], v[240:255]\n"
    "s_waitcnt vmcnt(12)\n"
    "ds_read_b64_tr_b16 a[140:141], v16 offset:20736\n"
    "ds_read_b64_tr_b16 a[142:143], v17 offset:20736\n"
    "v_cmp_ne_u32 vcc, v29, v13\n"
    "s_cmp_lg_u64 vcc, 0\n"
    "s_cbranch_scc1 L_slow6_%=\n"
    "L_ret6_%=:\n"
    "v_mfma_f32_32x32x16_bf16 v[112:127], a[116:119], v[84:87], v[112:127]\n"
    "s_waitcnt lgkmcnt(6)\n"
    "s_add_u32 s22, s84, 57344\n"
    "ds_read_b64_tr_b16 a[104:105], v16 offset:20992\n"
    "ds_read_b64_tr_b16 a[106:107], v17 offset:20992\n"
    "buffer_load_dwordx4 a[64:67], v25, s[60:63], s22 offen sc1\n"
    "v_mfma_f32_32x32x16_bf16 v[128:143], a[120:123], a[220:223], 0\n"
    "s_add_u32 s22, s84, 61440\n"
    "ds_read_b64_tr_b16 a[108:109], v16 offset:21248\n"
    "ds_read_b64_tr_b16 a[110:111], v17 offset:21248\n"
    "buffer_load_dwordx4 a[68:71], v25, s[60:63], s22 offen sc1\n"
    "v_mfma_f32_32x32x16_bf16 v[128:143], a[124:127], a[224:227], v[128:143]\n"
    "s_add_u32 s22, s84, 65536\n"
    "ds_read_b64_tr_b16 a[112:113], v16 offset:21760\n"
    "ds_read_b64_tr_b16 a[114:115], v17 offset:21760\n"
    "buffer_load_dwordx4 a[72:75], v25, s[60:63], s22 offen sc1\n"
    "v_mfma_f32_32x32x16_bf16 v[128:143], a[128:131], a[228:231], v[128:143]\n"
    "s_waitcnt lgkmcnt(8)\n"
    "s_add_u32 s22, s84, 69632\n"
    "ds_read_b64_tr_b16 a[116:117], v16 offset:22016\n"
    "ds_read_b64_tr_b16 a[118:119], v17 offset:22016\n"
    "buffer_load_dwordx4 a[76:79], v25, s[60:63], s22 offen sc1\n"
    "v_mfma_f32_32x32x16_bf16 v[128:143], a[132:135], a[232:235], v[128:143]\n"
    "s_add_u32 s22, s85, 57344\n"
    "ds_read_b64_tr_b16 a[120:121], v16 offset:22272\n"
    "ds_read_b64_tr_b16 a[122:123], v17 offset:22272\n"
    "v_mfma_f32_32x32x16_bf16 v[128:143], a[136:139], a[80:83], v[128:143]\n"
    "s_waitcnt lgkmcnt(6)\n"
    "ds_read_b64_tr_b16 a[124:125], v16 offset:22784\n"
    "ds_read_b64_tr_b16 a[126:127], v17 offset:22784\n"
    "buffer_load_dwordx4 a[0:3], v25, s[60:63], s22 offen sc1\n"
    "v_mfma_f32_32x32x16_bf16 v[208:223], a[140:143], a[40:43], v[208:223]\n"
    "s_add_u32 s22, s85, 61440\n"
    "ds_read_b64_tr_b16 a[128:129], v16 offset:23040\n"
    "ds_read_b64_tr_b16 a[130:131], v17 offset:23040\n"
    "v_mfma_f32_32x32x16_bf16 v[224:239], a[104:107], a[40:43], v[224:239]\n"
    "ds_read_b64_tr_b16 a[132:133], v16 offset:23296\n"
    "ds_read_b64_tr_b16 a[134:135], v17 offset:23296\n"
    "buffer_load_dwordx4 a[4:7], v25, s[60:63], s22 offen sc1\n"
    "v_cvt_pk_bf16_f32 v128, v128, v129\n"
    "v_cvt_pk_bf16_f32 v129, v130, v131\n"
    "v_mfma_f32_32x32x16_bf16 v[240:255], a[108:111], a[40:43], v[240:255]\n"
    "s_waitcnt lgkmcnt(6)\n"
    "s_barrier\n"
    "v_mfma_f32_32x32x16_bf16 v[208:223], a[112:115], a[44:47], v[208:223]\n"
    "s_add_u32 s20, s76, 52224\n"
    "ds_read_b64_tr_b16 a[136:137], v16 offset:23552\n"
    "ds_read_b64_tr_b16 a[138:139], v17 offset:23552\n"
    "ds_read_b64_tr_b16 a[140:141], v16 offset:23808\n"
    "ds_read_b64_tr_b16 a[142:143], v17 offset:23808\n"
    "buffer_load_dwordx4 v21, s[52:55], s20 offen offset:3072 lds\n"
    "v_mfma_f32_32x32x16_bf16 v[224:239], a[116:119], a[44:47], v[224:239]\n"
    "s_add_u32 s22, s85, 65536\n"
    "s_add_u32 s10, s10, s82\n"
    "s_and_b32 s10, s10, 7\n"
    "ds_read_b64_tr_b16 a[104:105], v16 offset:24064\n"
    "ds_read_b64_tr_b16 a[106:107], v17 offset:24064\n"
    "buffer_load_dwordx4 a[8:11], v25, s[60:63], s22 offen sc1\n"
    "v_mfma_f32_32x32x16_bf16 v[240:255], a[120:123], a[44:47], v[240:255]\n"
    "s_waitcnt lgkmcnt(6)\n"
    "s_add_u32 s22, s85, 69632\n"
    "ds_read_b64_tr_b16 a[108:109], v16 offset:24320\n"
    "ds_read_b64_tr_b16 a[110:111], v17 offset:24320\n"
    "buffer_load_dwordx4 a[12:15], v25, s[60:63], s22 offen sc1\n"
    "v_cvt_pk_bf16_f32 v130, v132, v133\n"
    "v_mfma_f32_32x32x16_bf16 v[208:223], a[124:127], a[48:51], v[208:223]\n"
    "s_add_u32 s22, s86, 57344\n"
    "s_mul_i32 s20, s10, 82944\n"
    "s_add_u32 s87, s72, s20\n"
    "s_add_u32 m0, s68, 9216\n"
    "ds_read_b64_tr_b16 a[112:113], v16 offset:12800\n"
    "ds_read_b64_tr_b16 a[114:115], v17 offset:12800\n"
    "v_cvt_pk_bf16_f32 v131, v134, v135\n"
    "v_mfma_f32_32x32x16_bf16 v[224:239], a[128:131], a[48:51], v[224:239]\n"
    "ds_read_b64_tr_b16 a[116:117], v16 offset:24832\n"
    "ds_read_b64_tr_b16 a[118:119], v17 offset:24832\n"
    "buffer_store_dwordx4 v[128:131], v22, s[60:63], s98 offen offset:2048 sc1\n"
    "v_cvt_pk_bf16_f32 v132, v136, v137\n"
    "v_mfma_f32_32x32x16_bf16 v[240:255], a[132:135], a[48:51], v[240:255]\n"
    "s_waitcnt lgkmcnt(6)\n"
    "ds_read_b64_tr_b16 a[120:121], v16 offset:25088\n"
    "ds_read_b64_tr_b16 a[122:123], v17 offset:25088\n"
    "v_cvt_pk_bf16_f32 v133, v138, v139\n"
    "v_cvt_pk_bf16_f32 v134, v140, v141\n"
    "v_cvt_pk_bf16_f32 v135, v142, v143\n"
    "v_mfma_f32_32x32x16_bf16 v[192:207], a[136:139], v[88:91], v[192:207]\n"
    "ds_read_b64_tr_b16 a[124:125], v16 offset:25344\n"
    "ds_read_b64_tr_b16 a[126:127], v17 offset:25344\n"
    "buffer_store_dwordx4 v[132:135], v22, s[60:63], s81 offen offset:2048 sc1\n"
    "v_mfma_f32_32x32x16_bf16 v[208:223], a[140:143], v[88:91], v[208:223]\n"
    "ds_read_b64_tr_b16 a[128:129], v16 offset:25856\n"
    "ds_read_b64_tr_b16 a[130:131], v17 offset:25856\n"
    "v_mfma_f32_32x32x16_bf16 v[224:239], a[104:107], v[88:91], v[224:239]\n"
    "s_waitcnt lgkmcnt(8)\n"
    "ds_read_b64_tr_b16 a[132:133], v16 offset:26112\n"
    "ds_read_b64_tr_b16 a[134:135], v17 offset:26112\n"
    "v_mfma_f32_32x32x16_bf16 v[240:255], a[108:111], v[88:91], v[240:255]\n"
    "ds_read_b64_tr_b16 a[136:137], v16 offset:26368\n"
    "ds_read_b64_tr_b16 a[138:139], v17 offset:26368\n"
    "v_mfma_f32_32x32x16_bf16 v[112:127], a[112:115], v[88:91], v[112:127]\n"
    "s_waitcnt lgkmcnt(6)\n"
    "ds_read_b64_tr_b16 a[140:141], v16 offset:26880\n"
    "ds_read_b64_tr_b16 a[142:143], v17 offset:26880\n"
    "v_mfma_f32_32x32x16_bf16 v[208:223], a[116:119], a[52:55], v[208:223]\n"
    "ds_read_b64_tr_b16 a[104:105], v16 offset:27136\n"
    "ds_read_b64_tr_b16 a[106:107], v17 offset:27136\n"
    "v_mfma_f32_32x32x16_bf16 v[224:239], a[120:123], a[52:55], v[224:239]\n"
    "ds_read_b64_tr_b16 a[108:109], v16 offset:27392\n"
    "ds_read_b64_tr_b16 a[110:111], v17 offset:27392\n"
    "buffer_load_dwordx4 a[16:19], v25, s[60:63], s22 offen sc1\n"
    "v_mfma_f32_32x32x16_bf16 v[240:255], a[124:127], a[52:55], v[240:255]\n"
    "s_waitcnt lgkmcnt(6)\n"
    "s_add_u32 s22, s86, 61440\n"
    "ds_read_b64_tr_b16 a[112:113], v16 offset:27648\n"
    "ds_read_b64_tr_b16 a[114:115], v17 offset:27648\n"
    "buffer_load_dwordx4 a[20:23], v25, s[60:63], s22 offen sc1\n"
    "v_mfma_f32_32x32x16_bf16 v[208:223], a[128:131], a[56:59], v[208:223]\n"
    "s_add_u32 s22, s86, 65536\n"
    "ds_read_b64_tr_b16 a[116:117], v16 offset:27904\n"
    "ds_read_b64_tr_b16 a[118:119], v17 offset:27904\n"
    "v_mfma_f32_32x32x16_bf16 v[224:239], a[132:135], a[56:59], v[224:239]\n"
    "ds_read_b64_tr_b16 a[120:121], v16 offset:28160\n"
    "ds_read_b64_tr_b16 a[122:123], v17 offset:28160\n"
    "buffer_load_dwordx4 a[24:27], v25, s[60:63], s22 offen sc1\n"
    "v_mfma_f32_32x32x16_bf16 v[240:255], a[136:139], a[56:59], v[240:255]\n"
    "s_waitcnt lgkmcnt(6)\n"
    "s_add_u32 s22, s86, 69632\n"
    "ds_read_b64_tr_b16 a[124:125], v16 offset:28416\n"
    "ds_read_b64_tr_b16 a[126:127], v17 offset:28416\n"
    "buffer_load_dwordx4 a[28:31], v25, s[60:63], s22 offen sc1\n"
    "v_mfma_f32_32x32x16_bf16 v[208:223], a[140:143], a[60:63], v[208:223]\n"
    "ds_read_b64_tr_b16 a[128:129], v16 offset:13056\n"
    "ds_read_b64_tr_b16 a[130:131], v17 offset:13056\n"
    "v_mfma_f32_32x32x16_bf16 v[224:239], a[104:107], a[60:63], v[224:239]\n"
    "ds_read_b64_tr_b16 a[132:133], v16 offset:29952\n"
    "ds_read_b64_tr_b16 a[134:135], v17 offset:29952\n"
    "buffer_load_dwordx4 v14, s[56:59], s87 offen lds\n"
    "v_mfma_f32_32x32x16_bf16 v[240:255], a[108:111], a[60:63], v[240:255]\n"
    "s_waitcnt lgkmcnt(6)\n"
    "s_barrier\n"
    "v_mfma_f32_32x32x16_bf16 v[192:207], a[112:115], v[92:95], v[192:207]\n"
    "s_add_u32 s10, s10, s82\n"
    "s_and_b32 s10, s10, 7\n"
    "s_mul_i32 s20, s10, 82944\n"
    "ds_read_b64_tr_b16 a[136:137], v16 offset:30208\n"
    "ds_read_b64_tr_b16 a[138:139], v17 offset:30208\n"
    "ds_read_b64_tr_b16 a[140:141], v16 offset:30464\n"
    "ds_read_b64_tr_b16 a[142:143], v17 offset:30464\n"
    "v_mfma_f32_32x32x16_bf16 v[208:223], a[116:119], v[92:95], v[208:223]\n"
    "s_add_u32 s87, s72, s20\n"
    "s_add_u32 m0, s68, 10240\n"
    "ds_read_b64_tr_b16 a[104:105], v16 offset:28672\n"
    "ds_read_b64_tr_b16 a[106:107], v17 offset:28672\n"
    "buffer_load_dwordx4 v14, s[56:59], s87 offen lds\n"
    "v_mfma_f32_32x32x16_bf16 v[224:239], a[120:123], v[92:95], v[224:239]\n"
    "s_waitcnt lgkmcnt(8)\n"
    "ds_read_b64_tr_b16 a[108:109], v16 offset:30976\n"
    "ds_read_b64_tr_b16 a[110:111], v17 offset:30976\n"
    "v_mfma_f32_32x32x16_bf16 v[240:255], a[124:127], v[92:95], v[240:255]\n"
    "s_add_u32 m0, s68, 11264\n"
    "ds_read_b64_tr_b16 a[112:113], v16 offset:31232\n"
    "ds_read_b64_tr_b16 a[114:115], v17 offset:31232\n"
    "buffer_load_dwordx4 v18, s[56:59], s88 offen lds\n"
    "v_mfma_f32_32x32x16_bf16 v[112:127], a[128:131], v[92:95], v[112:127]\n"
    "s_waitcnt vmcnt(17) lgkmcnt(4)\n"
    "ds_read_b64_tr_b16 a[116:117], v16 offset:31488\n"
    "ds_read_b64_tr_b16 a[118:119], v17 offset:31488\n"
    "v_mfma_f32_32x32x16_bf16 v[208:223], a[132:135], a[64:67], v[208:223]\n"
    "ds_read_b64_tr_b16 a[120:121], v16 offset:28928\n"
    "ds_read_b64_tr_b16 a[122:123], v17 offset:28928\n"
    "v_mfma_f32_32x32x16_bf16 v[224:239], a[136:139], a[64:67], v[224:239]\n"
    "ds_read_b64_tr_b16 a[124:125], v16 offset:32000\n"
    "ds_read_b64_tr_b16 a[126:127], v17 offset:32000\n"
    "buffer_load_dwordx4 v[64:67], v25, s[60:63], s28 offen sc1\n"
    "v_mfma_f32_32x32x16_bf16 v[240:255], a[140:143], a[64:67], v[240:255]\n"
    "ds_read_b64_tr_b16 a[128:129], v16 offset:32256\n"
    "ds_read_b64_tr_b16 a[130:131], v17 offset:32256\n"
    "v_mfma_f32_32x32x16_bf16 v[112:127], a[104:107], a[64:67], v[112:127]\n"
    "s_waitcnt vmcnt(17) lgkmcnt(4)\n"
    "ds_read_b64_tr_b16 a[132:133], v16 offset:32512\n"
    "ds_read_b64_tr_b16 a[134:135], v17 offset:32512\n"
    "buffer_load_dwordx4 v[68:71], v25, s[60:63], s28 offen offset:1024 sc1\n"
    "buffer_load_dword a89, v15, s[60:63], s28 offen offset:2048 sc1\n"
    "v_mfma_f32_32x32x16_bf16 v[208:223], a[108:111], a[68:71], v[208:223]\n"
    "s_add_u32 s94, s94, s82\n"
    "s_and_b32 s94, s94, 7\n"
    "s_mul_i32 s28, s94, 4608\n"
    "s_add_u32 s95, s97, s28\n"
    "ds_read_b64_tr_b16 a[136:137], v16 offset:29184\n"
    "ds_read_b64_tr_b16 a[138:139], v17 offset:29184\n"
    "v_mfma_f32_32x32x16_bf16 v[224:239], a[112:115], a[68:71], v[224:239]\n"
    "ds_read_b64_tr_b16 a[140:141], v16 offset:256\n"
    "ds_read_b64_tr_b16 a[142:143], v17 offset:256\n"
    "buffer_load_dwordx4 v[72:75], v25, s[60:63], s95 offen sc1\n"
    "v_mfma_f32_32x32x16_bf16 v[240:255], a[116:119], a[68:71], v[240:255]\n"
    "ds_read_b64_tr_b16 a[104:105], v16 offset:512\n"
    "ds_read_b64_tr_b16 a[106:107], v17 offset:512\n"
    "buffer_load_dwordx4 v[76:79], v25, s[60:63], s95 offen offset:1024 sc1\n"
    "buffer_load_dword a90, v15, s[60:63], s95 offen offset:2048 sc1\n"
    "v_mfma_f32_32x32x16_bf16 v[112:127], a[120:123], a[68:71], v[112:127]\n"
    "s_waitcnt vmcnt(21) lgkmcnt(4)\n"
    "ds_read_b64_tr_b16 a[108:109], v16 offset:768\n"
    "ds_read_b64_tr_b16 a[110:111], v17 offset:768\n"
    "v_mfma_f32_32x32x16_bf16 v[208:223], a[124:127], a[72:75], v[208:223]\n"
    "s_add_u32 s28, s95, 2304\n"
    "ds_read_b64_tr_b16 a[112:113], v16 offset:29440\n"
    "ds_read_b64_tr_b16 a[114:115], v17 offset:29440\n"
    "buffer_load_dwordx4 a[180:183], v25, s[60:63], s28 offen sc1\n"
    "v_mfma_f32_32x32x16_bf16 v[224:239], a[128:131], a[72:75], v[224:239]\n"
    "ds_read_b64_tr_b16 a[116:117], v16 offset:1280\n"
    "ds_read_b64_tr_b16 a[118:119], v17 offset:1280\n"
    "v_mfma_f32_32x32x16_bf16 v[240:255], a[132:135], a[72:75], v[240:255]\n"
    "ds_read_b64_tr_b16 a[120:121], v16 offset:1536\n"
    "ds_read_b64_tr_b16 a[122:123], v17 offset:1536\n"
    "buffer_load_dwordx4 a[184:187], v25, s[60:63], s28 offen offset:1024 sc1\n"
    "buffer_load_dword a91, v15, s[60:63], s28 offen offset:2048 sc1\n"
    "v_mfma_f32_32x32x16_bf16 v[112:127], a[136:139], a[72:75], v[112:127]\n"
    "s_waitcnt vmcnt(23) lgkmcnt(4)\n"
    "s_add_u32 s94, s94, s82\n"
    "s_and_b32 s94, s94, 7\n"
    "s_mul_i32 s28, s94, 4608\n"
    "s_add_u32 s95, s97, s28\n"
    "ds_read_b64_tr_b16 a[124:125], v16 offset:1792\n"
    "ds_read_b64_tr_b16 a[126:127], v17 offset:1792\n"
    "v_mfma_f32_32x32x16_bf16 v[208:223], a[140:143], a[76:79], v[208:223]\n"
    "ds_read_b64_tr_b16 a[128:129], v16 offset:2304\n"
    "ds_read_b64_tr_b16 a[130:131], v17 offset:2304\n"
    "buffer_load_dwordx4 a[188:191], v25, s[60:63], s95 offen sc1\n"
    "v_mfma_f32_32x32x16_bf16 v[224:239], a[104:107], a[76:79], v[224:239]\n"
    "ds_read_b64_tr_b16 a[132:133], v16 offset:2560\n"
    "ds_read_b64_tr_b16 a[134:135], v17 offset:2560\n"
    "v_mfma_f32_32x32x16_bf16 v[240:255], a[108:111], a[76:79], v[240:255]\n"
    "s_waitcnt vmcnt(21)\n"
    "s_barrier\n"
    "s_add_u32 m0, s68, 12288\n"
    "v_mfma_f32_32x32x16_bf16 v[112:127], a[112:115], a[76:79], v[112:127]\n"
    "s_waitcnt lgkmcnt(4)\n"
    "ds_read_b64_tr_b16 a[136:137], v16 offset:2816\n"
    "ds_read_b64_tr_b16 a[138:139], v17 offset:2816\n"
    "ds_read_b64_tr_b16 a[140:141], v16 offset:3328\n"
    "ds_read_b64_tr_b16 a[142:143], v17 offset:3328\n"
    "buffer_load_dwordx4 v19, s[40:43], s4 offen lds\n"
    "v_mfma_f32_32x32x16_bf16 v[208:223], a[116:119], a[0:3], v[208:223]\n"
    "s_add_u32 s20, s4, 17408\n"
    "ds_read_b64_tr_b16 a[104:105], v16 offset:3584\n"
    "ds_read_b64_tr_b16 a[106:107], v17 offset:3584\n"
    "v_mfma_f32_32x32x16_bf16 v[224:239], a[120:123], a[0:3], v[224:239]\n"
    "ds_read_b64_tr_b16 a[108:109], v16 offset:3840\n"
    "ds_read_b64_tr_b16 a[110:111], v17 offset:3840\n"
    "buffer_load_dwordx4 v19, s[40:43], s20 offen offset:1024 lds\n"
    "v_cvt_pk_bf16_f32 v112, v112, v113\n"
    "v_cvt_pk_bf16_f32 v113, v114, v115\n"
    "v_mfma_f32_32x32x16_bf16 v[240:255], a[124:127], a[0:3], v[240:255]\n"
    "s_waitcnt lgkmcnt(6)\n"
    "ds_read_b64_tr_b16 a[112:113], v16 offset:4352\n"
    "ds_read_b64_tr_b16 a[114:115], v17 offset:4352\n"
    "v_cvt_pk_bf16_f32 v114, v116, v117\n"
    "v_cvt_pk_bf16_f32 v115, v118, v119\n"
    "v_mfma_f32_32x32x16_bf16 v[208:223], a[128:131], a[4:7], v[208:223]\n"
    "ds_read_b64_tr_b16 a[116:117], v16 offset:4608\n"
    "ds_read_b64_tr_b16 a[118:119], v17 offset:4608\n"
    "buffer_store_dwordx4 v[112:115], v22, s[60:63], s98 offen sc1\n"
    "v_cvt_pk_bf16_f32 v116, v120, v121\n"
    "v_mfma_f32_32x32x16_bf16 v[224:239], a[132:135], a[4:7], v[224:239]\n"
    "ds_read_b64_tr_b16 a[120:121], v16 offset:4864\n"
    "ds_read_b64_tr_b16 a[122:123], v17 offset:4864\n"
    "v_cvt_pk_bf16_f32 v117, v122, v123\n"
    "v_cvt_pk_bf16_f32 v118, v124, v125\n"
    "v_cvt_pk_bf16_f32 v119, v126, v127\n"
    "v_mfma_f32_32x32x16_bf16 v[240:255], a[136:139], a[4:7], v[240:255]\n"
    "s_waitcnt vmcnt(23) lgkmcnt(6)\n"
    "ds_read_b64_tr_b16 a[124:125], v16 offset:5376\n"
    "ds_read_b64_tr_b16 a[126:127], v17 offset:5376\n"
    "buffer_store_dwordx4 v[116:119], v22, s[60:63], s81 offen sc1\n"
    "v_mfma_f32_32x32x16_bf16 v[208:223], a[140:143], a[8:11], v[208:223]\n"
    "ds_read_b64_tr_b16 a[128:129], v16 offset:5632\n"
    "ds_read_b64_tr_b16 a[130:131], v17 offset:5632\n"
    "v_mfma_f32_32x32x16_bf16 v[224:239], a[104:107], a[8:11], v[224:239]\n"
    "ds_read_b64_tr_b16 a[132:133], v16 offset:5888\n"
    "ds_read_b64_tr_b16 a[134:135], v17 offset:5888\n"
    "v_mfma_f32_32x32x16_bf16 v[240:255], a[108:111], a[8:11], v[240:255]\n"
    "s_waitcnt vmcnt(23) lgkmcnt(6)\n"
    "ds_read_b64_tr_b16 a[136:137], v16 offset:6400\n"
    "ds_read_b64_tr_b16 a[138:139], v17 offset:6400\n"
    "v_mfma_f32_32x32x16_bf16 v[208:223], a[112:115], a[12:15], v[208:223]\n"
    "ds_read_b64_tr_b16 a[140:141], v16 offset:6656\n"
    "ds_read_b64_tr_b16 a[142:143], v17 offset:6656\n"
    "v_mfma_f32_32x32x16_bf16 v[224:239], a[116:119], a[12:15], v[224:239]\n"
    "ds_read_b64_tr_b16 a[104:105], v16 offset:6912\n"
    "ds_read_b64_tr_b16 a[106:107], v17 offset:6912\n"
    "v_mfma_f32_32x32x16_bf16 v[240:255], a[120:123], a[12:15], v[240:255]\n"
    "s_waitcnt vmcnt(20) lgkmcnt(6)\n"
    "ds_read_b64_tr_b16 a[108:109], v16 offset:7424\n"
    "ds_read_b64_tr_b16 a[110:111], v17 offset:7424\n"
    "buffer_load_dwordx4 a[192:195], v25, s[60:63], s95 offen offset:1024 sc1\n"
    "buffer_load_dword a92, v15, s[60:63], s95 offen offset:2048 sc1\n"
    "v_mfma_f32_32x32x16_bf16 v[208:223], a[124:127], a[16:19], v[208:223]\n"
    "ds_read_b64_tr_b16 a[112:113], v16 offset:7680\n"
    "ds_read_b64_tr_b16 a[114:115], v17 offset:7680\n"
    "v_mfma_f32_32x32x16_bf16 v[224:239], a[128:131], a[16:19], v[224:239]\n"
    "s_add_u32 s28, s95, 2304\n"
    "ds_read_b64_tr_b16 a[116:117], v16 offset:7936\n"
    "ds_read_b64_tr_b16 a[118:119], v17 offset:7936\n"
    "buffer_load_dwordx4 v[96:99], v25, s[60:63], s28 offen sc1\n"
    "v_mfma_f32_32x32x16_bf16 v[240:255], a[132:135], a[16:19], v[240:255]\n"
    "s_waitcnt vmcnt(22) lgkmcnt(6)\n"
    "ds_read_b64_tr_b16 a[120:121], v16 offset:8448\n"
    "ds_read_b64_tr_b16 a[122:123], v17 offset:8448\n"
    "v_mfma_f32_32x32x16_bf16 v[208:223], a[136:139], a[20:23], v[208:223]\n"
    "ds_read_b64_tr_b16 a[124:125], v16 offset:8704\n"
    "ds_read_b64_tr_b16 a[126:127], v17 offset:8704\n"
    "buffer_load_dwordx4 v[100:103], v25, s[60:63], s28 offen offset:1024 sc1\n"
    "buffer_load_dword a93, v15, s[60:63], s28 offen offset:2048 sc1\n"
    "v_mfma_f32_32x32x16_bf16 v[224:239], a[140:143], a[20:23], v[224:239]\n"
    "s_add_u32 s94, s94, s82\n"
    "s_and_b32 s94, s94, 7\n"
    "s_mul_i32 s28, s94, 4608\n"
    "s_add_u32 s95, s97, s28\n"
    "ds_read_b64_tr_b16 a[128:129], v16 offset:8960\n"
    "ds_read_b64_tr_b16 a[130:131], v17 offset:8960\n"
    "v_mfma_f32_32x32x16_bf16 v[240:255], a[104:107], a[20:23], v[240:255]\n"
    "s_waitcnt vmcnt(23) lgkmcnt(6)\n"
    "ds_read_b64_tr_b16 a[132:133], v16 offset:15360\n"
    "ds_read_b64_tr_b16 a[134:135], v17 offset:15360\n"
    "buffer_load_dwordx4 v[104:107], v25, s[60:63], s95 offen sc1\n"
    "v_mfma_f32_32x32x16_bf16 v[208:223], a[108:111], a[24:27], v[208:223]\n"
    "s_barrier\n"
    "v_mfma_f32_32x32x16_bf16 v[224:239], a[112:115], a[24:27], v[224:239]\n"
    "ds_read_b64_tr_b16 a[136:137], v16 offset:16384\n"
    "ds_read_b64_tr_b16 a[138:139], v17 offset:16384\n"
    "ds_read_b64_tr_b16 a[140:141], v16 offset:20480\n"
    "ds_read_b64_tr_b16 a[142:143], v17 offset:20480\n"
    "buffer_load_dwordx4 v[108:111], v25, s[60:63], s95 offen offset:1024 sc1\n"
    "v_mfma_f32_32x32x16_bf16 v[240:255], a[116:119], a[24:27], v[240:255]\n"
    "s_waitcnt vmcnt(24) lgkmcnt(6)\n"
    "ds_read_b64_tr_b16 a[104:105], v16 offset:21504\n"
    "ds_read_b64_tr_b16 a[106:107], v17 offset:21504\n"
    "buffer_load_dword a94, v15, s[60:63], s95 offen offset:2048 sc1\n"
    "v_mfma_f32_32x32x16_bf16 v[208:223], a[120:123], a[28:31], v[208:223]\n"
    "s_add_u32 s28, s95, 2304\n"
    "ds_read_b64_tr_b16 a[108:109], v16 offset:22528\n"
    "ds_read_b64_tr_b16 a[110:111], v17 offset:22528\n"
    "buffer_load_dwordx4 a[204:207], v25, s[60:63], s28 offen sc1\n"
    "v_mfma_f32_32x32x16_bf16 v[224:239], a[124:127], a[28:31], v[224:239]\n"
    "ds_read_b64_tr_b16 a[112:113], v16 offset:24576\n"
    "ds_read_b64_tr_b16 a[114:115], v17 offset:24576\n"
    "buffer_load_dwordx4 a[208:211], v25, s[60:63], s28 offen offset:1024 sc1\n"
    "buffer_load_dword a95, v15, s[60:63], s28 offen offset:2048 sc1\n"
    "v_mfma_f32_32x32x16_bf16 v[240:255], a[128:131], a[28:31], v[240:255]\n"
    "s_waitcnt lgkmcnt(4)\n"
    "ds_read_b64_tr_b16 a[116:117], v16 offset:25600\n"
    "ds_read_b64_tr_b16 a[118:119], v17 offset:25600\n"
    "v_mfma_f32_32x32x16_bf16 v[192:207], a[132:135], a[32:35], v[192:207]\n"
    "s_waitcnt vmcnt(11)\n"
    "ds_read_b64_tr_b16 a[120:121], v16 offset:26624\n"
    "ds_read_b64_tr_b16 a[122:123], v17 offset:26624\n"
    "buffer_store_dword v29, v26, s[60:63], s77 offen sc1\n"
    "v_mfma_f32_32x32x16_bf16 v[192:207], a[136:139], a[36:39], v[192:207]\n"
    "ds_read_b64_tr_b16 a[124:125], v16 offset:29696\n"
    "ds_read_b64_tr_b16 a[126:127], v17 offset:29696\n"
    "v_cvt_pk_bf16_f32 v208, v208, v209\n"
    "v_cvt_pk_bf16_f32 v209, v210, v211\n"
    "v_cvt_pk_bf16_f32 v210, v212, v213\n"
    "v_cvt_pk_bf16_f32 v211, v214, v215\n"
    "v_mfma_f32_32x32x16_bf16 v[192:207], a[140:143], a[40:43], v[192:207]\n"
    "ds_read_b64_tr_b16 a[128:129], v16 offset:30720\n"
    "ds_read_b64_tr_b16 a[130:131], v17 offset:30720\n"
    "buffer_store_dwordx4 v[208:211], v25, s[60:63], s91 offen sc1\n"
    "v_cvt_pk_bf16_f32 v212, v216, v217\n"
    "v_mfma_f32_32x32x16_bf16 v[192:207], a[104:107], a[44:47], v[192:207]\n"
    "s_waitcnt lgkmcnt(4)\n"
    "ds_read_b64_tr_b16 a[132:133], v16 offset:31744\n"
    "ds_read_b64_tr_b16 a[134:135], v17 offset:31744\n"
    "v_cvt_pk_bf16_f32 v213, v218, v219\n"
    "v_cvt_pk_bf16_f32 v214, v220, v221\n"
    "v_cvt_pk_bf16_f32 v215, v222, v223\n"
    "v_mfma_f32_32x32x16_bf16 v[192:207], a[108:111], a[48:51], v[192:207]\n"
    "ds_read_b64_tr_b16 a[136:137], v16\n"
    "ds_read_b64_tr_b16 a[138:139], v17\n"
    "buffer_store_dwordx4 v[212:215], v25, s[60:63], s91 offen offset:1024 sc1\n"
    "v_cvt_pk_bf16_f32 v216, v224, v225\n"
    "v_mfma_f32_32x32x16_bf16 v[192:207], a[112:115], a[52:55], v[192:207]\n"
    "ds_read_b64_tr_b16 a[140:141], v16 offset:1024\n"
    "ds_read_b64_tr_b16 a[142:143], v17 offset:1024\n"
    "v_cvt_pk_bf16_f32 v217, v226, v227\n"
    "v_cvt_pk_bf16_f32 v218, v228, v229\n"
    "v_cvt_pk_bf16_f32 v219, v230, v231\n"
    "v_mfma_f32_32x32x16_bf16 v[192:207], a[116:119], a[56:59], v[192:207]\n"
    "ds_read_b64_tr_b16 a[104:105], v16 offset:2048\n"
    "ds_read_b64_tr_b16 a[106:107], v17 offset:2048\n"
    "buffer_store_dwordx4 v[216:219], v25, s[60:63], s92 offen sc1\n"
    "v_cvt_pk_bf16_f32 v220, v232, v233\n"
    "v_mfma_f32_32x32x16_bf16 v[192:207], a[120:123], a[60:63], v[192:207]\n"
    "s_waitcnt lgkmcnt(4)\n"
    "ds_read_b64_tr_b16 a[108:109], v16 offset:3072\n"
    "ds_read_b64_tr_b16 a[110:111], v17 offset:3072\n"
    "v_cvt_pk_bf16_f32 v221, v234, v235\n"
    "v_cvt_pk_bf16_f32 v222, v236, v237\n"
    "v_cvt_pk_bf16_f32 v223, v238, v239\n"
    "v_mfma_f32_32x32x16_bf16 v[192:207], a[124:127], a[64:67], v[192:207]\n"
    "ds_read_b64_tr_b16 a[112:113], v16 offset:4096\n"
    "ds_read_b64_tr_b16 a[114:115], v17 offset:4096\n"
    "buffer_store_dwordx4 v[220:223], v25, s[60:63], s92 offen offset:1024 sc1\n"
    "v_cvt_pk_bf16_f32 v224, v240, v241\n"
    "v_mfma_f32_32x32x16_bf16 v[192:207], a[128:131], a[68:71], v[192:207]\n"
    "ds_read_b64_tr_b16 a[116:117], v16 offset:5120\n"
    "ds_read_b64_tr_b16 a[118:119], v17 offset:5120\n"
    "v_cvt_pk_bf16_f32 v225, v242, v243\n"
    "v_cvt_pk_bf16_f32 v226, v244, v245\n"
    "v_cvt_pk_bf16_f32 v227, v246, v247\n"
    "v_mfma_f32_32x32x16_bf16 v[192:207], a[132:135], a[72:75], v[192:207]\n"
    "ds_read_b64_tr_b16 a[120:121], v16 offset:6144\n"
    "ds_read_b64_tr_b16 a[122:123], v17 offset:6144\n"
    "buffer_store_dwordx4 v[224:227], v25, s[60:63], s93 offen sc1\n"
    "v_cvt_pk_bf16_f32 v228, v248, v249\n"
    "v_mfma_f32_32x32x16_bf16 v[192:207], a[136:139], a[76:79], v[192:207]\n"
    "s_waitcnt lgkmcnt(4)\n"
    "ds_read_b64_tr_b16 a[124:125], v16 offset:7168\n"
    "ds_read_b64_tr_b16 a[126:127], v17 offset:7168\n"
    "v_cvt_pk_bf16_f32 v229, v250, v251\n"
    "v_cvt_pk_bf16_f32 v230, v252, v253\n"
    "v_cvt_pk_bf16_f32 v231, v254, v255\n"
    "v_mfma_f32_32x32x16_bf16 v[192:207], a[140:143], a[0:3], v[192:207]\n"
    "ds_read_b64_tr_b16 a[128:129], v16 offset:8192\n"
    "ds_read_b64_tr_b16 a[130:131], v17 offset:8192\n"
    "buffer_store_dwordx4 v[228:231], v25, s[60:63], s93 offen offset:1024 sc1\n"
    "v_mfma_f32_32x32x16_bf16 v[192:207], a[104:107], a[4:7], v[192:207]\n"
    "ds_read_b64_tr_b16 a[132:133], v16 offset:32768\n"
    "ds_read_b64_tr_b16 a[134:135], v17 offset:32768\n"
    "v_mfma_f32_32x32x16_bf16 v[192:207], a[108:111], a[8:11], v[192:207]\n"
    "s_barrier\n"
    "v_mfma_f32_32x32x16_bf16 v[192:207], a[112:115], a[12:15], v[192:207]\n"
    "s_waitcnt lgkmcnt(2)\n"
    "ds_read_b64_tr_b16 a[136:137], v16 offset:33024\n"
    "ds_read_b64_tr_b16 a[138:139], v17 offset:33024\n"
    "ds_read_b64_tr_b16 a[140:141], v16 offset:33280\n"
    "ds_read_b64_tr_b16 a[142:143], v17 offset:33280\n"
    "v_mfma_f32_32x32x16_bf16 v[192:207], a[116:119], a[16:19], v[192:207]\n"
    "ds_read_b64_tr_b16 a[104:105], v16 offset:33536\n"
    "ds_read_b64_tr_b16 a[106:107], v17 offset:33536\n"
    "v_mfma_f32_32x32x16_bf16 v[192:207], a[120:123], a[20:23], v[192:207]\n"
    "ds_read_b64_tr_b16 a[108:109], v16 offset:33792\n"
    "ds_read_b64_tr_b16 a[110:111], v17 offset:33792\n"
    "v_mfma_f32_32x32x16_bf16 v[192:207], a[124:127], a[24:27], v[192:207]\n"
    "ds_read_b64_tr_b16 a[112:113], v16 offset:34048\n"
    "ds_read_b64_tr_b16 a[114:115], v17 offset:34048\n"
    "v_mfma_f32_32x32x16_bf16 v[192:207], a[128:131], a[28:31], v[192:207]\n"
    "s_waitcnt lgkmcnt(4)\n"
    "ds_read_b64_tr_b16 a[116:117], v16 offset:34304\n"
    "ds_read_b64_tr_b16 a[118:119], v17 offset:34304\n"
    "v_mfma_f32_32x32x16_bf16 v[176:191], a[132:135], a[220:223], 0\n"
    "ds_read_b64_tr_b16 a[120:121], v16 offset:34560\n"
    "ds_read_b64_tr_b16 a[122:123], v17 offset:34560\n"
    "v_mfma_f32_32x32x16_bf16 v[176:191], a[136:139], a[224:227], v[176:191]\n"
    "ds_read_b64_tr_b16 a[124:125], v16 offset:34816\n"
    "ds_read_b64_tr_b16 a[126:127], v17 offset:34816\n"
    "v_mfma_f32_32x32x16_bf16 v[176:191], a[140:143], a[228:231], v[176:191]\n"
    "ds_read_b64_tr_b16 a[128:129], v16 offset:35072\n"
    "ds_read_b64_tr_b16 a[130:131], v17 offset:35072\n"
    "v_mfma_f32_32x32x16_bf16 v[176:191], a[104:107], a[232:235], v[176:191]\n"
    "s_waitcnt lgkmcnt(4)\n"
    "ds_read_b64_tr_b16 a[132:133], v16 offset:35328\n"
    "ds_read_b64_tr_b16 a[134:135], v17 offset:35328\n"
    "v_mfma_f32_32x32x16_bf16 v[176:191], a[108:111], a[236:239], v[176:191]\n"
    "ds_read_b64_tr_b16 a[136:137], v16 offset:35584\n"
    "ds_read_b64_tr_b16 a[138:139], v17 offset:35584\n"
    "v_mfma_f32_32x32x16_bf16 v[176:191], a[112:115], a[240:243], v[176:191]\n"
    "ds_read_b64_tr_b16 a[140:141], v16 offset:35840\n"
    "ds_read_b64_tr_b16 a[142:143], v17 offset:35840\n"
    "v_mfma_f32_32x32x16_bf16 v[176:191], a[116:119], a[244:247], v[176:191]\n"
    "ds_read_b64_tr_b16 a[104:105], v16 offset:36096\n"
    "ds_read_b64_tr_b16 a[106:107], v17 offset:36096\n"
    "v_mfma_f32_32x32x16_bf16 v[176:191], a[120:123], a[248:251], v[176:191]\n"
    "s_waitcnt lgkmcnt(4)\n"
    "ds_read_b64_tr_b16 a[108:109], v16 offset:36352\n"
    "ds_read_b64_tr_b16 a[110:111], v17 offset:36352\n"
    "v_mfma_f32_32x32x16_bf16 v[176:191], a[124:127], a[196:199], v[176:191]\n"
    "ds_read_b64_tr_b16 a[112:113], v16 offset:36608\n"
    "ds_read_b64_tr_b16 a[114:115], v17 offset:36608\n"
    "v_mfma_f32_32x32x16_bf16 v[176:191], a[128:131], a[200:203], v[176:191]\n"
    "ds_read_b64_tr_b16 a[116:117], v16 offset:36864\n"
    "ds_read_b64_tr_b16 a[118:119], v17 offset:36864\n"
    "v_mfma_f32_32x32x16_bf16 v[176:191], a[132:135], a[164:167], v[176:191]\n"
    "ds_read_b64_tr_b16 a[120:121], v16 offset:37120\n"
    "ds_read_b64_tr_b16 a[122:123], v17 offset:37120\n"
    "v_mfma_f32_32x32x16_bf16 v[176:191], a[136:139], a[168:171], v[176:191]\n"
    "s_waitcnt lgkmcnt(4)\n"
    "ds_read_b64_tr_b16 a[124:125], v16 offset:37376\n"
    "ds_read_b64_tr_b16 a[126:127], v17 offset:37376\n"
    "v_mfma_f32_32x32x16_bf16 v[176:191], a[140:143], a[172:175], v[176:191]\n"
    "ds_read_b64_tr_b16 a[128:129], v16 offset:37632\n"
    "ds_read_b64_tr_b16 a[130:131], v17 offset:37632\n"
    "v_mfma_f32_32x32x16_bf16 v[176:191], a[104:107], a[176:179], v[176:191]\n"
    "ds_read_b64_tr_b16 a[132:133], v16 offset:37888\n"
    "ds_read_b64_tr_b16 a[134:135], v17 offset:37888\n"
    "v_mfma_f32_32x32x16_bf16 v[176:191], a[108:111], v[48:51], v[176:191]\n"
    "s_barrier\n"
    "v_mfma_f32_32x32x16_bf16 v[176:191], a[112:115], v[52:55], v[176:191]\n"
    "s_waitcnt lgkmcnt(2)\n"
    "ds_read_b64_tr_b16 a[136:137], v16 offset:38144\n"
    "ds_read_b64_tr_b16 a[138:139], v17 offset:38144\n"
    "ds_read_b64_tr_b16 a[140:141], v16 offset:38400\n"
    "ds_read_b64_tr_b16 a[142:143], v17 offset:38400\n"
    "v_mfma_f32_32x32x16_bf16 v[176:191], a[116:119], v[56:59], v[176:191]\n"
    "ds_read_b64_tr_b16 a[104:105], v16 offset:38656\n"
    "ds_read_b64_tr_b16 a[106:107], v17 offset:38656\n"
    "v_mfma_f32_32x32x16_bf16 v[176:191], a[120:123], v[60:63], v[176:191]\n"
    "s_waitcnt vmcnt(0)\n"
    "ds_read_b64_tr_b16 a[108:109], v16 offset:9216\n"
    "ds_read_b64_tr_b16 a[110:111], v17 offset:9216\n"
    "buffer_store_dword v29, v26, s[60:63], s78 offen sc1\n"
    "v_mfma_f32_32x32x16_bf16 v[176:191], a[124:127], v[64:67], v[176:191]\n"
    "ds_read_b64_tr_b16 a[112:113], v16 offset:9472\n"
    "ds_read_b64_tr_b16 a[114:115], v17 offset:9472\n"
    "v_mfma_f32_32x32x16_bf16 v[176:191], a[128:131], v[68:71], v[176:191]\n"
    "s_waitcnt lgkmcnt(4)\n"
    "ds_read_b64_tr_b16 a[116:117], v16 offset:9728\n"
    "ds_read_b64_tr_b16 a[118:119], v17 offset:9728\n"
    "v_mfma_f32_32x32x16_bf16 v[176:191], a[132:135], v[72:75], v[176:191]\n"
    "ds_read_b64_tr_b16 a[120:121], v16 offset:9984\n"
    "ds_read_b64_tr_b16 a[122:123], v17 offset:9984\n"
    "v_mfma_f32_32x32x16_bf16 v[176:191], a[136:139], v[76:79], v[176:191]\n"
    "ds_read_b64_tr_b16 a[124:125], v16 offset:10240\n"
    "ds_read_b64_tr_b16 a[126:127], v17 offset:10240\n"
    "v_mfma_f32_32x32x16_bf16 v[176:191], a[140:143], a[180:183], v[176:191]\n"
    "ds_read_b64_tr_b16 a[128:129], v16 offset:10496\n"
    "ds_read_b64_tr_b16 a[130:131], v17 offset:10496\n"
    "buffer_load_dword v42, v43, s[60:63], s79 offen sc1\n"
    "v_mfma_f32_32x32x16_bf16 v[176:191], a[104:107], a[184:187], v[176:191]\n"
    "s_waitcnt lgkmcnt(4)\n"
    "ds_read_b64_tr_b16 a[132:133], v16 offset:10752\n"
    "ds_read_b64_tr_b16 a[134:135], v17 offset:10752\n"
    "v_mfma_f32_32x32x16_bf16 v[176:191], a[108:111], a[188:191], v[176:191]\n"
    "ds_read_b64_tr_b16 a[136:137], v16 offset:11008\n"
    "ds_read_b64_tr_b16 a[138:139], v17 offset:11008\n"
    "v_mfma_f32_32x32x16_bf16 v[176:191], a[112:115], a[192:195], v[176:191]\n"
    "ds_read_b64_tr_b16 a[140:141], v16 offset:11264\n"
    "ds_read_b64_tr_b16 a[142:143], v17 offset:11264\n"
    "v_mfma_f32_32x32x16_bf16 v[176:191], a[116:119], v[96:99], v[176:191]\n"
    "ds_read_b64_tr_b16 a[104:105], v16 offset:11520\n"
    "ds_read_b64_tr_b16 a[106:107], v17 offset:11520\n"
    "v_mfma_f32_32x32x16_bf16 v[176:191], a[120:123], v[100:103], v[176:191]\n"
    "s_waitcnt lgkmcnt(4)\n"
    "ds_read_b64_tr_b16 a[108:109], v16 offset:11776\n"
    "ds_read_b64_tr_b16 a[110:111], v17 offset:11776\n"
    "v_mfma_f32_32x32x16_bf16 v[176:191], a[124:127], v[104:107], v[176:191]\n"
    "ds_read_b64_tr_b16 a[112:113], v16 offset:12032\n"
    "ds_read_b64_tr_b16 a[114:115], v17 offset:12032\n"
    "v_mfma_f32_32x32x16_bf16 v[176:191], a[128:131], v[108:111], v[176:191]\n"
    "v_mfma_f32_32x32x16_bf16 v[176:191], a[132:135], a[204:207], v[176:191]\n"
    "v_mfma_f32_32x32x16_bf16 v[176:191], a[136:139], a[208:211], v[176:191]\n"
    "s_waitcnt lgkmcnt(0)\n"
    "v_mfma_f32_32x32x16_bf16 v[176:191], a[140:143], a[80:83], v[176:191]\n"
    "buffer_load_dword v13, v31, s[60:63], s80 offen sc1\n"
    "v_mfma_f32_32x32x16_bf16 v[176:191], a[104:107], a[84:87], v[176:191]\n"
    "v_mfma_f32_32x32x16_bf16 v[176:191], a[108:111], a[88:91], v[176:191]\n"
    "v_mfma_f32_32x32x16_bf16 v[176:191], a[112:115], a[92:95], v[176:191]\n"
    "s_waitcnt vmcnt(1)\n"
    "v_cmp_ne_u32 vcc, v29, v42\n"
    "s_cmp_lg_u64 vcc, 0\n"
    "s_cbranch_scc1 L_slow8_%=\n"
    "L_ret8_%=:\n"
    "buffer_load_dword v144, v15, s[60:63], s99 offen sc1\n"
    "buffer_load_dword v145, v15, s[60:63], s99 offen offset:256 sc1\n"
    "buffer_load_dword v146, v15, s[60:63], s99 offen offset:512 sc1\n"
    "buffer_load_dword v147, v15, s[60:63], s99 offen offset:768 sc1\n"
    "buffer_load_dword v148, v15, s[60:63], s99 offen offset:1024 sc1\n"
    "buffer_load_dword v149, v15, s[60:63], s99 offen offset:1280 sc1\n"
    "buffer_load_dword v150, v15, s[60:63], s99 offen offset:1536 sc1\n"
    "buffer_load_dword v151, v15, s[60:63], s99 offen offset:1792 sc1\n"
    "buffer_load_dword v152, v15, s[60:63], s99 offen offset:2048 sc1\n"
    "buffer_load_dword v153, v15, s[60:63], s99 offen offset:2304 sc1\n"
    "buffer_load_dword v154, v15, s[60:63], s99 offen offset:2560 sc1\n"
    "buffer_load_dword v155, v15, s[60:63], s99 offen offset:2816 sc1\n"
    "buffer_load_dword v156, v15, s[60:63], s99 offen offset:3072 sc1\n"
    "buffer_load_dword v157, v15, s[60:63], s99 offen offset:3328 sc1\n"
    "buffer_load_dword v158, v15, s[60:63], s99 offen offset:3584 sc1\n"
    "buffer_load_dword v159, v15, s[60:63], s99 offen offset:3840 sc1\n"
    "v_mov_b32 v32, 16256\n"
    "v_mov_b32 v33, 1065353216\n"
    "v_mov_b32 v34, 1056308720\n"
    "v_mov_b32 v35, 1056308720\n"
    "v_accvgpr_read_b32 v38, a163\n"
    "ds_read_b64 v[88:89], v38 offset:12288\n"
    "ds_read_b64 v[90:91], v38 offset:12304\n"
    "ds_read_b64 v[92:93], v38 offset:12320\n"
    "ds_read_b64 v[94:95], v38 offset:12336\n"
    "v_accvgpr_read_b32 v168, a100\n"
    "s_waitcnt lgkmcnt(3)\n"
    "v_lshlrev_b32 v232, 16, v88\n"
    "v_and_b32 v233, s25, v88\n"
    "v_lshlrev_b32 v234, 16, v89\n"
    "v_and_b32 v235, s25, v89\n"
    "s_waitcnt lgkmcnt(2)\n"
    "v_lshlrev_b32 v236, 16, v90\n"
    "v_and_b32 v237, s25, v90\n"
    "v_lshlrev_b32 v238, 16, v91\n"
    "v_and_b32 v239, s25, v91\n"
    "s_waitcnt lgkmcnt(1)\n"
    "v_lshlrev_b32 v240, 16, v92\n"
    "v_and_b32 v241, s25, v92\n"
    "v_lshlrev_b32 v242, 16, v93\n"
    "v_and_b32 v243, s25, v93\n"
    "s_waitcnt lgkmcnt(0)\n"
    "v_lshlrev_b32 v244, 16, v94\n"
    "v_and_b32 v245, s25, v94\n"
    "v_lshlrev_b32 v246, 16, v95\n"
    "v_and_b32 v247, s25, v95\n"
    "v_lshlrev_b32 v248, 16, v168\n"
    "v_and_b32 v249, s25, v168\n"
    "v_pk_mul_f32 v[176:177], v[176:177], v[34:35]\n"
    "v_pk_mul_f32 v[178:179], v[178:179], v[34:35]\n"
    "v_pk_mul_f32 v[180:181], v[180:181], v[34:35]\n"
    "v_pk_mul_f32 v[182:183], v[182:183], v[34:35]\n"
    "v_pk_mul_f32 v[184:185], v[184:185], v[34:35]\n"
    "v_pk_mul_f32 v[186:187], v[186:187], v[34:35]\n"
    "v_pk_mul_f32 v[188:189], v[188:189], v[34:35]\n"
    "v_pk_mul_f32 v[190:191], v[190:191], v[34:35]\n"
    "s_waitcnt vmcnt(16)\n"
    "v_cmp_ne_u32 vcc, v29, v13\n"
    "s_cmp_lg_u64 vcc, 0\n"
    "s_cbranch_scc1 L_slow9_%=\n"
    "L_ret9_%=:\n"
    "buffer_load_dwordx4 v[208:211], v25, s[60:63], s90 offen sc1\n"
    "buffer_load_dwordx4 v[212:215], v25, s[60:63], s90 offen offset:1024 sc1\n"
    "buffer_load_dwordx4 v[216:219], v25, s[60:63], s90 offen offset:2048 sc1\n"
    "buffer_load_dwordx4 v[220:223], v25, s[60:63], s90 offen offset:3072 sc1\n"
    "buffer_load_dwordx4 v[224:227], v25, s[60:63], s14 offen sc1\n"
    "buffer_load_dwordx4 v[228:231], v25, s[60:63], s14 offen offset:1024 sc1\n"
    "s_waitcnt vmcnt(21)\n"
    "v_dot2_f32_bf16 v250, v144, v32, 0\n"
    "v_dot2_f32_bf16 v251, v144, v33, 0\n"
    "s_waitcnt vmcnt(13)\n"
    "v_dot2_f32_bf16 v252, v152, v32, 0\n"
    "v_dot2_f32_bf16 v253, v152, v33, 0\n"
    "v_dot2_f32_bf16 v250, v145, v32, v250\n"
    "v_dot2_f32_bf16 v251, v145, v33, v251\n"
    "s_waitcnt vmcnt(12)\n"
    "v_dot2_f32_bf16 v252, v153, v32, v252\n"
    "v_dot2_f32_bf16 v253, v153, v33, v253\n"
    "v_dot2_f32_bf16 v250, v146, v32, v250\n"
    "v_dot2_f32_bf16 v251, v146, v33, v251\n"
    "s_waitcnt vmcnt(11)\n"
    "v_dot2_f32_bf16 v252, v154, v32, v252\n"
    "v_dot2_f32_bf16 v253, v154, v33, v253\n"
    "v_dot2_f32_bf16 v250, v147, v32, v250\n"
    "v_dot2_f32_bf16 v251, v147, v33, v251\n"
    "s_waitcnt vmcnt(10)\n"
    "v_dot2_f32_bf16 v252, v155, v32, v252\n"
    "v_dot2_f32_bf16 v253, v155, v33, v253\n"
    "v_dot2_f32_bf16 v250, v148, v32, v250\n"
    "v_dot2_f32_bf16 v251, v148, v33, v251\n"
    "s_waitcnt vmcnt(9)\n"
    "v_dot2_f32_bf16 v252, v156, v32, v252\n"
    "v_dot2_f32_bf16 v253, v156, v33, v253\n"
    "v_dot2_f32_bf16 v250, v149, v32, v250\n"
    "v_dot2_f32_bf16 v251, v149, v33, v251\n"
    "s_waitcnt vmcnt(8)\n"
    "v_dot2_f32_bf16 v252, v157, v32, v252\n"
    "v_dot2_f32_bf16 v253, v157, v33, v253\n"
    "v_dot2_f32_bf16 v250, v150, v32, v250\n"
    "v_dot2_f32_bf16 v251, v150, v33, v251\n"
    "s_waitcnt vmcnt(7)\n"
    "v_dot2_f32_bf16 v252, v158, v32, v252\n"
    "v_dot2_f32_bf16 v253, v158, v33, v253\n"
    "v_dot2_f32_bf16 v250, v151, v32, v250\n"
    "v_dot2_f32_bf16 v251, v151, v33, v251\n"
    "s_waitcnt vmcnt(6)\n"
    "v_dot2_f32_bf16 v252, v159, v32, v252\n"
    "v_dot2_f32_bf16 v253, v159, v33, v253\n"
    "v_exp_f32 v36, v250\n"
    "v_exp_f32 v37, v251\n"
    "s_nop 0\n"
    "v_pk_mul_f32 v[252:253], v[252:253], v[34:35]\n"
    "v_pk_mul_f32 v[252:253], v[252:253], v[250:251]\n"
    "v_add_f32 v36, 1.0, v36\n"
    "v_add_f32 v37, 1.0, v37\n"
    "v_rcp_f32 v36, v36\n"
    "v_rcp_f32 v37, v37\n"
    "s_nop 0\n"
    "v_pk_fma_f32 v[248:249], v[252:253], v[36:37], v[248:249]\n"
    "v_cvt_pk_bf16_f32 v168, v248, v249\n"
    "buffer_store_dword v168, v41, s[64:67], s5 offen\n"
    "s_waitcnt vmcnt(6)\n"
    "v_dot2_f32_bf16 v192, v208, v32, v192\n"
    "v_dot2_f32_bf16 v193, v208, v33, v193\n"
    "v_dot2_f32_bf16 v194, v209, v32, v194\n"
    "v_dot2_f32_bf16 v195, v209, v33, v195\n"
    "v_dot2_f32_bf16 v196, v210, v32, v196\n"
    "v_dot2_f32_bf16 v197, v210, v33, v197\n"
    "v_dot2_f32_bf16 v198, v211, v32, v198\n"
    "v_dot2_f32_bf16 v199, v211, v33, v199\n"
    "s_waitcnt vmcnt(5)\n"
    "v_dot2_f32_bf16 v200, v212, v32, v200\n"
    "v_dot2_f32_bf16 v201, v212, v33, v201\n"
    "v_dot2_f32_bf16 v202, v213, v32, v202\n"
    "v_dot2_f32_bf16 v203, v213, v33, v203\n"
    "v_dot2_f32_bf16 v204, v214, v32, v204\n"
    "v_dot2_f32_bf16 v205, v214, v33, v205\n"
    "v_dot2_f32_bf16 v206, v215, v32, v206\n"
    "v_dot2_f32_bf16 v207, v215, v33, v207\n"
    "s_waitcnt vmcnt(4)\n"
    "v_dot2_f32_bf16 v192, v216, v32, v192\n"
    "v_dot2_f32_bf16 v193, v216, v33, v193\n"
    "v_dot2_f32_bf16 v194, v217, v32, v194\n"
    "v_dot2_f32_bf16 v195, v217, v33, v195\n"
    "v_dot2_f32_bf16 v196, v218, v32, v196\n"
    "v_dot2_f32_bf16 v197, v218, v33, v197\n"
    "v_dot2_f32_bf16 v198, v219, v32, v198\n"
    "v_dot2_f32_bf16 v199, v219, v33, v199\n"
    "s_waitcnt vmcnt(3)\n"
    "v_dot2_f32_bf16 v200, v220, v32, v200\n"
    "v_dot2_f32_bf16 v201, v220, v33, v201\n"
    "v_dot2_f32_bf16 v202, v221, v32, v202\n"
    "v_dot2_f32_bf16 v203, v221, v33, v203\n"
    "v_dot2_f32_bf16 v204, v222, v32, v204\n"
    "v_dot2_f32_bf16 v205, v222, v33, v205\n"
    "v_dot2_f32_bf16 v206, v223, v32, v206\n"
    "v_dot2_f32_bf16 v207, v223, v33, v207\n"
    "s_waitcnt vmcnt(2)\n"
    "v_dot2_f32_bf16 v192, v224, v32, v192\n"
    "v_dot2_f32_bf16 v193, v224, v33, v193\n"
    "v_dot2_f32_bf16 v194, v225, v32, v194\n"
    "v_dot2_f32_bf16 v195, v225, v33, v195\n"
    "v_dot2_f32_bf16 v196, v226, v32, v196\n"
    "v_dot2_f32_bf16 v197, v226, v33, v197\n"
    "v_dot2_f32_bf16 v198, v227, v32, v198\n"
    "v_dot2_f32_bf16 v199, v227, v33, v199\n"
    "s_waitcnt vmcnt(1)\n"
    "v_dot2_f32_bf16 v200, v228, v32, v200\n"
    "v_dot2_f32_bf16 v201, v228, v33, v201\n"
    "v_dot2_f32_bf16 v202, v229, v32, v202\n"
    "v_dot2_f32_bf16 v203, v229, v33, v203\n"
    "v_dot2_f32_bf16 v204, v230, v32, v204\n"
    "v_dot2_f32_bf16 v205, v230, v33, v205\n"
    "v_dot2_f32_bf16 v206, v231, v32, v206\n"
    "v_dot2_f32_bf16 v207, v231, v33, v207\n"
    "v_exp_f32 v48, v192\n"
    "v_exp_f32 v49, v193\n"
    "v_exp_f32 v50, v194\n"
    "v_exp_f32 v51, v195\n"
    "v_exp_f32 v52, v196\n"
    "v_exp_f32 v53, v197\n"
    "v_exp_f32 v54, v198\n"
    "v_exp_f32 v55, v199\n"
    "v_exp_f32 v56, v200\n"
    "v_exp_f32 v57, v201\n"
    "v_exp_f32 v58, v202\n"
    "v_exp_f32 v59, v203\n"
    "v_exp_f32 v60, v204\n"
    "v_exp_f32 v61, v205\n"
    "v_exp_f32 v62, v206\n"
    "v_exp_f32 v63, v207\n"
    "v_pk_mul_f32 v[176:177], v[176:177], v[192:193]\n"
    "v_pk_mul_f32 v[178:179], v[178:179], v[194:195]\n"
    "v_pk_mul_f32 v[180:181], v[180:181], v[196:197]\n"
    "v_pk_mul_f32 v[182:183], v[182:183], v[198:199]\n"
    "v_pk_mul_f32 v[184:185], v[184:185], v[200:201]\n"
    "v_pk_mul_f32 v[186:187], v[186:187], v[202:203]\n"
    "v_pk_mul_f32 v[188:189], v[188:189], v[204:205]\n"
    "v_pk_mul_f32 v[190:191], v[190:191], v[206:207]\n"
    "v_add_f32 v48, 1.0, v48\n"
    "v_add_f32 v49, 1.0, v49\n"
    "v_add_f32 v50, 1.0, v50\n"
    "v_add_f32 v51, 1.0, v51\n"
    "v_add_f32 v52, 1.0, v52\n"
    "v_add_f32 v53, 1.0, v53\n"
    "v_add_f32 v54, 1.0, v54\n"
    "v_add_f32 v55, 1.0, v55\n"
    "v_add_f32 v56, 1.0, v56\n"
    "v_add_f32 v57, 1.0, v57\n"
    "v_add_f32 v58, 1.0, v58\n"
    "v_add_f32 v59, 1.0, v59\n"
    "v_add_f32 v60, 1.0, v60\n"
    "v_add_f32 v61, 1.0, v61\n"
    "v_add_f32 v62, 1.0, v62\n"
    "v_add_f32 v63, 1.0, v63\n"
    "v_rcp_f32 v48, v48\n"
    "v_rcp_f32 v49, v49\n"
    "v_rcp_f32 v50, v50\n"
    "v_rcp_f32 v51, v51\n"
    "v_rcp_f32 v52, v52\n"
    "v_rcp_f32 v53, v53\n"
    "v_rcp_f32 v54, v54\n"
    "v_rcp_f32 v55, v55\n"
    "v_rcp_f32 v56, v56\n"
    "v_rcp_f32 v57, v57\n"
    "v_rcp_f32 v58, v58\n"
    "v_rcp_f32 v59, v59\n"
    "v_rcp_f32 v60, v60\n"
    "v_rcp_f32 v61, v61\n"
    "v_rcp_f32 v62, v62\n"
    "v_rcp_f32 v63, v63\n"
    "v_pk_fma_f32 v[232:233], v[176:177], v[48:49], v[232:233]\n"
    "v_pk_fma_f32 v[234:235], v[178:179], v[50:51], v[234:235]\n"
    "v_pk_fma_f32 v[236:237], v[180:181], v[52:53], v[236:237]\n"
    "v_pk_fma_f32 v[238:239], v[182:183], v[54:55], v[238:239]\n"
    "v_pk_fma_f32 v[240:241], v[184:185], v[56:57], v[240:241]\n"
    "v_pk_fma_f32 v[242:243], v[186:187], v[58:59], v[242:243]\n"
    "v_pk_fma_f32 v[244:245], v[188:189], v[60:61], v[244:245]\n"
    "v_pk_fma_f32 v[246:247], v[190:191], v[62:63], v[246:247]\n"
    "v_cvt_pk_bf16_f32 v88, v232, v233\n"
    "v_cvt_pk_bf16_f32 v89, v234, v235\n"
    "v_cvt_pk_bf16_f32 v90, v236, v237\n"
    "v_cvt_pk_bf16_f32 v91, v238, v239\n"
    "v_cvt_pk_bf16_f32 v92, v240, v241\n"
    "v_cvt_pk_bf16_f32 v93, v242, v243\n"
    "v_cvt_pk_bf16_f32 v94, v244, v245\n"
    "v_cvt_pk_bf16_f32 v95, v246, v247\n"
    "v_permlane32_swap_b32 v88, v90\n"
    "v_permlane32_swap_b32 v89, v91\n"
    "v_permlane32_swap_b32 v92, v94\n"
    "v_permlane32_swap_b32 v93, v95\n"
    "buffer_store_dwordx4 v[88:91], v40, s[64:67], s4 offen\n"
    "buffer_store_dwordx4 v[92:95], v40, s[64:67], s4 offen offset:32\n"
    "s_cmp_lg_u32 s37, 0\n"
    "s_cbranch_scc1 L_nob_%=\n"
    "buffer_store_dword v29, v26, s[60:63], 0 offen sc1\n"
    "L_nob_%=:\n"
    "s_waitcnt vmcnt(0)\n"
    "s_branch L_end_%=\n"
    "L_slow0_%=:\n"
    "s_mov_b32 s24, 200000\n"
    "L_spin0_%=:\n"
    "buffer_load_dword v42, v28, s[60:63], s9 offen sc1\n"
    "s_waitcnt vmcnt(0)\n"
    "v_cmp_ne_u32 vcc, v29, v42\n"
    "s_cmp_eq_u64 vcc, 0\n"
    "s_cbranch_scc1 L_ret0_%=\n"
    "s_sleep 1\n"
    "s_sub_u32 s24, s24, 1\n"
    "s_cmp_lg_u32 s24, 0\n"
    "s_cbranch_scc1 L_spin0_%=\n"
    "s_branch L_ret0_%=\n"
    "L_slow1_%=:\n"
    "s_mov_b32 s24, 200000\n"
    "L_spin1_%=:\n"
    "buffer_load_dword v42, v28, s[60:63], s17 offen sc1\n"
    "s_waitcnt vmcnt(0)\n"
    "v_cmp_ne_u32 vcc, v29, v42\n"
    "s_cmp_eq_u64 vcc, 0\n"
    "s_cbranch_scc1 L_ret1_%=\n"
    "s_sleep 1\n"
    "s_sub_u32 s24, s24, 1\n"
    "s_cmp_lg_u32 s24, 0\n"
    "s_cbranch_scc1 L_spin1_%=\n"
    "s_branch L_ret1_%=\n"
    "L_slow2_%=:\n"
    "s_mov_b32 s24, 200000\n"
    "L_spin2_%=:\n"
    "buffer_load_dword v13, v27, s[60:63], s7 offen offset:2048 sc1\n"
    "s_waitcnt vmcnt(0)\n"
    "v_cmp_ne_u32 vcc, v29, v13\n"
    "s_cmp_eq_u64 vcc, 0\n"
    "s_cbranch_scc1 L_ret2_%=\n"
    "s_sleep 1\n"
    "s_sub_u32 s24, s24, 1\n"
    "s_cmp_lg_u32 s24, 0\n"
    "s_cbranch_scc1 L_spin2_%=\n"
    "s_branch L_ret2_%=\n"
    "L_slow3_%=:\n"
    "s_mov_b32 s24, 200000\n"
    "L_spin3_%=:\n"
    "buffer_load_dword v20, v27, s[60:63], s7 offen sc1\n"
    "s_waitcnt vmcnt(0)\n"
    "v_cmp_ne_u32 vcc, v29, v20\n"
    "s_cmp_eq_u64 vcc, 0\n"
    "s_cbranch_scc1 L_ret3_%=\n"
    "s_sleep 1\n"
    "s_sub_u32 s24, s24, 1\n"
    "s_cmp_lg_u32 s24, 0\n"
    "s_cbranch_scc1 L_spin3_%=\n"
    "s_branch L_ret3_%=\n"
    "L_slow4_%=:\n"
    "s_mov_b32 s24, 200000\n"
    "L_spin4_%=:\n"
    "buffer_load_dword v13, v27, s[60:63], s7 offen offset:1024 sc1\n"
    "s_waitcnt vmcnt(0)\n"
    "v_cmp_ne_u32 vcc, v29, v13\n"
    "s_cmp_eq_u64 vcc, 0\n"
    "s_cbranch_scc1 L_ret4_%=\n"
    "s_sleep 1\n"
    "s_sub_u32 s24, s24, 1\n"
    "s_cmp_lg_u32 s24, 0\n"
    "s_cbranch_scc1 L_spin4_%=\n"
    "s_branch L_ret4_%=\n"
    "L_slow5_%=:\n"
    "s_mov_b32 s24, 200000\n"
    "L_spin5_%=:\n"
    "buffer_load_dword v20, v27, s[60:63], s7 offen offset:512 sc1\n"
    "s_waitcnt vmcnt(0)\n"
    "v_cmp_ne_u32 vcc, v29, v20\n"
    "s_cmp_eq_u64 vcc, 0\n"
    "s_cbranch_scc1 L_ret5_%=\n"
    "s_sleep 1\n"
    "s_sub_u32 s24, s24, 1\n"
    "s_cmp_lg_u32 s24, 0\n"
    "s_cbranch_scc1 L_spin5_%=\n"
    "s_branch L_ret5_%=\n"
    "L_slow6_%=:\n"
    "s_mov_b32 s24, 200000\n"
    "L_spin6_%=:\n"
    "buffer_load_dword v13, v27, s[60:63], s7 offen offset:1536 sc1\n"
    "s_waitcnt vmcnt(0)\n"
    "v_cmp_ne_u32 vcc, v29, v13\n"
    "s_cmp_eq_u64 vcc, 0\n"
    "s_cbranch_scc1 L_ret6_%=\n"
    "s_sleep 1\n"
    "s_sub_u32 s24, s24, 1\n"
    "s_cmp_lg_u32 s24, 0\n"
    "s_cbranch_scc1 L_spin6_%=\n"
    "s_branch L_ret6_%=\n"
    "L_slow7_%=:\n"
    "s_mov_b32 s24, 200000\n"
    "L_spin7_%=:\n"
    "buffer_load_dword v42, v28, s[60:63], s12 offen sc1\n"
    "s_waitcnt vmcnt(0)\n"
    "v_cmp_ne_u32 vcc, v29, v42\n"
    "s_cmp_eq_u64 vcc, 0\n"
    "s_cbranch_scc1 L_ret7_%=\n"
    "s_sleep 1\n"
    "s_sub_u32 s24, s24, 1\n"
    "s_cmp_lg_u32 s24, 0\n"
    "s_cbranch_scc1 L_spin7_%=\n"
    "s_branch L_ret7_%=\n"
    "L_slow8_%=:\n"
    "s_mov_b32 s24, 200000\n"
    "L_spin8_%=:\n"
    "buffer_load_dword v42, v43, s[60:63], s79 offen sc1\n"
    "s_waitcnt vmcnt(0)\n"
    "v_cmp_ne_u32 vcc, v29, v42\n"
    "s_cmp_eq_u64 vcc, 0\n"
    "s_cbranch_scc1 L_ret8_%=\n"
    "s_sleep 1\n"
    "s_sub_u32 s24, s24, 1\n"
    "s_cmp_lg_u32 s24, 0\n"
    "s_cbranch_scc1 L_spin8_%=\n"
    "s_branch L_ret8_%=\n"
    "L_slow9_%=:\n"
    "s_mov_b32 s24, 200000\n"
    "L_spin9_%=:\n"
    "buffer_load_dword v13, v31, s[60:63], s80 offen sc1\n"
    "s_waitcnt vmcnt(0)\n"
    "v_cmp_ne_u32 vcc, v29, v13\n"
    "s_cmp_eq_u64 vcc, 0\n"
    "s_cbranch_scc1 L_ret9_%=\n"
    "s_sleep 1\n"
    "s_sub_u32 s24, s24, 1\n"
    "s_cmp_lg_u32 s24, 0\n"
    "s_cbranch_scc1 L_spin9_%=\n"
    "s_branch L_ret9_%=\n"
    "L_end_%=:\n"
    ".Lfused_body_end:\n"
    :
    : "{s[0:1]}"(__builtin_amdgcn_kernarg_segment_ptr()), "{s2}"(blockIdx.x), "{v0}"(threadIdx.x), "i"((unsigned)(size_t)(__attribute__((address_space(3))) char*)smem)
    : "v1","v2","v3","v4","v5","v6","v7","v8","v9","v10","v11","v12","v13","v14","v15","v16","v17","v18","v19","v20","v21","v22","v23","v24","v25","v26","v27","v28","v29","v30","v31","v32","v33","v34","v35","v36","v37","v38","v39","v40","v41","v42","v43","v44","v45","v46","v47","v48","v49","v50","v51","v52","v53","v54","v55","v56","v57","v58","v59","v60","v61","v62","v63","v64","v65","v66","v67","v68","v69","v70","v71","v72","v73","v74","v75","v76","v77","v78","v79","v80","v81","v82","v83","v84","v85","v86","v87","v88","v89","v90","v91","v92","v93","v94","v95","v96","v97","v98","v99","v100","v101","v102","v103","v104","v105","v106","v107","v108","v109","v110","v111","v112","v113","v114","v115","v116","v117","v118","v119","v120","v121","v122","v123","v124","v125","v126","v127","v128","v129","v130","v131","v132","v133","v134","v135","v136","v137","v138","v139","v140","v141","v142","v143","v144","v145","v146","v147","v148","v149","v150","v151","v152","v153","v154","v155","v156","v157","v158","v159","v160","v161","v162","v163","v164","v165","v166","v167","v168","v169","v170","v171","v172","v173","v174","v175","v176","v177","v178","v179","v180","v181","v182","v183","v184","v185","v186","v187","v188","v189","v190","v191","v192","v193","v194","v195","v196","v197","v198","v199","v200","v201","v202","v203","v204","v205","v206","v207","v208","v209","v210","v211","v212","v213","v214","v215","v216","v217","v218","v219","v220","v221","v222","v223","v224","v225","v226","v227","v228","v229","v230","v231","v232","v233","v234","v235","v236","v237","v238","v239","v240","v241","v242","v243","v244","v245","v246","v247","v248","v249","v250","v251","v252","v253","v254","v255","a0","a1","a2","a3","a4","a5","a6","a7","a8","a9","a10","a11","a12","a13","a14","a15","a16","a17","a18","a19","a20","a21","a22","a23","a24","a25","a26","a27","a28","a29","a30","a31","a32","a33","a34","a35","a36","a37","a38","a39","a40","a41","a42","a43","a44","a45","a46","a47","a48","a49","a50","a51","a52","a53","a54","a55","a56","a57","a58","a59","a60","a61","a62","a63","a64","a65","a66","a67","a68","a69","a70","a71","a72","a73","a74","a75","a76","a77","a78","a79","a80","a81","a82","a83","a84","a85","a86","a87","a88","a89","a90","a91","a92","a93","a94","a95","a96","a97","a98","a99","a100","a101","a102","a103","a104","a105","a106","a107","a108","a109","a110","a111","a112","a113","a114","a115","a116","a117","a118","a119","a120","a121","a122","a123","a124","a125","a126","a127","a128","a129","a130","a131","a132","a133","a134","a135","a136","a137","a138","a139","a140","a141","a142","a143","a144","a145","a146","a147","a148","a149","a150","a151","a152","a153","a154","a155","a156","a157","a158","a159","a160","a161","a162","a163","a164","a165","a166","a167","a168","a169","a170","a171","a172","a173","a174","a175","a176","a177","a178","a179","a180","a181","a182","a183","a184","a185","a186","a187","a188","a189","a190","a191","a192","a193","a194","a195","a196","a197","a198","a199","a200","a201","a202","a203","a204","a205","a206","a207","a208","a209","a210","a211","a212","a213","a214","a215","a216","a217","a218","a219","a220","a221","a222","a223","a224","a225","a226","a227","a228","a229","a230","a231","a232","a233","a234","a235","a236","a237","a238","a239","a240","a241","a242","a243","a244","a245","a246","a247","a248","a249","a250","a251","a252","a253","a254","a255","s3","s4","s5","s6","s7","s8","s9","s10","s11","s12","s13","s14","s15","s16","s17","s18","s19","s20","s21","s22","s23","s24","s25","s26","s27","s28","s29","s30","s31","s34","s35","s36","s37","s38","s39","s40","s41","s42","s43","s44","s45","s46","s47","s48","s49","s50","s51","s52","s53","s54","s55","s56","s57","s58","s59","s60","s61","s62","s63","s64","s65","s66","s67","s68","s69","s70","s71","s72","s73","s74","s75","s76","s77","s78","s79","s80","s81","s82","s83","s84","s85","s86","s87","s88","s89","s90","s91","s92","s93","s94","s95","s96","s97","s98","s99","vcc","scc","memory"
  );
}

extern "C" void kernel_launch(void* const* d_in, const int* in_sizes, int n_in, void* d_out, int out_size, void* d_ws, size_t ws_size,
                              hipStream_t stream) {
  fused_kernel<<<256, 256, 0, stream>>>(d_in[0], d_in[1], d_in[2], d_in[3], d_in[5], d_in[6], d_out, d_ws);
}
```
